```python
import torch

def _softmax(x, axis=-1):
    return torch.softmax(x, dim=axis)
def _gelu(x):
    return torch.nn.functional.gelu(x, approximate="tanh")
def _silu(x):
    return torch.nn.functional.silu(x)
def _sigmoid(x):
    return torch.sigmoid(x)
def _layernorm(x, axis=-1, eps=1e-5):
    m = x.mean(dim=axis, keepdim=True)
    v = x.var(dim=axis, keepdim=True, unbiased=False)
    return (x - m) / torch.sqrt(v + eps)
def _scatter_add(tgt, idx, src, axis=0):
    return tgt.clone().index_add_(axis, idx, src)
def _segment_sum(src, seg, n):
    out = torch.zeros((n,) + src.shape[1:], dtype=src.dtype)
    return out.index_add_(0, seg, src)
def _pad(x, pw):
    flat = tuple(v for pair in reversed(pw) for v in pair)
    return torch.nn.functional.pad(x, flat)

kernel_name = 'parallel_sum__sliding_block__dense_activation_gelu__prompt'

def setup_inputs():

    _w = (lambda a: (lambda t: t.to(torch.bfloat16) if t.is_floating_point() else t)(torch.as_tensor(a)))
    def _A_setup():
        B, S, D = 1, 2048, 448
        return {
            "x": _w((lambda *s: torch.randn(*s, dtype=torch.bfloat16) * 0.05)(B, S, D)),
            "wqkv": _w((lambda *s: torch.randn(*s, dtype=torch.bfloat16) * 0.05)(D, 3 * D)),
            "wo": _w((lambda *s: torch.randn(*s, dtype=torch.bfloat16) * 0.05)(D, D)),
        }
    _A_in = _A_setup()

    def _B_setup():
        B, S, D, F = 1, 2048, 448, 1792
        return {
            "x": _w((lambda *s: torch.randn(*s, dtype=torch.bfloat16) * 0.05)(B, S, D)),
            "w1": _w((lambda *s: torch.randn(*s, dtype=torch.bfloat16) * 0.8)(D, F) * (D ** -0.5)),
            "b1": _w((lambda *s: torch.randn(*s, dtype=torch.bfloat16) * 0.8)(F) * 0.01),
            "w2": _w((lambda *s: torch.randn(*s, dtype=torch.bfloat16) * 0.8)(F, D) * (F ** -0.5)),
            "b2": _w((lambda *s: torch.randn(*s, dtype=torch.bfloat16) * 0.8)(D) * 0.01),
        }
    _B_in = _B_setup()

    return {
        "A_x": _A_in["x"],
        "A_wqkv": _A_in["wqkv"],
        "A_wo": _A_in["wo"],
        "B_x": _B_in["x"],
        "B_w1": _B_in["w1"],
        "B_b1": _B_in["b1"],
        "B_w2": _B_in["w2"],
        "B_b2": _B_in["b2"],
    }

def reference(A_x, A_wqkv, A_wo, B_x, B_w1, B_b1, B_w2, B_b2):
    def _A_ref(x, wqkv, wo):
        B, S, D = x.shape
        H = 16; Dh = D // H; W = 256
        qkv = (lambda x, *d: x.permute(*d))((x @ wqkv).reshape(B, S, 3, H, Dh), 2, 0, 3, 1, 4)
        q, k, v = qkv[0], qkv[1], qkv[2]
        scores = torch.einsum("bhqd,bhkd->bhqk", q, k) * (Dh ** -0.5)
        idx = torch.arange(S)
        mask = (idx[:, None] - idx[None, :] >= 0) & (idx[:, None] - idx[None, :] < W)
        scores = torch.where(mask[None, None], scores, -1e9)
        p = _softmax(scores, axis=-1)
        o = torch.einsum("bhqk,bhkd->bhqd", p, v)
        return (lambda x, *d: x.permute(*d))(o, 0, 2, 1, 3).reshape(B, S, D) @ wo

    def _B_ref(x, w1, b1, w2, b2):
        h = torch.einsum("bsd,df->bsf", x, w1) + b1
        h = _gelu(h)
        return torch.einsum("bsf,fd->bsd", h, w2) + b2

    x = A_x
    xn = _layernorm(x, axis=-1)
    return x + _A_ref(xn, A_wqkv, A_wo) + _B_ref(xn, B_w1, B_b1, B_w2, B_b2)
```

```cpp
#include <hip/hip_runtime.h>
#include <hip/hip_bf16.h>
typedef unsigned short u16;
__device__ __forceinline__ float bf2f(u16 b){ return __uint_as_float(((unsigned)b)<<16); }
__device__ __forceinline__ u16 f2bf(float f){ unsigned u=__float_as_uint(f); u += 0x7fffu + ((u>>16)&1u); return (u16)(u>>16); }
constexpr int S=2048, D=448, H=16, DH=28, W=256, F=1792;

__global__ void k_ln(const u16* x, u16* xn){
  int row=blockIdx.x; int t=threadIdx.x;
  float v[7]; float s=0;
  for(int i=0;i<7;i++){ v[i]=bf2f(x[row*D+t+64*i]); s+=v[i]; }
  for(int m=32;m>=1;m>>=1) s+=__shfl_xor(s,m);
  float mean=s/D; float q=0;
  for(int i=0;i<7;i++){ float d=v[i]-mean; q+=d*d; }
  for(int m=32;m>=1;m>>=1) q+=__shfl_xor(q,m);
  float r=rsqrtf(q/D+1e-5f);
  for(int i=0;i<7;i++) xn[row*D+t+64*i]=f2bf((v[i]-mean)*r);
}
__global__ void k_g1(const u16* xn,const u16* wqkv,const u16* w1,const u16* b1,u16* qkv,u16* h){
  int row=blockIdx.y; int c=blockIdx.x*64+threadIdx.x;
  float acc=0;
  if(c<3*D){ for(int k=0;k<D;k++) acc+=bf2f(xn[row*D+k])*bf2f(wqkv[k*3*D+c]); qkv[row*3*D+c]=f2bf(acc);}
  else { int f=c-3*D; for(int k=0;k<D;k++) acc+=bf2f(xn[row*D+k])*bf2f(w1[k*F+f]); acc+=bf2f(b1[f]);
    float u=0.7978845608028654f*(acc+0.044715f*acc*acc*acc); h[row*F+f]=f2bf(0.5f*acc*(1.f+tanhf(u))); }
}
__global__ void k_attn(const u16* qkv,u16* o){
  int i=blockIdx.x*64+threadIdx.x; int hd=blockIdx.y;
  float q[DH]; for(int d=0;d<DH;d++) q[d]=bf2f(qkv[i*3*D+hd*DH+d]);
  float m=-1e30f,l=0; float acc[DH]; for(int d=0;d<DH;d++) acc[d]=0;
  int j0=i-W+1; if(j0<0) j0=0;
  for(int j=j0;j<=i;j++){ float s=0; for(int d=0;d<DH;d++) s+=q[d]*bf2f(qkv[j*3*D+D+hd*DH+d]); s*=0.1889822365046136f;
    float mn=fmaxf(m,s); float a=__expf(m-mn); float p=__expf(s-mn); l=l*a+p; for(int d=0;d<DH;d++) acc[d]=acc[d]*a+p*bf2f(qkv[j*3*D+2*D+hd*DH+d]); m=mn; }
  for(int d=0;d<DH;d++) o[i*D+hd*DH+d]=f2bf(acc[d]/l);
}
__global__ void k_g2(const u16* x,const u16* o,const u16* h,const u16* wo,const u16* w2,const u16* b2,u16* out){
  int row=blockIdx.y; int c=blockIdx.x*64+threadIdx.x;
  float a=0; for(int k=0;k<D;k++) a+=bf2f(o[row*D+k])*bf2f(wo[k*D+c]);
  float b=0; for(int k=0;k<F;k++) b+=bf2f(h[row*F+k])*bf2f(w2[k*D+c]);
  out[row*D+c]=f2bf(bf2f(x[row*D+c])+a+b+bf2f(b2[c]));
}
extern "C" void kernel_launch(void* const* d_in, const int* in_sizes, int n_in, void* d_out, int out_size, void* d_ws, size_t ws_size, hipStream_t stream){
  const u16* x=(const u16*)d_in[0]; const u16* wqkv=(const u16*)d_in[1]; const u16* wo=(const u16*)d_in[2];
  const u16* w1=(const u16*)d_in[4]; const u16* b1=(const u16*)d_in[5]; const u16* w2=(const u16*)d_in[6]; const u16* b2=(const u16*)d_in[7];
  u16* ws=(u16*)d_ws; u16* xn=ws; u16* qkv=xn+S*D; u16* h=qkv+S*3*D; u16* o=h+S*F;
  k_ln<<<S,64,0,stream>>>(x,xn);
  k_g1<<<dim3(49,S),64,0,stream>>>(xn,wqkv,w1,b1,qkv,h);
  k_attn<<<dim3(S/64,H),64,0,stream>>>(qkv,o);
  k_g2<<<dim3(7,S),64,0,stream>>>(x,o,h,wo,w2,b2,(u16*)d_out);
}
```

```cpp
#include <hip/hip_runtime.h>

typedef unsigned short u16;
typedef unsigned int u32;
typedef unsigned long long u64;
typedef u32 v4u __attribute__((ext_vector_type(4)));
typedef u32 v2u __attribute__((ext_vector_type(2)));
typedef int v4i __attribute__((ext_vector_type(4)));
typedef short s16x4 __attribute__((ext_vector_type(4)));
typedef __bf16 bf16x8 __attribute__((ext_vector_type(8)));
typedef __bf16 bf16x2 __attribute__((ext_vector_type(2)));
typedef float f32x2 __attribute__((ext_vector_type(2)));
typedef float f32x4 __attribute__((ext_vector_type(4)));
typedef float f32x16 __attribute__((ext_vector_type(16)));
#define GAS __attribute__((address_space(1)))
#define LAS __attribute__((address_space(3)))
#define DI __device__ __forceinline__

#define EXP_NOSTREAM 0
#define EXP_XST 0
#define STAMPS 0

constexpr int S_ = 2048, D_ = 448, F_ = 1792;
constexpr int WS_E = 0;
constexpr int WS_FQ = 4096;
constexpr int WS_FH = WS_FQ + 64 * 48 * 8;
constexpr int WS_FO = WS_FH + 64 * 64 * 8;
constexpr int WS_ONE = 1 << 19;
constexpr int WS_QK = 1 << 20;
constexpr int QK_TB = 112 * 512;
constexpr int WS_VT = WS_QK + 64 * QK_TB;
constexpr int VT_TB = 4 * 448 * 16;
constexpr int WS_HH = WS_VT + 64 * VT_TB;
constexpr int HH_TB = 280 * 512;
constexpr int WS_END = WS_HH + 64 * HH_TB;
constexpr int WS_DBG = 32 << 20;
constexpr int LDS_BYTES = 163840;
constexpr u32 SPIN_CAP = 1u << 16;

DI v4i make_srd(const void* p, u32 bytes) {
  const u64 a = (u64)p;
  v4i r;
  r[0] = (int)(u32)a; r[1] = (int)((u32)(a >> 32) & 0xffffu); r[2] = (int)bytes; r[3] = 0x00020000;
  return r;
}
template <int OFF> DI void ld16(v4u& d, u32 voff, v4i srd, u32 soff) {
  asm volatile("buffer_load_dwordx4 %0, %1, %2, %3 offen offset:%4" : "=v"(d) : "v"(voff), "s"(srd), "s"(soff), "i"(OFF) : "memory");
}
template <int OFF> DI void ld16c(v4u& d, u32 voff, v4i srd, u32 soff) {
  asm volatile("buffer_load_dwordx4 %0, %1, %2, %3 offen offset:%4 sc1" : "=v"(d) : "v"(voff), "s"(srd), "s"(soff), "i"(OFF) : "memory");
}
DI void ld8c(v2u& d, u32 voff, v4i srd) {
  asm volatile("buffer_load_dwordx2 %0, %1, %2, 0 offen sc1" : "=v"(d) : "v"(voff), "s"(srd) : "memory");
}
DI void ld2(u32& d, u32 voff, v4i srd, u32 soff) {
  asm volatile("buffer_load_ushort %0, %1, %2, %3 offen" : "=v"(d) : "v"(voff), "s"(srd), "s"(soff) : "memory");
}
DI void glds16(u32 lds, u32 voff, v4i srd, u32 soff) {
  asm volatile("s_mov_b32 m0, %0\n\ts_nop 0\n\tbuffer_load_dwordx4 %1, %2, %3 offen lds" ::"s"(lds), "v"(voff), "s"(srd), "s"(soff) : "memory");
}
DI void load_cells(u32 lds, u32 voff, v4i srd, u32 soff, u32 stride, int n) {
#pragma unroll 1
  for (int c = 0; c < n; ++c) {
    u32 t;
    asm volatile("s_mov_b32 m0, %1\n\ts_add_u32 %0, %4, %5\n\tbuffer_load_dwordx4 %2, %3, %4 offen lds\n\t"
                 "s_add_u32 m0, %1, 0x400\n\ts_nop 0\n\tbuffer_load_dwordx4 %2, %3, %0 offen lds"
                 : "=&s"(t) : "s"(lds), "v"(voff), "s"(srd), "s"(soff), "s"(stride * 8) : "memory", "scc");
    lds += 8192; soff += stride * 64;
  }
}
DI void load_pieces(u32 lds, u32 voff, v4i srd, u32 soff, u32 lstep, u32 sstep, int n) {
#pragma unroll 1
  for (int c = 0; c < n; ++c) { glds16(lds, voff, srd, soff); lds += lstep; soff += sstep; }
}
template <int OFF> DI void st16c(v4u d, u32 voff, v4i srd, u32 soff) {
  asm volatile("buffer_store_dwordx4 %0, %1, %2, %3 offen offset:%4 sc1\n\ts_nop 1" ::"v"(d), "v"(voff), "s"(srd), "s"(soff), "i"(OFF) : "memory");
}
template <int OFF> DI void st8c(v2u d, u32 voff, v4i srd, u32 soff) {
  asm volatile("buffer_store_dwordx2 %0, %1, %2, %3 offen offset:%4 sc1" ::"v"(d), "v"(voff), "s"(srd), "s"(soff), "i"(OFF) : "memory");
}
DI void st16(v4u d, u32 voff, v4i srd) {
  asm volatile("buffer_store_dwordx4 %0, %1, %2, 0 offen\n\ts_nop 1" ::"v"(d), "v"(voff), "s"(srd) : "memory");
}
template <int N> DI void vm_wait() { asm volatile("s_waitcnt vmcnt(%0)" ::"i"(N) : "memory"); }
DI void pin(v4u& a) { asm volatile("" : "+v"(a)); }
DI void pin(v2u& a) { asm volatile("" : "+v"(a)); }
DI void pin(u32& a) { asm volatile("" : "+v"(a)); }
DI void keep_s(u32& a) { a = __builtin_amdgcn_readfirstlane(a); asm volatile("" : "+s"(a)); }
DI void wg_barrier() { asm volatile("s_waitcnt lgkmcnt(0)\n\ts_barrier" ::: "memory"); }
DI f32x16 mfma(v4u a, v4u b, f32x16 c) {
  return __builtin_amdgcn_mfma_f32_32x32x16_bf16(__builtin_bit_cast(bf16x8, a), __builtin_bit_cast(bf16x8, b), c, 0, 0, 0);
}
DI u32 pk(float a, float b) { f32x2 f = {a, b}; return __builtin_bit_cast(u32, __builtin_convertvector(f, bf16x2)); }
DI v4u tr_frag(LAS char* p) {
  const s16x4 a = __builtin_amdgcn_ds_read_tr16_b64_v4i16((LAS s16x4*)p);
  const s16x4 b = __builtin_amdgcn_ds_read_tr16_b64_v4i16((LAS s16x4*)(p + 512));
  const v2u x = __builtin_bit_cast(v2u, a), y = __builtin_bit_cast(v2u, b);
  return v4u{x[0], x[1], y[0], y[1]};
}
template <int C> DI float dpp_add(float x) {
  return x + __builtin_bit_cast(float, __builtin_amdgcn_update_dpp(0, __builtin_bit_cast(int, x), C, 0xf, 0xf, true));
}
DI float red8(float x) { x = dpp_add<0xB1>(x); x = dpp_add<0x4E>(x); return dpp_add<0x141>(x); }
DI float swap32(float x) {
  const u32 u = __builtin_bit_cast(u32, x);
  const auto r = __builtin_amdgcn_permlane32_swap(u, u, false, false);
  return __builtin_bit_cast(float, (threadIdx.x & 32) ? r[0] : r[1]);
}
DI void pack_tile(const f32x16& a, v4u& o0, v4u& o1) {
  u32 p[8];
#pragma unroll
  for (int i = 0; i < 8; ++i) p[i] = pk(a[2 * i], a[2 * i + 1]);
  const auto r0 = __builtin_amdgcn_permlane32_swap(p[0], p[2], false, false);
  const auto r1 = __builtin_amdgcn_permlane32_swap(p[1], p[3], false, false);
  const auto r2 = __builtin_amdgcn_permlane32_swap(p[4], p[6], false, false);
  const auto r3 = __builtin_amdgcn_permlane32_swap(p[5], p[7], false, false);
  o0 = v4u{r0[0], r1[0], r0[1], r1[1]};
  o1 = v4u{r2[0], r3[0], r2[1], r3[1]};
}
DI void wait_flags(u32 voff, bool active, v2u tag, v4i ws) {
#pragma unroll 1
  for (u32 spins = 0; spins < SPIN_CAP; ++spins) {
    v2u f;
    ld8c(f, voff, ws);
    vm_wait<0>();
    pin(f);
    const bool ok = !active || (f[0] == tag[0] && f[1] == tag[1]);
    if (__all(ok)) break;
    __builtin_amdgcn_s_sleep(2);
  }
}
DI void post_flag(u32 off, v2u tag, v4i ws) {
  asm volatile("s_mov_b64 exec, 1\n\tbuffer_store_dwordx2 %0, off, %1, %2 sc1\n\ts_mov_b64 exec, -1" ::"v"(tag), "s"(ws), "s"(off) : "memory");
}
#define STAMP(i) do {} while (0)
#define STAMP_DECL
#define XSTAMP(i) do {} while (0)
#define CSTAMP(i) do {} while (0)
#define STAMP_OUT() do {} while (0)

struct Stream { v4i srd; u32 voff, soff, s8, lds, n; };
template <int P, bool COND> DI void stream_piece(const Stream& g) {
  u32 t;
  if constexpr (COND)
    asm volatile("s_cmp_gt_u32 %6, %9\n\ts_cbranch_scc0 1f\n\ts_mul_i32 %0, %5, %8\n\ts_add_u32 m0, %1, %7\n\ts_add_u32 %0, %0, %2\n\t"
                 "buffer_load_dwordx4 %3, %4, %0 offen lds\n1:"
                 : "=&s"(t) : "s"(g.lds), "s"(g.soff), "v"(g.voff), "s"(g.srd), "s"(g.s8), "s"(g.n),
                   "i"((P >> 2) * 16384 + (P & 3) * 1024), "i"((P >> 2) * 16 + (P & 3)), "i"(P) : "memory", "scc");
  else
    asm volatile("s_mul_i32 %0, %5, %7\n\ts_add_u32 m0, %1, %6\n\ts_add_u32 %0, %0, %2\n\tbuffer_load_dwordx4 %3, %4, %0 offen lds"
                 : "=&s"(t) : "s"(g.lds), "s"(g.soff), "v"(g.voff), "s"(g.srd), "s"(g.s8),
                   "i"((P >> 2) * 16384 + (P & 3) * 1024), "i"((P >> 2) * 16 + (P & 3)) : "memory", "scc");
}
template <int LO, int HI> DI void stream_run(const Stream& g) {
  if constexpr (LO < HI) { stream_piece<LO, false>(g); stream_run<LO + 1, HI>(g); }
}
DI u32 mad24(u32 a, u32 s, u32 c) { u32 d; asm("v_mad_u32_u24 %0, %1, %2, %3" : "=v"(d) : "v"(a), "s"(s), "v"(c)); return d; }
DI bool flags_ok(v2u f, bool active, v2u tag) { return __all(!active || (f[0] == tag[0] && f[1] == tag[1])); }

template <int SLOT, bool SWAP, bool BIAS>
DI f32x16 gemm1_block(const v4u (&xa)[28], LAS char* p1, LAS char* p2, LAS char* p3, u32 bias, const Stream& g, bool grpb, bool post, bool heavy,
                      u32 pend, v2u tag, v4i ws, u64* xst = nullptr) {
  constexpr int PD = 5;
  constexpr int NP = SLOT == 0 ? 12 : (SLOT == 1 ? 4 : (SLOT == 2 ? 8 : 0)), NU = SLOT == 0 ? 0 : 4;
  auto rd = [&](int s) { return s < 4 ? tr_frag(p1 + s * 2048) : (s < 24 ? tr_frag(p2 + (s - 4) * 2048) : tr_frag(p3 + (s - 24) * 2048)); };
  __builtin_amdgcn_s_setprio(1);
  v4u wf[PD];
#pragma unroll
  for (int s = 0; s < PD; ++s) wf[s] = rd(s);
  f32x16 acc;
#pragma unroll
  for (int i = 0; i < 16; ++i) acc[i] = 0.f;
  if constexpr (BIAS) {
    const v4u bf = {bias, 0u, 0u, 0u};
    const v4u one = {(threadIdx.x & 32) ? 0u : 0x3f80u, 0u, 0u, 0u};
    acc = mfma(bf, one, acc);
  }
  __builtin_amdgcn_sched_barrier(0);
#define G1STEP(s) { \
    const v4u cur = wf[(s) % PD]; \
    if ((s) == 19) { \
      if (SLOT == 0) { if (!grpb) { vm_wait<0>(); asm volatile("s_barrier" ::: "memory"); } } \
      else if (SLOT == 1) { vm_wait<4>(); post_flag(pend, tag, ws); } \
      else if (SLOT == 2) { if (post) { vm_wait<4>(); post_flag(pend, tag, ws); } } \
    } \
    if ((s) == 24 && SLOT < 3) { \
      if (grpb) { \
        if (SLOT == 0) vm_wait<12>(); \
        if (SLOT == 2) { if (heavy) vm_wait<0>(); } \
        asm volatile("s_barrier" ::: "memory"); \
        __builtin_amdgcn_s_setprio(2); \
      } \
    } \
    acc = SWAP ? mfma(xa[s], cur, acc) : mfma(cur, xa[s], acc); \
    if ((s) + PD < 28) wf[(s) % PD] = rd((s) + PD); \
    if (((s) & 1) == 0 && (s) / 2 < NP && !(EXP_NOSTREAM && SLOT > 0)) stream_piece<(s) / 2, ((s) / 2 >= NU)>(g); \
    if (SLOT == 2 && EXP_XST && ((s) == 0 || (s) == 6 || (s) == 12 || (s) == 18 || (s) == 23)) XSTAMP(53 + ((s) + 1) / 6); \
    __builtin_amdgcn_sched_barrier(0); }
  G1STEP(0) G1STEP(1) G1STEP(2) G1STEP(3) G1STEP(4) G1STEP(5) G1STEP(6) G1STEP(7) G1STEP(8) G1STEP(9)
  G1STEP(10) G1STEP(11) G1STEP(12) G1STEP(13) G1STEP(14) G1STEP(15) G1STEP(16) G1STEP(17) G1STEP(18) G1STEP(19)
  G1STEP(20) G1STEP(21) G1STEP(22) G1STEP(23) G1STEP(24) G1STEP(25) G1STEP(26) G1STEP(27)
#undef G1STEP
  __builtin_amdgcn_s_setprio(0);
  return acc;
}

DI void gelu4(u32& o0, u32& o1, float a, float b, float c, float d, float c1) {
  float t0, t1, t2, t3;
  asm volatile(
      "v_mul_f32 %2, %6, %6\n\tv_mul_f32 %3, %7, %7\n\tv_mul_f32 %4, %8, %8\n\tv_mul_f32 %5, %9, %9\n\t"
      "v_fmaak_f32 %2, %2, %10, 0xc0135761\n\tv_fmaak_f32 %3, %3, %10, 0xc0135761\n\t"
      "v_fmaak_f32 %4, %4, %10, 0xc0135761\n\tv_fmaak_f32 %5, %5, %10, 0xc0135761\n\t"
      "v_mul_f32 %2, %2, %6\n\tv_mul_f32 %3, %3, %7\n\tv_mul_f32 %4, %4, %8\n\tv_mul_f32 %5, %5, %9\n\t"
      "v_exp_f32 %2, %2\n\tv_exp_f32 %3, %3\n\tv_exp_f32 %4, %4\n\tv_exp_f32 %5, %5\n\t"
      "v_add_f32 %2, 1.0, %2\n\tv_add_f32 %3, 1.0, %3\n\tv_add_f32 %4, 1.0, %4\n\tv_add_f32 %5, 1.0, %5\n\t"
      "v_rcp_f32 %2, %2\n\tv_rcp_f32 %3, %3\n\tv_rcp_f32 %4, %4\n\tv_rcp_f32 %5, %5\n\t"
      "v_mul_f32 %2, %2, %6\n\tv_mul_f32 %3, %3, %7\n\tv_mul_f32 %4, %4, %8\n\tv_mul_f32 %5, %5, %9\n\t"
      "v_cvt_pk_bf16_f32 %0, %2, %3\n\tv_cvt_pk_bf16_f32 %1, %4, %5\n\ts_nop 1"
      : "=&v"(o0), "=&v"(o1), "=&v"(t0), "=&v"(t1), "=&v"(t2), "=&v"(t3) : "v"(a), "v"(b), "v"(c), "v"(d), "v"(c1));
}
DI void swap_tile(const u32 (&p)[8], v4u& o0, v4u& o1) {
  const auto r0 = __builtin_amdgcn_permlane32_swap(p[0], p[2], false, false);
  const auto r1 = __builtin_amdgcn_permlane32_swap(p[1], p[3], false, false);
  const auto r2 = __builtin_amdgcn_permlane32_swap(p[4], p[6], false, false);
  const auto r3 = __builtin_amdgcn_permlane32_swap(p[5], p[7], false, false);
  o0 = v4u{r0[0], r1[0], r0[1], r1[1]};
  o1 = v4u{r2[0], r3[0], r2[1], r3[1]};
}
DI void mfma_settle(const f32x16& a) { asm volatile("s_nop 7\n\ts_nop 4" ::"v"(a)); }
DI void store_v(const f32x16& a, u32 vo, v4i ws, u32 so) {
  st16c<0>(v4u{pk(a[0], a[1]), pk(a[2], a[3]), pk(a[4], a[5]), pk(a[6], a[7])}, vo, ws, so);
  st16c<0>(v4u{pk(a[8], a[9]), pk(a[10], a[11]), pk(a[12], a[13]), pk(a[14], a[15])}, vo, ws, so + 14336u);
}
DI void store_gelu(const f32x16& a, float gc1, u32 lo16, v4i ws, u32 so) {
  u32 p[8];
  mfma_settle(a);
#pragma unroll
  for (int i = 0; i < 4; ++i) gelu4(p[2 * i], p[2 * i + 1], a[4 * i], a[4 * i + 1], a[4 * i + 2], a[4 * i + 3], gc1);
  v4u o0, o1;
  swap_tile(p, o0, o1);
  st16c<0>(o0, lo16, ws, so);
  st16c<1024>(o1, lo16, ws, so);
}
DI float max16(const f32x16& s) {
  float a, b;
  asm volatile("v_max3_f32 %0, %2, %3, %4\n\tv_max3_f32 %1, %5, %6, %7\n\tv_max3_f32 %0, %0, %8, %9\n\tv_max3_f32 %1, %1, %10, %11\n\t"
               "v_max3_f32 %0, %0, %12, %13\n\tv_max3_f32 %1, %1, %14, %15\n\tv_max3_f32 %0, %0, %16, %17\n\tv_max_f32 %0, %0, %1"
               : "=&v"(a), "=&v"(b) : "v"(s[0]), "v"(s[1]), "v"(s[2]), "v"(s[3]), "v"(s[4]), "v"(s[5]), "v"(s[6]), "v"(s[7]), "v"(s[8]),
                 "v"(s[9]), "v"(s[10]), "v"(s[11]), "v"(s[12]), "v"(s[13]), "v"(s[14]), "v"(s[15]));
  return a;
}
DI v4u exp8(const f32x16& s, int o, float cs, float nb) {
  v4u p;
#pragma unroll
  for (int j = 0; j < 4; ++j)
    p[j] = pk(__builtin_amdgcn_exp2f(__builtin_fmaf(s[o + 2 * j], cs, nb)), __builtin_amdgcn_exp2f(__builtin_fmaf(s[o + 2 * j + 1], cs, nb)));
  return p;
}

__global__ void __launch_bounds__(512, 2)
fused_block(const u16* __restrict__ x, const u16* __restrict__ wqkv, const u16* __restrict__ wo, const u16* __restrict__ w1,
            const u16* __restrict__ b1, const u16* __restrict__ w2, const u16* __restrict__ b2, u16* __restrict__ out, char* wsp) {
  extern __shared__ __attribute__((aligned(16))) char smem_[];
  LAS char* const L = (LAS char*)smem_;
  const u32 lds0 = (u32)(size_t)L;
  const int lane = threadIdx.x & 63;
  const int w = __builtin_amdgcn_readfirstlane(threadIdx.x >> 6);
  const int m = w & 3, grp = w >> 2;
  const int b = blockIdx.x, xcd = b & 7, r = b >> 3;
  const int r32 = lane & 31, hh = lane >> 5;
  const v4i ws = make_srd(wsp, WS_END);
  const v4i sx = make_srd(x, S_ * D_ * 2);
  const v4i sqkv = make_srd(wqkv, D_ * 3 * D_ * 2);
  const v4i sw1 = make_srd(w1, D_ * F_ * 2);
  const v4i sw2 = make_srd(w2, F_ * D_ * 2);
  STAMP_DECL;
  STAMP(0);
  CSTAMP(60);

  const int rt = 4 * (xcd >> 1) + (r & 3);
  const int cg = 8 * (xcd & 1) + (r >> 2);
  const bool heavy = r >= 28;
  const int tt = 4 * xcd + (r & 3), ct = r >> 2;
  const bool s1q = cg < 5;
  const int r8 = lane >> 3, c8 = lane & 7;

  v2u tag;
  v4u xr[14];
  {
    const u32 xo = (u32)((rt * 128 + 16 * w + r8) * 896 + c8 * 16);
#define LDX(g, i) ld16<(i) * 128>(xr[(g) * 7 + (i)], xo, sx, (u32)((g) * 7168))
    LDX(0, 0); LDX(0, 1); LDX(0, 2); LDX(0, 3); LDX(0, 4); LDX(0, 5); LDX(0, 6);
    LDX(1, 0); LDX(1, 1); LDX(1, 2); LDX(1, 3); LDX(1, 4); LDX(1, 5); LDX(1, 6);
#undef LDX
  }
  ld8c(tag, 0u, ws);
  u32 bias1, bias2, bias3;
  {
    const v4i sb1 = make_srd(b1, F_ * 2);
    const u32 bo = (u32)(r32 * 2);
    ld2(bias1, bo, sb1, (u32)(((s1q ? 0 : cg - 5) * 64 + grp * 32) * 2));
    ld2(bias2, bo, sb1, (u32)(((11 + cg) * 64 + grp * 32) * 2));
    ld2(bias3, bo, sb1, (u32)((27 * 64 + (xcd & 1) * 32) * 2));
  }
  const u32 gsw = (u32)((c8 ^ (((r8 >> 1) & 1) << 2)) * 16);
  u32 goff_q = (u32)(r8 * 2688) + gsw, goff_1 = (u32)(r8 * 3584) + gsw, goff_2 = (u32)(r8 * 896) + gsw;
  const int tq = (lane & 15) >> 2, tp = lane & 3, tg = lane >> 4;
  int trl = ((tg >> 1) * 8 + tq) * 128 + (((2 * (tg & 1) + (tp >> 1)) ^ (((tq >> 1) & 1) << 2)) << 4) + (tp & 1) * 8;
  const int tb1 = 4 * rt + m;
  u32 lo16 = (u32)(lane * 16);
  float gc1 = -0.1029432397f;
  asm volatile("" : "+v"(gc1));
  const int hd = 2 * xcd + (r & 1), tbq = 4 * (15 - (r >> 1)) + m;
  const int c0 = (28 * hd) >> 3;
  const bool odd = hd & 1;
  u32 fo_qk, fo_v;
  {
    const int n = lane >> 1;
    int kbl = tbq - (grp == 0 ? (n == 0 ? 0 : 4 + n) : 1 + n);
    kbl = (kbl < 0 || n >= 5 - grp) ? tbq : kbl;
    const int fbl = (8 * c0 + (lane & 1) * 31) >> 5;
    fo_v = (u32)(WS_FQ + (kbl * 48 + fbl + 28) * 8);
    fo_qk = (u32)(WS_FQ + (kbl * 48 + fbl + (n < 5 ? 14 : 0)) * 8);
  }

  u32 vkeep = r32 == (odd ? 0 : 28) ? 0u : 1u;
  u32 vbase = vkeep ? (u32)(WS_VT + hh * 7168 + (8 * c0 + r32) * 16) : (u32)(WS_ONE + b * 128);
  asm volatile("" : "+v"(vkeep), "+v"(vbase));
  u32 xb[4];
#pragma unroll
  for (int j = 0; j < 4; ++j) xb[j] = (u32)(65536 - grp * 16384 + m * 4096 + (2 * j + hh) * 512 + ((r32 ^ (2 * j + hh)) << 4));
  asm volatile("" : "+v"(goff_q), "+v"(goff_1), "+v"(goff_2), "+v"(trl), "+v"(lo16), "+v"(fo_qk), "+v"(fo_v));
  asm volatile("" : "+v"(xb[0]), "+v"(xb[1]), "+v"(xb[2]), "+v"(xb[3]));
  {
    u32 sel = ((lane & 15) == 4 * (lane >> 4)) ? 0x3f803f80u : 0u;
    v4u asum = {sel, sel, sel, sel};
    const int l3 = lane & 3;
    u32 dm0 = l3 == 0 ? 0xffffu : (l3 == 1 ? 0xffff0000u : 0u), dm1 = l3 == 2 ? 0xffffu : (l3 == 3 ? 0xffff0000u : 0u);
    u32 dsto[2][2];
#pragma unroll
    for (int g = 0; g < 2; ++g) {
      dsto[g][0] = (u32)(49152 + (w >> 1) * 4096 + c8 * 512 + (((16 * (w & 1) + 8 * g + r8) ^ c8) << 4));
      dsto[g][1] = dsto[g][0] + 65536u;
    }
    asm volatile("" : "+v"(asum), "+v"(dm0), "+v"(dm1), "+v"(dsto[0][0]), "+v"(dsto[0][1]), "+v"(dsto[1][0]), "+v"(dsto[1][1]));
#pragma unroll
    for (int g = 0; g < 2; ++g) {
      if (g == 0) vm_wait<11>(); else vm_wait<4>();
#pragma unroll
      for (int i = 0; i < 7; ++i) pin(xr[g * 7 + i]);
      if (g == 0) STAMP(1); else STAMP(2);
      f32x4 s1 = {0.f, 0.f, 0.f, 0.f}, qa = s1, qb = s1;
#pragma unroll
      for (int i = 0; i < 7; ++i) {
        const v4u d = xr[g * 7 + i];
        const s16x4 lo = __builtin_bit_cast(s16x4, v2u{d[0], d[1]}), hi = __builtin_bit_cast(s16x4, v2u{d[2], d[3]});
        s1 = __builtin_amdgcn_mfma_f32_16x16x32_bf16(__builtin_bit_cast(bf16x8, asum), __builtin_bit_cast(bf16x8, d), s1, 0, 0, 0);
        qa = __builtin_amdgcn_mfma_f32_4x4x4bf16_1k(lo, lo, qa, 0, 0, 0);
        qb = __builtin_amdgcn_mfma_f32_4x4x4bf16_1k(hi, hi, qb, 0, 0, 0);
      }
      asm volatile("" : "+v"(s1), "+v"(qa), "+v"(qb));
      qa += qb;
      const float q01 = (l3 & 1) ? qa[1] : qa[0], q23 = (l3 & 1) ? qa[3] : qa[2];
      const float mean = red8(s1[0]) * (1.f / 448.f);
      const float ex2 = red8((l3 & 2) ? q23 : q01) * (1.f / 448.f);
      const float var = __builtin_fmaxf(__builtin_fmaf(-mean, mean, ex2), 0.f);
      const float rstd = __builtin_amdgcn_rsqf(var + 1e-5f);
      const u32 rr = pk(rstd, rstd);
      const float nc = -mean * __builtin_bit_cast(float, rr << 16);
      const f32x4 cc = {nc, nc, nc, nc};
      const s16x4 an = __builtin_bit_cast(s16x4, v2u{rr & dm0, rr & dm1});
#pragma unroll
      for (int i = 0; i < 7; ++i) {
        const v4u d = xr[g * 7 + i];
        const f32x4 y0 = __builtin_amdgcn_mfma_f32_4x4x4bf16_1k(an, __builtin_bit_cast(s16x4, v2u{d[0], d[1]}), cc, 0, 0, 0);
        const f32x4 y1 = __builtin_amdgcn_mfma_f32_4x4x4bf16_1k(an, __builtin_bit_cast(s16x4, v2u{d[2], d[3]}), cc, 0, 0, 0);
        *(LAS v4u*)(L + (i == 6 ? dsto[g][0] : (i < 3 ? dsto[g][0] + 16384 * (i + 1) : dsto[g][1] + 16384 * (i - 3)))) = v4u{pk(y0[0], y0[1]), pk(y0[2], y0[3]), pk(y1[0], y1[1]), pk(y1[2], y1[3])};
      }
    }
  }
  STAMP(3);
  if (grp == 0) {
#pragma unroll 1
    for (int i = 0; i < 3; ++i)
      load_pieces(lds0 + (u32)(16384 * i + 4096 * m), goff_q, sqkv, (u32)((128 * i + 32 * m) * 2688 + cg * 128), 1024u, 8u * 2688u, 4);
  }
  if (threadIdx.x == 256) st16c<0>(v4u{0x3f803f80u, 0x3f803f80u, 0x3f803f80u, 0x3f803f80u}, 0u, ws, (u32)(WS_ONE + b * 128));
  Stream g;
  wg_barrier();
  STAMP(4);
  v4u xa[28];
  const int xlast = grp == 0 ? -16384 : 6 * 16384;
#define RDX(s) xa[s] = *(LAS v4u*)(L + xb[(s) & 3] + (((s) >> 2) == 6 ? xlast : 16384 * ((s) >> 2)))
#pragma unroll
  for (int s = 0; s < 4; ++s) RDX(s);
  const u32 st1 = s1q ? 2688u : 3584u;
  const int strip1 = s1q ? 16 + cg : cg - 5;
  g.srd = s1q ? sqkv : sw1; g.voff = s1q ? goff_q : goff_1; g.s8 = 8 * st1;
  if (grp == 1) {
    vm_wait<0>();
    wg_barrier();
    load_cells(lds0 + (u32)(57344 + m * 2048), g.voff, g.srd, (u32)(m * 16) * st1 + (u32)(strip1 * 128), st1, 1);
  }
#pragma unroll
  for (int s = 4; s < 16; ++s) RDX(s);
  if (grp == 1) asm volatile("s_waitcnt lgkmcnt(0)" ::: "memory");
#pragma unroll
  for (int s = 16; s < 28; ++s) RDX(s);
#undef RDX
  STAMP(5);
  const bool grpb = grp == 1;
  u32 q1 = (u32)((trl ^ (grp * 64)) + (grpb ? 49152 : 0)), q2 = (u32)((trl ^ (grp * 64)) + (grpb ? 0 : 8192)),
      q3 = (u32)((trl ^ (grp * 64)) + (grpb ? 40960 : 49152));
  u32 vst = (u32)(hh * 7168 + r32 * 16);
  Stream g0;
  g0.srd = sw1; g0.voff = goff_1; g0.s8 = 8 * 3584; g0.lds = lds0 + (u32)(114688 + 4096 * m);
  g0.soff = (u32)(32 * m * 3584 + (11 + cg) * 128); g0.n = grpb ? 12 : 0;
  asm volatile("" : "+v"(q1), "+v"(q2), "+v"(q3), "+v"(vst));
  keep_s(g0.lds); keep_s(g0.soff); keep_s(g0.n);
  if (!grpb) {
    vm_wait<0>();
    STAMP(6);
    wg_barrier();
    load_cells(lds0 + (u32)(49152 + m * 2048), goff_q, sqkv, (u32)((384 + m * 16) * 2688 + cg * 128), 2688u, 1);
  } else {
    g.soff = (u32)(64 + 32 * m) * st1 + (u32)(strip1 * 128); g.lds = lds0 + (u32)(65536 + 4096 * m); g.n = 12;
    stream_run<0, 6>(g);
    STAMP(6);
    wg_barrier();
    stream_run<6, 12>(g);
  }
  pin(tag); pin(bias1); pin(bias2); pin(bias3);
  STAMP(8);
  f32x16 acc;
  v2u fpoll;
  u32 pend;
  bool pending;
  int blk;
  if (cg >= 14) {
    acc = gemm1_block<0, true, false>(xa, L + q1, L + q2, L + q3, 0u, g0, grpb, false, false, 0u, tag, ws);
    STAMP(9);
    store_v(acc, vst, ws, (u32)(WS_VT + tb1 * VT_TB + (2 * cg + grp - 28) * 512));
  } else {
    acc = gemm1_block<0, false, false>(xa, L + q1, L + q2, L + q3, 0u, g0, grpb, false, false, 0u, tag, ws);
    STAMP(9);
    v4u o0, o1;
    pack_tile(acc, o0, o1);
    const u32 so = (u32)(WS_QK + tb1 * QK_TB + (2 * cg + grp) * 2048);
    st16c<0>(o0, lo16, ws, so);
    st16c<1024>(o1, lo16, ws, so);
  }
  { const u64 t = (((u64)tag[1] << 32) | tag[0]) + 1; tag[0] = (u32)t; tag[1] = (u32)(t >> 32); }
  pend = (u32)(WS_FQ + (tb1 * 48 + 2 * cg + grp) * 8);
  const u32 st2 = heavy ? 3584u : 896u;
  g.srd = heavy ? sw1 : sw2; g.voff = heavy ? goff_1 : goff_2; g.s8 = 8 * st2;
  const u32 so2 = (u32)(32 * m) * st2 + (u32)((heavy ? 27 : ct) * 128);
  g.soff = so2 + (u32)(grp * 128) * st2; g.lds = lds0 + (u32)(grp * 16384 + 4096 * m); g.n = 4;
  {
    u32 p1 = q1 + 57344u, p2 = q2 + 57344u, p3 = q3 + 57344u;
    u32 tl = lds0 + (u32)(49152 + m * 2048), ts = (u32)((384 + m * 16) * 3584 + (11 + cg) * 128);
    asm volatile("" : "+v"(p1), "+v"(p2), "+v"(p3), "+v"(tag), "+v"(g.voff));
    keep_s(g.lds); keep_s(g.soff); keep_s(g.s8); keep_s(pend); keep_s(tl); keep_s(ts);
    STAMP(10);
    if (!grpb) {
      wg_barrier();
      load_cells(tl, goff_1, sw1, ts, 3584u, 1);
    }
    STAMP(12);
    if (s1q) {
      acc = gemm1_block<1, true, false>(xa, L + p1, L + p2, L + p3, 0u, g, grpb, true, false, pend, tag, ws);
      STAMP(13);
      store_v(acc, vst, ws, (u32)(WS_VT + tb1 * VT_TB + (2 * cg + grp + 4) * 512));
    } else {
      acc = gemm1_block<1, false, true>(xa, L + p1, L + p2, L + p3, bias1, g, grpb, true, false, pend, tag, ws);
      STAMP(13);
      store_gelu(acc, gc1, lo16, ws, (u32)(WS_HH + tb1 * HH_TB + (2 * cg - 10 + grp) * 2048));
    }
  }
  pend = (u32)((s1q ? WS_FQ + (tb1 * 48 + 32) * 8 : WS_FH + (tb1 * 64 - 10) * 8) + (2 * cg + grp) * 8);
  {
    g.soff = so2 + (grpb ? 256u : 384u) * st2; g.lds = lds0 + (u32)((grpb ? 4 : 7) * 8192 + 4096 * m); g.n = (grpb || heavy) ? 4 : 8;
    u32 p1 = q1 + (grpb ? 0u : 114688u), p2 = q2 + 114688u, p3 = q3 + (grpb ? 114688u : 0u);
    asm volatile("" : "+v"(p1), "+v"(p2), "+v"(p3));
    keep_s(g.lds); keep_s(g.soff); keep_s(g.n); keep_s(pend);
    STAMP(14);
    if (!grpb) wg_barrier();
    STAMP(16);
    u64 xst[5];
    acc = gemm1_block<2, false, true>(xa, L + p1, L + p2, L + p3, bias2, g, grpb, s1q, heavy, pend, tag, ws, xst);
    STAMP(17);
    pending = !s1q;
    blk = 22 + 2 * cg + grp;
  }
  if (heavy && !grpb) {
    vm_wait<0>();
    post_flag(pend, tag, ws);
    store_gelu(acc, gc1, lo16, ws, (u32)(WS_HH + tb1 * HH_TB + blk * 2048));
    pend = (u32)(WS_FH + (tb1 * 64 + blk) * 8);
    blk = 54 + (xcd & 1);
    const u32 t = (u32)(trl ^ ((xcd & 1) * 64));
    wg_barrier();
    STAMP(20);
    acc = gemm1_block<3, false, true>(xa, L + t, L + t + 8192, L + t + 57344, bias3, g, false, false, false, pend, tag, ws);
    STAMP(21);
  }

  {
    int kb[5];
#pragma unroll
    for (int n = 0; n < 5; ++n) kb[n] = tbq - (grp == 0 ? (n == 0 ? 0 : 4 + n) : 1 + n);
    v4u qf[2], kf[5][2], vf[5][2];
    {
      ld8c(fpoll, fo_qk, ws);
      u32 p[8];
      mfma_settle(acc);
      gelu4(p[0], p[1], acc[0], acc[1], acc[2], acc[3], gc1);
      gelu4(p[2], p[3], acc[4], acc[5], acc[6], acc[7], gc1);
      STAMP(24);
      vm_wait<0>();
      pin(fpoll);
      if (pending) post_flag(pend, tag, ws);
      if (!flags_ok(fpoll, lane < 12, tag)) wait_flags(fo_qk, lane < 12, tag, ws);
      STAMP(25);
      const u32 kbase = (u32)(WS_QK + (56 + c0) * 512);
      ld16c<0>(qf[0], lo16, ws, (u32)(WS_QK + tbq * QK_TB + c0 * 512));
      ld16c<1024>(qf[1], lo16, ws, (u32)(WS_QK + tbq * QK_TB + c0 * 512));
#define LDK(n) { const u32 so_ = kbase + (u32)((kb[n] < 0 ? tbq : kb[n]) * QK_TB); ld16c<0>(kf[n][0], lo16, ws, so_); ld16c<1024>(kf[n][1], lo16, ws, so_); }
      LDK(0)
      gelu4(p[4], p[5], acc[8], acc[9], acc[10], acc[11], gc1);
      LDK(1) LDK(2)
      gelu4(p[6], p[7], acc[12], acc[13], acc[14], acc[15], gc1);
      LDK(3)
      if (grp == 0) LDK(4)
#undef LDK
      v4u o0, o1;
      swap_tile(p, o0, o1);
      const u32 so = (u32)(WS_HH + tb1 * HH_TB + blk * 2048);
      st16c<0>(o0, lo16, ws, so);
      st16c<1024>(o1, lo16, ws, so);
      pend = (u32)(WS_FH + (tb1 * 64 + blk) * 8);
    }
    STAMP(26);
    f32x16 md, mw;
    {
      const u32 ninf = grp == 0 ? 0xff800000u : 0u;
      const int dd = r32 - 4 * hh;
#pragma unroll
      for (int i = 0; i < 16; ++i) {
        const u32 t = (u32)((dd - ((i & 3) + 8 * (i >> 2))) >> 31) & ninf;
        md[i] = __builtin_bit_cast(float, t);
        mw[i] = __builtin_bit_cast(float, t ^ 0xff800000u);
      }
    }
    asm volatile("" : "+v"(md), "+v"(mw));
    if (grp == 0 && !heavy) wg_barrier();
    ld8c(fpoll, fo_v, ws);
    STAMP(32);
    CSTAMP(61);
    vm_wait<3>();
    pin(qf[0]); pin(qf[1]);
#pragma unroll
    for (int n = 0; n < 5; ++n) { pin(kf[n][0]); pin(kf[n][1]); }
    STAMP(33);
    {
      const u32 k0 = (odd && hh == 0) ? 0u : ~0u, k1 = (!odd && hh == 1) ? 0u : ~0u;
      qf[0][0] &= k0; qf[0][1] &= k0; qf[1][2] &= k1; qf[1][3] &= k1;
    }
    f32x16 sc[5];
    f32x16 zero;
#pragma unroll
    for (int i = 0; i < 16; ++i) zero[i] = 0.f;
    float mx = -1e30f;
#pragma unroll
    for (int n = 0; n < 4; ++n) {
      sc[n] = mfma(kf[n][0], qf[0], n == 0 ? md : zero); sc[n] = mfma(kf[n][1], qf[1], sc[n]);
      __builtin_amdgcn_sched_barrier(0);
    }
    if (grp == 0) { sc[4] = mfma(kf[4][0], qf[0], mw); sc[4] = mfma(kf[4][1], qf[1], sc[4]); }
    float gate[5];
#pragma unroll
    for (int n = 0; n < 5; ++n) gate[n] = kb[n] < 0 ? -__builtin_inff() : 0.f;
    mfma_settle(sc[0]);
    mx = __builtin_fmaxf(mx, max16(sc[0]) + gate[0]);
    mx = __builtin_fmaxf(mx, max16(sc[1]) + gate[1]);
    mx = __builtin_fmaxf(mx, max16(sc[2]) + gate[2]);
    mfma_settle(sc[3]);
    mx = __builtin_fmaxf(mx, max16(sc[3]) + gate[3]);
    if (grp == 0) { mfma_settle(sc[4]); mx = __builtin_fmaxf(mx, max16(sc[4]) + gate[4]); }
    mx = __builtin_fmaxf(mx, swap32(mx));
    float cs = 0.18898223650461362f * 1.4426950408889634f;
    asm volatile("" : "+v"(cs));
    const float nb = -mx * cs;
    float nbn[5];
#pragma unroll
    for (int n = 0; n < 5; ++n) nbn[n] = nb + gate[n];
    v4u pf[5][2];
    pf[0][0] = exp8(sc[0], 0, cs, nbn[0]); pf[0][1] = exp8(sc[0], 8, cs, nbn[0]);
    pf[1][0] = exp8(sc[1], 0, cs, nbn[1]); pf[1][1] = exp8(sc[1], 8, cs, nbn[1]);
    pin(pf[0][0]); pin(pf[0][1]); pin(pf[1][0]); pin(pf[1][1]);
    STAMP(34);
    vm_wait<0>();
    pin(fpoll);
    post_flag(pend, tag, ws);
    if (!flags_ok(fpoll, lane < 10, tag)) wait_flags(fo_v, lane < 10, tag, ws);
    STAMP(35);
    {
#define LDV(n) { const u32 so_ = (u32)((kb[n] < 0 ? tbq : kb[n]) * VT_TB); \
      ld16c<0>(vf[n][0], mad24(vkeep, so_, vbase), ws, 0u); ld16c<0>(vf[n][1], mad24(vkeep, so_ + 14336u, vbase), ws, 0u); \
      __builtin_amdgcn_sched_barrier(0); }
      LDV(0) LDV(1)
      pf[2][0] = exp8(sc[2], 0, cs, nbn[2]); pin(pf[2][0]);
      LDV(2)
      pf[2][1] = exp8(sc[2], 8, cs, nbn[2]); pin(pf[2][1]);
      LDV(3)
      pf[3][0] = exp8(sc[3], 0, cs, nbn[3]); pin(pf[3][0]);
      if (grp == 0) LDV(4)
      pf[3][1] = exp8(sc[3], 8, cs, nbn[3]); pin(pf[3][1]);
#undef LDV
    }
    if (grp == 0) { pf[4][0] = exp8(sc[4], 0, cs, nbn[4]); pf[4][1] = exp8(sc[4], 8, cs, nbn[4]); pin(pf[4][0]); pin(pf[4][1]); }
    STAMP(36);
    vm_wait<0>();
#pragma unroll
    for (int n = 0; n < 5; ++n) { pin(vf[n][0]); pin(vf[n][1]); }
    f32x16 o = zero;
#pragma unroll
    for (int n = 0; n < 4; ++n) { o = mfma(vf[n][0], pf[n][0], o); o = mfma(vf[n][1], pf[n][1], o); }
    LAS char* sp = L + 17 * 8192 + m * 4096 + lane * 16;
    LAS float* mp = (LAS float*)(L + 6 * 8192 + m * 256 + lane * 4);
    if (grp == 0) {
      o = mfma(vf[4][0], pf[4][0], o); o = mfma(vf[4][1], pf[4][1], o);
#pragma unroll
      for (int q = 0; q < 4; ++q) *(LAS f32x4*)(sp + q * 1024) = f32x4{o[4 * q], o[4 * q + 1], o[4 * q + 2], o[4 * q + 3]};
      *mp = nb;
    }
    STAMP(37);
    wg_barrier();
    STAMP(38);
    if (grp == 1) {
      const float na = *mp;
      const float nm = __builtin_fminf(na, nb);
      const float fa = __builtin_amdgcn_exp2f(nm - na), fb = __builtin_amdgcn_exp2f(nm - nb);
#pragma unroll
      for (int q = 0; q < 4; ++q) {
        const f32x4 t = *(LAS f32x4*)(sp + q * 1024);
#pragma unroll
        for (int j = 0; j < 4; ++j) o[4 * q + j] = o[4 * q + j] * fb + t[j] * fa;
      }
      const float lown = odd ? o[0] : o[12], loth = swap32(lown);
      const float rl = __builtin_amdgcn_rcpf((odd ? hh == 0 : hh == 1) ? lown : loth);
#pragma unroll
      for (int i = 0; i < 16; ++i) o[i] *= rl;
      v4u o0, o1;
      pack_tile(o, o0, o1);
      const u32 so = (u32)(WS_HH + tbq * HH_TB + (224 + c0) * 512);
      if (odd) {
        st16c<1024>(o1, lo16, ws, so);
        if (hh == 1) st16c<0>(o0, lo16, ws, so); else st8c<8>(v2u{o0[2], o0[3]}, lo16, ws, so);
      } else {
        st16c<0>(o0, lo16, ws, so);
        if (hh == 0) st16c<1024>(o1, lo16, ws, so); else st8c<1024>(v2u{o1[0], o1[1]}, lo16, ws, so);
      }
    }
    pend = (u32)(WS_FO + (tbq * 16 + hd) * 8);
    STAMP(39);
  }

  if (heavy) {
    if (grp == 1) { vm_wait<0>(); post_flag(pend, tag, ws); }
    if (b == 255 && w == 0) {
#pragma unroll 1
      for (u32 spins = 0; spins < SPIN_CAP; ++spins) {
        v4u f[8];
        ld16c<0>(f[0], lo16, ws, (u32)WS_FO); ld16c<1024>(f[1], lo16, ws, (u32)WS_FO);
        ld16c<2048>(f[2], lo16, ws, (u32)WS_FO); ld16c<3072>(f[3], lo16, ws, (u32)WS_FO);
        ld16c<0>(f[4], lo16, ws, (u32)(WS_FO + 4096)); ld16c<1024>(f[5], lo16, ws, (u32)(WS_FO + 4096));
        ld16c<2048>(f[6], lo16, ws, (u32)(WS_FO + 4096)); ld16c<3072>(f[7], lo16, ws, (u32)(WS_FO + 4096));
        vm_wait<0>();
        bool ok = true;
#pragma unroll
        for (int i = 0; i < 8; ++i) { pin(f[i]); ok = ok && f[i][0] == tag[0] && f[i][1] == tag[1] && f[i][2] == tag[0] && f[i][3] == tag[1]; }
        if (__all(ok)) break;
        __builtin_amdgcn_s_sleep(8);
      }
      post_flag((u32)WS_E, tag, ws);
    }
    STAMP_OUT();
    return;
  }

  {
    const int kq = m, mh = grp, tb = 2 * tt + mh;
    const __attribute__((address_space(4))) u64* ka = (const __attribute__((address_space(4))) u64*)__builtin_amdgcn_kernarg_segment_ptr();
    asm volatile("" : "+s"(ka));
    const v4i sx2 = make_srd((const void*)ka[0], S_ * D_ * 2), swo = make_srd((const void*)ka[2], D_ * D_ * 2);
    const v4i sb2 = make_srd((const void*)ka[6], D_ * 2), sout = make_srd((const void*)ka[7], S_ * D_ * 2);
    auto cell_of = [&](int pos) { const int i = 4 * pos + kq; return i < 6 ? i : (i < 19 ? i + 1 : 6); };
    const u32 fo_h = (u32)(WS_FH + (tb * 64 + ((lane >> 1) * 4 + kq) * 2 + (lane & 1)) * 8);
    ld8c(fpoll, fo_h, ws);
    load_pieces(lds0 + (u32)(11 * 8192 + w * 1024), goff_2, sw2, (u32)((640 + w * 8) * 896 + ct * 128), 8192u, 64u * 896u, 6);
    v4u xres, bres;
    const u32 oo = (u32)((tb * 32 + r32) * 896 + (ct * 64 + kq * 16 + hh * 8) * 2);
    ld16<0>(xres, oo, sx2, 0u);
    ld16<0>(bres, (u32)(hh * 16), sb2, (u32)((ct * 64 + kq * 16) * 2));
    vm_wait<8>();
    pin(fpoll);
    if (grp == 1) post_flag(pend, tag, ws);
    if (!flags_ok(fpoll, lane < 14, tag)) wait_flags(fo_h, lane < 14, tag, ws);
    STAMP(40);
    v4u hf[35];
    const u32 hb = (u32)(WS_HH + tb * HH_TB + kq * 4096);
#define LDH1(j, s) ld16c<(s) * 1024>(hf[4 * (j) + (s)], lo16, ws, (j) < 8 ? hb + (j) * 16384 : hb + 8 * 16384 - kq * 1024)
#pragma unroll
    for (int j = 0; j < 2; ++j) { LDH1(j, 0); LDH1(j, 1); LDH1(j, 2); LDH1(j, 3); }
    f32x16 acc0, acc1;
#pragma unroll
    for (int i = 0; i < 16; ++i) { acc0[i] = 0.f; acc1[i] = 0.f; }
    const u32 fo_o = (u32)(WS_FO + (tb * 16 + (lane & 15)) * 8);
#define STAGE(j, WAIT) { \
      constexpr int ns = (j) == 8 ? 3 : 4; \
      vm_wait<WAIT>(); \
      pin(hf[4 * (j)]); pin(hf[4 * (j) + 1]); pin(hf[4 * (j) + 2]); if (ns == 4) pin(hf[4 * (j) + (ns - 1)]); \
      if ((j) == 5) { pin(fpoll); if (!flags_ok(fpoll, true, tag)) wait_flags(fo_o, true, tag, ws); } \
      wg_barrier(); \
      STAMP(41 + (j)); \
      LAS char* p0 = L + cell_of((j) % 5) * 8192 + trl; \
      LAS char* p1 = L + cell_of((j) % 5) * 8192 + (trl ^ 64); \
      v4u wa[4], wb[4]; \
      wa[0] = tr_frag(p0); wb[0] = tr_frag(p1); wa[1] = tr_frag(p0 + 2048); wb[1] = tr_frag(p1 + 2048); \
      wa[2] = tr_frag(p0 + 4096); wb[2] = tr_frag(p1 + 4096); \
      if (ns == 4) { wa[3] = tr_frag(p0 + 6144); wb[3] = tr_frag(p1 + 6144); } \
      u32 gl = lds0 + (u32)(cell_of(((j) + 4) % 5) * 8192 + mh * ((j) == 4 ? 3072 : 4096)); \
      u32 gs = (u32)(((j) < 3 ? 256 * ((j) + 4) + 64 * kq + 32 * mh : ((j) == 3 ? 64 * kq + 32 * mh : 256 + 48 * kq + 24 * mh)) * 896 + ct * 128); \
      __builtin_amdgcn_sched_barrier(0); \
      if ((j) <= 6) LDH1(((j) <= 6 ? (j) + 2 : 0), 0); \
      acc0 = mfma(wa[0], hf[4 * (j)], acc0); __builtin_amdgcn_sched_barrier(0); \
      if ((j) <= 6) LDH1(((j) <= 6 ? (j) + 2 : 0), 1); \
      acc1 = mfma(wb[0], hf[4 * (j)], acc1); __builtin_amdgcn_sched_barrier(0); \
      if ((j) <= 6) LDH1(((j) <= 6 ? (j) + 2 : 0), 2); \
      acc0 = mfma(wa[1], hf[4 * (j) + 1], acc0); __builtin_amdgcn_sched_barrier(0); \
      if ((j) <= 5) LDH1(((j) <= 5 ? (j) + 2 : 0), 3); \
      acc1 = mfma(wb[1], hf[4 * (j) + 1], acc1); __builtin_amdgcn_sched_barrier(0); \
      if ((j) <= 4) glds16(gl, goff_2, (j) < 3 ? sw2 : swo, gs); \
      acc0 = mfma(wa[2], hf[4 * (j) + 2], acc0); __builtin_amdgcn_sched_barrier(0); \
      if ((j) <= 4) glds16(gl + 1024, goff_2, (j) < 3 ? sw2 : swo, gs + 7168); \
      acc1 = mfma(wb[2], hf[4 * (j) + 2], acc1); __builtin_amdgcn_sched_barrier(0); \
      if ((j) <= 4) glds16(gl + 2048, goff_2, (j) < 3 ? sw2 : swo, gs + 14336); \
      if (ns == 4) { acc0 = mfma(wa[3], hf[4 * (j) + (ns - 1)], acc0); __builtin_amdgcn_sched_barrier(0); } \
      if ((j) <= 3) glds16(gl + 3072, goff_2, (j) < 3 ? sw2 : swo, gs + 21504); \
      if ((j) == 3) ld8c(fpoll, fo_o, ws); \
      if (ns == 4) { acc1 = mfma(wb[3], hf[4 * (j) + (ns - 1)], acc1); __builtin_amdgcn_sched_barrier(0); } \
    }
    STAGE(0, 4) STAGE(1, 8) STAGE(2, 12) STAGE(3, 12) STAGE(4, 13) STAGE(5, 7) STAGE(6, 7) STAGE(7, 3) STAGE(8, 0)
#undef STAGE
#undef LDH1
    STAMP(50);
    {
      LAS char* rp = L + (w < 6 ? w : w + 1) * 8192 + lane * 16;
#pragma unroll
      for (int q = 0; q < 4; ++q) {
        *(LAS f32x4*)(rp + q * 1024) = f32x4{acc0[4 * q], acc0[4 * q + 1], acc0[4 * q + 2], acc0[4 * q + 3]};
        *(LAS f32x4*)(rp + 4096 + q * 1024) = f32x4{acc1[4 * q], acc1[4 * q + 1], acc1[4 * q + 2], acc1[4 * q + 3]};
      }
    }
    wg_barrier();
    STAMP(51);
    pin(xres); pin(bres);
    f32x4 a = {0.f, 0.f, 0.f, 0.f}, c = {0.f, 0.f, 0.f, 0.f};
#pragma unroll
    for (int k = 0; k < 4; ++k) {
      const int ww = 4 * mh + k;
      LAS char* rp = L + (ww < 6 ? ww : ww + 1) * 8192 + kq * 2048 + hh * 1024 + r32 * 16;
      a += *(LAS f32x4*)rp;
      c += *(LAS f32x4*)(rp + 512);
    }
    auto lo_f = [](u32 d) { return __builtin_bit_cast(float, d << 16); };
    auto hi_f = [](u32 d) { return __builtin_bit_cast(float, d & 0xffff0000u); };
    v4u o;
    o[0] = pk(a[0] + lo_f(xres[0]) + lo_f(bres[0]), a[1] + hi_f(xres[0]) + hi_f(bres[0]));
    o[1] = pk(a[2] + lo_f(xres[1]) + lo_f(bres[1]), a[3] + hi_f(xres[1]) + hi_f(bres[1]));
    o[2] = pk(c[0] + lo_f(xres[2]) + lo_f(bres[2]), c[1] + hi_f(xres[2]) + hi_f(bres[2]));
    o[3] = pk(c[2] + lo_f(xres[3]) + lo_f(bres[3]), c[3] + hi_f(xres[3]) + hi_f(bres[3]));
    st16(o, oo, sout);
    STAMP(52);
    CSTAMP(62);
    STAMP_OUT();
  }
}

extern "C" void kernel_launch(void* const* d_in, const int* in_sizes, int n_in, void* d_out, int out_size, void* d_ws, size_t ws_size,
                              hipStream_t stream) {
  (void)hipFuncSetAttribute((const void*)fused_block, hipFuncAttributeMaxDynamicSharedMemorySize, LDS_BYTES);
  fused_block<<<256, 512, LDS_BYTES, stream>>>((const u16*)d_in[0], (const u16*)d_in[1], (const u16*)d_in[2], (const u16*)d_in[4],
                                               (const u16*)d_in[5], (const u16*)d_in[6], (const u16*)d_in[7], (u16*)d_out, (char*)d_ws);
}
```

```cpp
#define STAMPS 1
#define PROBE_I 0
#define PROBE_J 52
#define PROBE_K 0
#define PROBE_W 4
#include <hip/hip_runtime.h>

typedef unsigned short u16;
typedef unsigned int u32;
typedef unsigned long long u64;
typedef u32 v4u __attribute__((ext_vector_type(4)));
typedef u32 v2u __attribute__((ext_vector_type(2)));
typedef int v4i __attribute__((ext_vector_type(4)));
typedef short s16x4 __attribute__((ext_vector_type(4)));
typedef __bf16 bf16x8 __attribute__((ext_vector_type(8)));
typedef __bf16 bf16x2 __attribute__((ext_vector_type(2)));
typedef float f32x2 __attribute__((ext_vector_type(2)));
typedef float f32x4 __attribute__((ext_vector_type(4)));
typedef float f32x16 __attribute__((ext_vector_type(16)));
#define GAS __attribute__((address_space(1)))
#define LAS __attribute__((address_space(3)))
#define DI __device__ __forceinline__

#define EXP_NOSTREAM 0
#define EXP_XST 0

constexpr int S_ = 2048, D_ = 448, F_ = 1792;
constexpr int WS_E = 0;
constexpr int WS_FQ = 4096;
constexpr int WS_FH = WS_FQ + 64 * 48 * 8;
constexpr int WS_FO = WS_FH + 64 * 64 * 8;
constexpr int WS_ONE = 1 << 19;
constexpr int WS_QK = 1 << 20;
constexpr int QK_TB = 112 * 512;
constexpr int WS_VT = WS_QK + 64 * QK_TB;
constexpr int VT_TB = 4 * 448 * 16;
constexpr int WS_HH = WS_VT + 64 * VT_TB;
constexpr int HH_TB = 280 * 512;
constexpr int WS_END = WS_HH + 64 * HH_TB;
constexpr int WS_DBG = 32 << 20;
constexpr int LDS_BYTES = 163840;
constexpr u32 SPIN_CAP = 1u << 16;

DI v4i make_srd(const void* p, u32 bytes) {
  const u64 a = (u64)p;
  v4i r;
  r[0] = (int)(u32)a; r[1] = (int)((u32)(a >> 32) & 0xffffu); r[2] = (int)bytes; r[3] = 0x00020000;
  return r;
}
template <int OFF> DI void ld16(v4u& d, u32 voff, v4i srd, u32 soff) {
  asm volatile("buffer_load_dwordx4 %0, %1, %2, %3 offen offset:%4" : "=v"(d) : "v"(voff), "s"(srd), "s"(soff), "i"(OFF) : "memory");
}
template <int OFF> DI void ld16c(v4u& d, u32 voff, v4i srd, u32 soff) {
  asm volatile("buffer_load_dwordx4 %0, %1, %2, %3 offen offset:%4 sc1" : "=v"(d) : "v"(voff), "s"(srd), "s"(soff), "i"(OFF) : "memory");
}
DI void ld8c(v2u& d, u32 voff, v4i srd) {
  asm volatile("buffer_load_dwordx2 %0, %1, %2, 0 offen sc1" : "=v"(d) : "v"(voff), "s"(srd) : "memory");
}
DI void ld2(u32& d, u32 voff, v4i srd, u32 soff) {
  asm volatile("buffer_load_ushort %0, %1, %2, %3 offen" : "=v"(d) : "v"(voff), "s"(srd), "s"(soff) : "memory");
}
DI void glds16(u32 lds, u32 voff, v4i srd, u32 soff) {
  asm volatile("s_mov_b32 m0, %0\n\ts_nop 0\n\tbuffer_load_dwordx4 %1, %2, %3 offen lds" ::"s"(lds), "v"(voff), "s"(srd), "s"(soff) : "memory");
}
DI void load_cells(u32 lds, u32 voff, v4i srd, u32 soff, u32 stride, int n) {
#pragma unroll 1
  for (int c = 0; c < n; ++c) {
    u32 t;
    asm volatile("s_mov_b32 m0, %1\n\ts_add_u32 %0, %4, %5\n\tbuffer_load_dwordx4 %2, %3, %4 offen lds\n\t"
                 "s_add_u32 m0, %1, 0x400\n\ts_nop 0\n\tbuffer_load_dwordx4 %2, %3, %0 offen lds"
                 : "=&s"(t) : "s"(lds), "v"(voff), "s"(srd), "s"(soff), "s"(stride * 8) : "memory", "scc");
    lds += 8192; soff += stride * 64;
  }
}
DI void load_pieces(u32 lds, u32 voff, v4i srd, u32 soff, u32 lstep, u32 sstep, int n) {
#pragma unroll 1
  for (int c = 0; c < n; ++c) { glds16(lds, voff, srd, soff); lds += lstep; soff += sstep; }
}
template <int OFF> DI void st16c(v4u d, u32 voff, v4i srd, u32 soff) {
  asm volatile("buffer_store_dwordx4 %0, %1, %2, %3 offen offset:%4 sc1\n\ts_nop 1" ::"v"(d), "v"(voff), "s"(srd), "s"(soff), "i"(OFF) : "memory");
}
template <int OFF> DI void st8c(v2u d, u32 voff, v4i srd, u32 soff) {
  asm volatile("buffer_store_dwordx2 %0, %1, %2, %3 offen offset:%4 sc1" ::"v"(d), "v"(voff), "s"(srd), "s"(soff), "i"(OFF) : "memory");
}
DI void st16(v4u d, u32 voff, v4i srd) {
  asm volatile("buffer_store_dwordx4 %0, %1, %2, 0 offen\n\ts_nop 1" ::"v"(d), "v"(voff), "s"(srd) : "memory");
}
template <int N> DI void vm_wait() { asm volatile("s_waitcnt vmcnt(%0)" ::"i"(N) : "memory"); }
DI void pin(v4u& a) { asm volatile("" : "+v"(a)); }
DI void pin(v2u& a) { asm volatile("" : "+v"(a)); }
DI void pin(u32& a) { asm volatile("" : "+v"(a)); }
DI void keep_s(u32& a) { a = __builtin_amdgcn_readfirstlane(a); asm volatile("" : "+s"(a)); }
DI void wg_barrier() { asm volatile("s_waitcnt lgkmcnt(0)\n\ts_barrier" ::: "memory"); }
DI f32x16 mfma(v4u a, v4u b, f32x16 c) {
  return __builtin_amdgcn_mfma_f32_32x32x16_bf16(__builtin_bit_cast(bf16x8, a), __builtin_bit_cast(bf16x8, b), c, 0, 0, 0);
}
DI u32 pk(float a, float b) { f32x2 f = {a, b}; return __builtin_bit_cast(u32, __builtin_convertvector(f, bf16x2)); }
DI v4u tr_frag(LAS char* p) {
  const s16x4 a = __builtin_amdgcn_ds_read_tr16_b64_v4i16((LAS s16x4*)p);
  const s16x4 b = __builtin_amdgcn_ds_read_tr16_b64_v4i16((LAS s16x4*)(p + 512));
  const v2u x = __builtin_bit_cast(v2u, a), y = __builtin_bit_cast(v2u, b);
  return v4u{x[0], x[1], y[0], y[1]};
}
template <int C> DI float dpp_add(float x) {
  return x + __builtin_bit_cast(float, __builtin_amdgcn_update_dpp(0, __builtin_bit_cast(int, x), C, 0xf, 0xf, true));
}
DI float red8(float x) { x = dpp_add<0xB1>(x); x = dpp_add<0x4E>(x); return dpp_add<0x141>(x); }
DI float swap32(float x) {
  const u32 u = __builtin_bit_cast(u32, x);
  const auto r = __builtin_amdgcn_permlane32_swap(u, u, false, false);
  return __builtin_bit_cast(float, (threadIdx.x & 32) ? r[0] : r[1]);
}
DI void pack_tile(const f32x16& a, v4u& o0, v4u& o1) {
  u32 p[8];
#pragma unroll
  for (int i = 0; i < 8; ++i) p[i] = pk(a[2 * i], a[2 * i + 1]);
  const auto r0 = __builtin_amdgcn_permlane32_swap(p[0], p[2], false, false);
  const auto r1 = __builtin_amdgcn_permlane32_swap(p[1], p[3], false, false);
  const auto r2 = __builtin_amdgcn_permlane32_swap(p[4], p[6], false, false);
  const auto r3 = __builtin_amdgcn_permlane32_swap(p[5], p[7], false, false);
  o0 = v4u{r0[0], r1[0], r0[1], r1[1]};
  o1 = v4u{r2[0], r3[0], r2[1], r3[1]};
}
DI void wait_flags(u32 voff, bool active, v2u tag, v4i ws) {
#pragma unroll 1
  for (u32 spins = 0; spins < SPIN_CAP; ++spins) {
    v2u f;
    ld8c(f, voff, ws);
    vm_wait<0>();
    pin(f);
    const bool ok = !active || (f[0] == tag[0] && f[1] == tag[1]);
    if (__all(ok)) break;
    __builtin_amdgcn_s_sleep(2);
  }
}
DI void post_flag(u32 off, v2u tag, v4i ws) {
  asm volatile("s_mov_b64 exec, 1\n\tbuffer_store_dwordx2 %0, off, %1, %2 sc1\n\ts_mov_b64 exec, -1" ::"v"(tag), "s"(ws), "s"(off) : "memory");
}
#define STAMP(i) do { u64 t_; __builtin_amdgcn_sched_barrier(0); asm volatile("s_memrealtime %0\n\ts_waitcnt lgkmcnt(0)" : "=s"(t_) :: "memory"); \
  asm volatile("s_mov_b32 m0, %2\n\ts_nop 0\n\tv_writelane_b32 %0, %1, m0" : "+v"(stv_) : "s"((u32)t_), "s"((u32)(i))); __builtin_amdgcn_sched_barrier(0); } while (0)
#define CSTAMP(i) do { u64 t_; asm volatile("s_memtime %0\n\ts_waitcnt lgkmcnt(0)" : "=s"(t_) :: "memory"); \
  asm volatile("s_mov_b32 m0, %2\n\ts_nop 0\n\tv_writelane_b32 %0, %1, m0" : "+v"(stv_) : "s"((u32)t_), "s"((u32)(i))); } while (0)
#define XSTAMP(i) asm volatile("s_memtime %0" : "=s"(xst[(i) - 53]) :: "memory")
#define STAMP_DECL u32 stv_ = 0
#define STAMP_OUT() do { if (blockIdx.x == 100 && w == PROBE_W) { \
  const u32 d_ = (u32)__builtin_amdgcn_readlane(stv_, PROBE_J) - (u32)__builtin_amdgcn_readlane(stv_, PROBE_I); \
  const u32 t0_ = (u32)__builtin_amdgcn_s_memrealtime(); \
  for (int i_ = 0; i_ < 1000000; ++i_) { if ((u32)__builtin_amdgcn_s_memrealtime() - t0_ >= PROBE_K * d_) break; __builtin_amdgcn_s_sleep(1); } } } while (0)

struct Stream { v4i srd; u32 voff, soff, s8, lds, n; };
template <int P, bool COND> DI void stream_piece(const Stream& g) {
  u32 t;
  if constexpr (COND)
    asm volatile("s_cmp_gt_u32 %6, %9\n\ts_cbranch_scc0 1f\n\ts_mul_i32 %0, %5, %8\n\ts_add_u32 m0, %1, %7\n\ts_add_u32 %0, %0, %2\n\t"
                 "buffer_load_dwordx4 %3, %4, %0 offen lds\n1:"
                 : "=&s"(t) : "s"(g.lds), "s"(g.soff), "v"(g.voff), "s"(g.srd), "s"(g.s8), "s"(g.n),
                   "i"((P >> 2) * 16384 + (P & 3) * 1024), "i"((P >> 2) * 16 + (P & 3)), "i"(P) : "memory", "scc");
  else
    asm volatile("s_mul_i32 %0, %5, %7\n\ts_add_u32 m0, %1, %6\n\ts_add_u32 %0, %0, %2\n\tbuffer_load_dwordx4 %3, %4, %0 offen lds"
                 : "=&s"(t) : "s"(g.lds), "s"(g.soff), "v"(g.voff), "s"(g.srd), "s"(g.s8),
                   "i"((P >> 2) * 16384 + (P & 3) * 1024), "i"((P >> 2) * 16 + (P & 3)) : "memory", "scc");
}
template <int LO, int HI> DI void stream_run(const Stream& g) {
  if constexpr (LO < HI) { stream_piece<LO, false>(g); stream_run<LO + 1, HI>(g); }
}
DI u32 mad24(u32 a, u32 s, u32 c) { u32 d; asm("v_mad_u32_u24 %0, %1, %2, %3" : "=v"(d) : "v"(a), "s"(s), "v"(c)); return d; }
DI bool flags_ok(v2u f, bool active, v2u tag) { return __all(!active || (f[0] == tag[0] && f[1] == tag[1])); }

template <int SLOT, bool SWAP, bool BIAS>
DI f32x16 gemm1_block(const v4u (&xa)[28], LAS char* p1, LAS char* p2, LAS char* p3, u32 bias, const Stream& g, bool grpb, bool post, bool heavy,
                      u32 pend, v2u tag, v4i ws, u64* xst = nullptr) {
  constexpr int PD = 5;
  constexpr int NP = SLOT == 0 ? 12 : (SLOT == 1 ? 4 : (SLOT == 2 ? 8 : 0)), NU = SLOT == 0 ? 0 : 4;
  auto rd = [&](int s) { return s < 4 ? tr_frag(p1 + s * 2048) : (s < 24 ? tr_frag(p2 + (s - 4) * 2048) : tr_frag(p3 + (s - 24) * 2048)); };
  __builtin_amdgcn_s_setprio(1);
  v4u wf[PD];
#pragma unroll
  for (int s = 0; s < PD; ++s) wf[s] = rd(s);
  f32x16 acc;
#pragma unroll
  for (int i = 0; i < 16; ++i) acc[i] = 0.f;
  if constexpr (BIAS) {
    const v4u bf = {bias, 0u, 0u, 0u};
    const v4u one = {(threadIdx.x & 32) ? 0u : 0x3f80u, 0u, 0u, 0u};
    acc = mfma(bf, one, acc);
  }
  __builtin_amdgcn_sched_barrier(0);
#define G1STEP(s) { \
    const v4u cur = wf[(s) % PD]; \
    if ((s) == 19) { \
      if (SLOT == 0) { if (!grpb) { vm_wait<0>(); asm volatile("s_barrier" ::: "memory"); } } \
      else if (SLOT == 1) { vm_wait<4>(); post_flag(pend, tag, ws); } \
      else if (SLOT == 2) { if (post) { vm_wait<4>(); post_flag(pend, tag, ws); } } \
    } \
    if ((s) == 24 && SLOT < 3) { \
      if (grpb) { \
        if (SLOT == 0) vm_wait<12>(); \
        if (SLOT == 2) { if (heavy) vm_wait<0>(); } \
        asm volatile("s_barrier" ::: "memory"); \
        __builtin_amdgcn_s_setprio(2); \
      } \
    } \
    acc = SWAP ? mfma(xa[s], cur, acc) : mfma(cur, xa[s], acc); \
    if ((s) + PD < 28) wf[(s) % PD] = rd((s) + PD); \
    if (((s) & 1) == 0 && (s) / 2 < NP && !(EXP_NOSTREAM && SLOT > 0)) stream_piece<(s) / 2, ((s) / 2 >= NU)>(g); \
    if (SLOT == 2 && EXP_XST && ((s) == 0 || (s) == 6 || (s) == 12 || (s) == 18 || (s) == 23)) XSTAMP(53 + ((s) + 1) / 6); \
    __builtin_amdgcn_sched_barrier(0); }
  G1STEP(0) G1STEP(1) G1STEP(2) G1STEP(3) G1STEP(4) G1STEP(5) G1STEP(6) G1STEP(7) G1STEP(8) G1STEP(9)
  G1STEP(10) G1STEP(11) G1STEP(12) G1STEP(13) G1STEP(14) G1STEP(15) G1STEP(16) G1STEP(17) G1STEP(18) G1STEP(19)
  G1STEP(20) G1STEP(21) G1STEP(22) G1STEP(23) G1STEP(24) G1STEP(25) G1STEP(26) G1STEP(27)
#undef G1STEP
  __builtin_amdgcn_s_setprio(0);
  return acc;
}

DI void gelu4(u32& o0, u32& o1, float a, float b, float c, float d, float c1) {
  float t0, t1, t2, t3;
  asm volatile(
      "v_mul_f32 %2, %6, %6\n\tv_mul_f32 %3, %7, %7\n\tv_mul_f32 %4, %8, %8\n\tv_mul_f32 %5, %9, %9\n\t"
      "v_fmaak_f32 %2, %2, %10, 0xc0135761\n\tv_fmaak_f32 %3, %3, %10, 0xc0135761\n\t"
      "v_fmaak_f32 %4, %4, %10, 0xc0135761\n\tv_fmaak_f32 %5, %5, %10, 0xc0135761\n\t"
      "v_mul_f32 %2, %2, %6\n\tv_mul_f32 %3, %3, %7\n\tv_mul_f32 %4, %4, %8\n\tv_mul_f32 %5, %5, %9\n\t"
      "v_exp_f32 %2, %2\n\tv_exp_f32 %3, %3\n\tv_exp_f32 %4, %4\n\tv_exp_f32 %5, %5\n\t"
      "v_add_f32 %2, 1.0, %2\n\tv_add_f32 %3, 1.0, %3\n\tv_add_f32 %4, 1.0, %4\n\tv_add_f32 %5, 1.0, %5\n\t"
      "v_rcp_f32 %2, %2\n\tv_rcp_f32 %3, %3\n\tv_rcp_f32 %4, %4\n\tv_rcp_f32 %5, %5\n\t"
      "v_mul_f32 %2, %2, %6\n\tv_mul_f32 %3, %3, %7\n\tv_mul_f32 %4, %4, %8\n\tv_mul_f32 %5, %5, %9\n\t"
      "v_cvt_pk_bf16_f32 %0, %2, %3\n\tv_cvt_pk_bf16_f32 %1, %4, %5\n\ts_nop 1"
      : "=&v"(o0), "=&v"(o1), "=&v"(t0), "=&v"(t1), "=&v"(t2), "=&v"(t3) : "v"(a), "v"(b), "v"(c), "v"(d), "v"(c1));
}
DI void swap_tile(const u32 (&p)[8], v4u& o0, v4u& o1) {
  const auto r0 = __builtin_amdgcn_permlane32_swap(p[0], p[2], false, false);
  const auto r1 = __builtin_amdgcn_permlane32_swap(p[1], p[3], false, false);
  const auto r2 = __builtin_amdgcn_permlane32_swap(p[4], p[6], false, false);
  const auto r3 = __builtin_amdgcn_permlane32_swap(p[5], p[7], false, false);
  o0 = v4u{r0[0], r1[0], r0[1], r1[1]};
  o1 = v4u{r2[0], r3[0], r2[1], r3[1]};
}
DI void mfma_settle(const f32x16& a) { asm volatile("s_nop 7\n\ts_nop 4" ::"v"(a)); }
DI void store_v(const f32x16& a, u32 vo, v4i ws, u32 so) {
  st16c<0>(v4u{pk(a[0], a[1]), pk(a[2], a[3]), pk(a[4], a[5]), pk(a[6], a[7])}, vo, ws, so);
  st16c<0>(v4u{pk(a[8], a[9]), pk(a[10], a[11]), pk(a[12], a[13]), pk(a[14], a[15])}, vo, ws, so + 14336u);
}
DI void store_gelu(const f32x16& a, float gc1, u32 lo16, v4i ws, u32 so) {
  u32 p[8];
  mfma_settle(a);
#pragma unroll
  for (int i = 0; i < 4; ++i) gelu4(p[2 * i], p[2 * i + 1], a[4 * i], a[4 * i + 1], a[4 * i + 2], a[4 * i + 3], gc1);
  v4u o0, o1;
  swap_tile(p, o0, o1);
  st16c<0>(o0, lo16, ws, so);
  st16c<1024>(o1, lo16, ws, so);
}
DI float max16(const f32x16& s) {
  float a, b;
  asm volatile("v_max3_f32 %0, %2, %3, %4\n\tv_max3_f32 %1, %5, %6, %7\n\tv_max3_f32 %0, %0, %8, %9\n\tv_max3_f32 %1, %1, %10, %11\n\t"
               "v_max3_f32 %0, %0, %12, %13\n\tv_max3_f32 %1, %1, %14, %15\n\tv_max3_f32 %0, %0, %16, %17\n\tv_max_f32 %0, %0, %1"
               : "=&v"(a), "=&v"(b) : "v"(s[0]), "v"(s[1]), "v"(s[2]), "v"(s[3]), "v"(s[4]), "v"(s[5]), "v"(s[6]), "v"(s[7]), "v"(s[8]),
                 "v"(s[9]), "v"(s[10]), "v"(s[11]), "v"(s[12]), "v"(s[13]), "v"(s[14]), "v"(s[15]));
  return a;
}
DI v4u exp8(const f32x16& s, int o, float cs, float nb) {
  v4u p;
#pragma unroll
  for (int j = 0; j < 4; ++j)
    p[j] = pk(__builtin_amdgcn_exp2f(__builtin_fmaf(s[o + 2 * j], cs, nb)), __builtin_amdgcn_exp2f(__builtin_fmaf(s[o + 2 * j + 1], cs, nb)));
  return p;
}

__global__ void __launch_bounds__(512, 2)
fused_block(const u16* __restrict__ x, const u16* __restrict__ wqkv, const u16* __restrict__ wo, const u16* __restrict__ w1,
            const u16* __restrict__ b1, const u16* __restrict__ w2, const u16* __restrict__ b2, u16* __restrict__ out, char* wsp) {
  extern __shared__ __attribute__((aligned(16))) char smem_[];
  LAS char* const L = (LAS char*)smem_;
  const u32 lds0 = (u32)(size_t)L;
  const int lane = threadIdx.x & 63;
  const int w = __builtin_amdgcn_readfirstlane(threadIdx.x >> 6);
  const int m = w & 3, grp = w >> 2;
  const int b = blockIdx.x, xcd = b & 7, r = b >> 3;
  const int r32 = lane & 31, hh = lane >> 5;
  const v4i ws = make_srd(wsp, WS_END);
  const v4i sx = make_srd(x, S_ * D_ * 2);
  const v4i sqkv = make_srd(wqkv, D_ * 3 * D_ * 2);
  const v4i sw1 = make_srd(w1, D_ * F_ * 2);
  const v4i sw2 = make_srd(w2, F_ * D_ * 2);
  STAMP_DECL;
  STAMP(0);
  CSTAMP(60);

  const int rt = 4 * (xcd >> 1) + (r & 3);
  const int cg = 8 * (xcd & 1) + (r >> 2);
  const bool heavy = r >= 28;
  const int tt = 4 * xcd + (r & 3), ct = r >> 2;
  const bool s1q = cg < 5;
  const int r8 = lane >> 3, c8 = lane & 7;

  v2u tag;
  v4u xr[14];
  {
    const u32 xo = (u32)((rt * 128 + 16 * w + r8) * 896 + c8 * 16);
#define LDX(g, i) ld16<(i) * 128>(xr[(g) * 7 + (i)], xo, sx, (u32)((g) * 7168))
    LDX(0, 0); LDX(0, 1); LDX(0, 2); LDX(0, 3); LDX(0, 4); LDX(0, 5); LDX(0, 6);
    LDX(1, 0); LDX(1, 1); LDX(1, 2); LDX(1, 3); LDX(1, 4); LDX(1, 5); LDX(1, 6);
#undef LDX
  }
  ld8c(tag, 0u, ws);
  u32 bias1, bias2, bias3;
  {
    const v4i sb1 = make_srd(b1, F_ * 2);
    const u32 bo = (u32)(r32 * 2);
    ld2(bias1, bo, sb1, (u32)(((s1q ? 0 : cg - 5) * 64 + grp * 32) * 2));
    ld2(bias2, bo, sb1, (u32)(((11 + cg) * 64 + grp * 32) * 2));
    ld2(bias3, bo, sb1, (u32)((27 * 64 + (xcd & 1) * 32) * 2));
  }
  const u32 gsw = (u32)((c8 ^ (((r8 >> 1) & 1) << 2)) * 16);
  u32 goff_q = (u32)(r8 * 2688) + gsw, goff_1 = (u32)(r8 * 3584) + gsw, goff_2 = (u32)(r8 * 896) + gsw;
  const int tq = (lane & 15) >> 2, tp = lane & 3, tg = lane >> 4;
  int trl = ((tg >> 1) * 8 + tq) * 128 + (((2 * (tg & 1) + (tp >> 1)) ^ (((tq >> 1) & 1) << 2)) << 4) + (tp & 1) * 8;
  const int tb1 = 4 * rt + m;
  u32 lo16 = (u32)(lane * 16);
  float gc1 = -0.1029432397f;
  asm volatile("" : "+v"(gc1));
  const int hd = 2 * xcd + (r & 1), tbq = 4 * (15 - (r >> 1)) + m;
  const int c0 = (28 * hd) >> 3;
  const bool odd = hd & 1;
  u32 fo_qk, fo_v;
  {
    const int n = lane >> 1;
    int kbl = tbq - (grp == 0 ? (n == 0 ? 0 : 4 + n) : 1 + n);
    kbl = (kbl < 0 || n >= 5 - grp) ? tbq : kbl;
    const int fbl = (8 * c0 + (lane & 1) * 31) >> 5;
    fo_v = (u32)(WS_FQ + (kbl * 48 + fbl + 28) * 8);
    fo_qk = (u32)(WS_FQ + (kbl * 48 + fbl + (n < 5 ? 14 : 0)) * 8);
  }

  u32 vkeep = r32 == (odd ? 0 : 28) ? 0u : 1u;
  u32 vbase = vkeep ? (u32)(WS_VT + hh * 7168 + (8 * c0 + r32) * 16) : (u32)(WS_ONE + b * 128);
  asm volatile("" : "+v"(vkeep), "+v"(vbase));
  u32 xb[4];
#pragma unroll
  for (int j = 0; j < 4; ++j) xb[j] = (u32)(65536 - grp * 16384 + m * 4096 + (2 * j + hh) * 512 + ((r32 ^ (2 * j + hh)) << 4));
  asm volatile("" : "+v"(goff_q), "+v"(goff_1), "+v"(goff_2), "+v"(trl), "+v"(lo16), "+v"(fo_qk), "+v"(fo_v));
  asm volatile("" : "+v"(xb[0]), "+v"(xb[1]), "+v"(xb[2]), "+v"(xb[3]));
  {
    u32 sel = ((lane & 15) == 4 * (lane >> 4)) ? 0x3f803f80u : 0u;
    v4u asum = {sel, sel, sel, sel};
    const int l3 = lane & 3;
    u32 dm0 = l3 == 0 ? 0xffffu : (l3 == 1 ? 0xffff0000u : 0u), dm1 = l3 == 2 ? 0xffffu : (l3 == 3 ? 0xffff0000u : 0u);
    u32 dsto[2][2];
#pragma unroll
    for (int g = 0; g < 2; ++g) {
      dsto[g][0] = (u32)(49152 + (w >> 1) * 4096 + c8 * 512 + (((16 * (w & 1) + 8 * g + r8) ^ c8) << 4));
      dsto[g][1] = dsto[g][0] + 65536u;
    }
    asm volatile("" : "+v"(asum), "+v"(dm0), "+v"(dm1), "+v"(dsto[0][0]), "+v"(dsto[0][1]), "+v"(dsto[1][0]), "+v"(dsto[1][1]));
#pragma unroll
    for (int g = 0; g < 2; ++g) {
      if (g == 0) vm_wait<11>(); else vm_wait<4>();
#pragma unroll
      for (int i = 0; i < 7; ++i) pin(xr[g * 7 + i]);
      if (g == 0) STAMP(1); else STAMP(2);
      f32x4 s1 = {0.f, 0.f, 0.f, 0.f}, qa = s1, qb = s1;
#pragma unroll
      for (int i = 0; i < 7; ++i) {
        const v4u d = xr[g * 7 + i];
        const s16x4 lo = __builtin_bit_cast(s16x4, v2u{d[0], d[1]}), hi = __builtin_bit_cast(s16x4, v2u{d[2], d[3]});
        s1 = __builtin_amdgcn_mfma_f32_16x16x32_bf16(__builtin_bit_cast(bf16x8, asum), __builtin_bit_cast(bf16x8, d), s1, 0, 0, 0);
        qa = __builtin_amdgcn_mfma_f32_4x4x4bf16_1k(lo, lo, qa, 0, 0, 0);
        qb = __builtin_amdgcn_mfma_f32_4x4x4bf16_1k(hi, hi, qb, 0, 0, 0);
      }
      asm volatile("" : "+v"(s1), "+v"(qa), "+v"(qb));
      qa += qb;
      const float q01 = (l3 & 1) ? qa[1] : qa[0], q23 = (l3 & 1) ? qa[3] : qa[2];
      const float mean = red8(s1[0]) * (1.f / 448.f);
      const float ex2 = red8((l3 & 2) ? q23 : q01) * (1.f / 448.f);
      const float var = __builtin_fmaxf(__builtin_fmaf(-mean, mean, ex2), 0.f);
      const float rstd = __builtin_amdgcn_rsqf(var + 1e-5f);
      const u32 rr = pk(rstd, rstd);
      const float nc = -mean * __builtin_bit_cast(float, rr << 16);
      const f32x4 cc = {nc, nc, nc, nc};
      const s16x4 an = __builtin_bit_cast(s16x4, v2u{rr & dm0, rr & dm1});
#pragma unroll
      for (int i = 0; i < 7; ++i) {
        const v4u d = xr[g * 7 + i];
        const f32x4 y0 = __builtin_amdgcn_mfma_f32_4x4x4bf16_1k(an, __builtin_bit_cast(s16x4, v2u{d[0], d[1]}), cc, 0, 0, 0);
        const f32x4 y1 = __builtin_amdgcn_mfma_f32_4x4x4bf16_1k(an, __builtin_bit_cast(s16x4, v2u{d[2], d[3]}), cc, 0, 0, 0);
        *(LAS v4u*)(L + (i == 6 ? dsto[g][0] : (i < 3 ? dsto[g][0] + 16384 * (i + 1) : dsto[g][1] + 16384 * (i - 3)))) = v4u{pk(y0[0], y0[1]), pk(y0[2], y0[3]), pk(y1[0], y1[1]), pk(y1[2], y1[3])};
      }
    }
  }
  STAMP(3);
  if (grp == 0) {
#pragma unroll 1
    for (int i = 0; i < 3; ++i)
      load_pieces(lds0 + (u32)(16384 * i + 4096 * m), goff_q, sqkv, (u32)((128 * i + 32 * m) * 2688 + cg * 128), 1024u, 8u * 2688u, 4);
  }
  if (threadIdx.x == 256) st16c<0>(v4u{0x3f803f80u, 0x3f803f80u, 0x3f803f80u, 0x3f803f80u}, 0u, ws, (u32)(WS_ONE + b * 128));
  Stream g;
  wg_barrier();
  STAMP(4);
  v4u xa[28];
  const int xlast = grp == 0 ? -16384 : 6 * 16384;
#define RDX(s) xa[s] = *(LAS v4u*)(L + xb[(s) & 3] + (((s) >> 2) == 6 ? xlast : 16384 * ((s) >> 2)))
#pragma unroll
  for (int s = 0; s < 4; ++s) RDX(s);
  const u32 st1 = s1q ? 2688u : 3584u;
  const int strip1 = s1q ? 16 + cg : cg - 5;
  g.srd = s1q ? sqkv : sw1; g.voff = s1q ? goff_q : goff_1; g.s8 = 8 * st1;
  if (grp == 1) {
    vm_wait<0>();
    wg_barrier();
    load_cells(lds0 + (u32)(57344 + m * 2048), g.voff, g.srd, (u32)(m * 16) * st1 + (u32)(strip1 * 128), st1, 1);
  }
#pragma unroll
  for (int s = 4; s < 16; ++s) RDX(s);
  if (grp == 1) asm volatile("s_waitcnt lgkmcnt(0)" ::: "memory");
#pragma unroll
  for (int s = 16; s < 28; ++s) RDX(s);
#undef RDX
  STAMP(5);
  const bool grpb = grp == 1;
  u32 q1 = (u32)((trl ^ (grp * 64)) + (grpb ? 49152 : 0)), q2 = (u32)((trl ^ (grp * 64)) + (grpb ? 0 : 8192)),
      q3 = (u32)((trl ^ (grp * 64)) + (grpb ? 40960 : 49152));
  u32 vst = (u32)(hh * 7168 + r32 * 16);
  Stream g0;
  g0.srd = sw1; g0.voff = goff_1; g0.s8 = 8 * 3584; g0.lds = lds0 + (u32)(114688 + 4096 * m);
  g0.soff = (u32)(32 * m * 3584 + (11 + cg) * 128); g0.n = grpb ? 12 : 0;
  asm volatile("" : "+v"(q1), "+v"(q2), "+v"(q3), "+v"(vst));
  keep_s(g0.lds); keep_s(g0.soff); keep_s(g0.n);
  if (!grpb) {
    vm_wait<0>();
    STAMP(6);
    wg_barrier();
    load_cells(lds0 + (u32)(49152 + m * 2048), goff_q, sqkv, (u32)((384 + m * 16) * 2688 + cg * 128), 2688u, 1);
  } else {
    g.soff = (u32)(64 + 32 * m) * st1 + (u32)(strip1 * 128); g.lds = lds0 + (u32)(65536 + 4096 * m); g.n = 12;
    stream_run<0, 6>(g);
    STAMP(6);
    wg_barrier();
    stream_run<6, 12>(g);
  }
  pin(tag); pin(bias1); pin(bias2); pin(bias3);
  STAMP(8);
  f32x16 acc;
  v2u fpoll;
  u32 pend;
  bool pending;
  int blk;
  if (cg >= 14) {
    acc = gemm1_block<0, true, false>(xa, L + q1, L + q2, L + q3, 0u, g0, grpb, false, false, 0u, tag, ws);
    STAMP(9);
    store_v(acc, vst, ws, (u32)(WS_VT + tb1 * VT_TB + (2 * cg + grp - 28) * 512));
  } else {
    acc = gemm1_block<0, false, false>(xa, L + q1, L + q2, L + q3, 0u, g0, grpb, false, false, 0u, tag, ws);
    STAMP(9);
    v4u o0, o1;
    pack_tile(acc, o0, o1);
    const u32 so = (u32)(WS_QK + tb1 * QK_TB + (2 * cg + grp) * 2048);
    st16c<0>(o0, lo16, ws, so);
    st16c<1024>(o1, lo16, ws, so);
  }
  { const u64 t = (((u64)tag[1] << 32) | tag[0]) + 1; tag[0] = (u32)t; tag[1] = (u32)(t >> 32); }
  pend = (u32)(WS_FQ + (tb1 * 48 + 2 * cg + grp) * 8);
  const u32 st2 = heavy ? 3584u : 896u;
  g.srd = heavy ? sw1 : sw2; g.voff = heavy ? goff_1 : goff_2; g.s8 = 8 * st2;
  const u32 so2 = (u32)(32 * m) * st2 + (u32)((heavy ? 27 : ct) * 128);
  g.soff = so2 + (u32)(grp * 128) * st2; g.lds = lds0 + (u32)(grp * 16384 + 4096 * m); g.n = 4;
  {
    u32 p1 = q1 + 57344u, p2 = q2 + 57344u, p3 = q3 + 57344u;
    u32 tl = lds0 + (u32)(49152 + m * 2048), ts = (u32)((384 + m * 16) * 3584 + (11 + cg) * 128);
    asm volatile("" : "+v"(p1), "+v"(p2), "+v"(p3), "+v"(tag), "+v"(g.voff));
    keep_s(g.lds); keep_s(g.soff); keep_s(g.s8); keep_s(pend); keep_s(tl); keep_s(ts);
    STAMP(10);
    if (!grpb) {
      wg_barrier();
      load_cells(tl, goff_1, sw1, ts, 3584u, 1);
    }
    STAMP(12);
    if (s1q) {
      acc = gemm1_block<1, true, false>(xa, L + p1, L + p2, L + p3, 0u, g, grpb, true, false, pend, tag, ws);
      STAMP(13);
      store_v(acc, vst, ws, (u32)(WS_VT + tb1 * VT_TB + (2 * cg + grp + 4) * 512));
    } else {
      acc = gemm1_block<1, false, true>(xa, L + p1, L + p2, L + p3, bias1, g, grpb, true, false, pend, tag, ws);
      STAMP(13);
      store_gelu(acc, gc1, lo16, ws, (u32)(WS_HH + tb1 * HH_TB + (2 * cg - 10 + grp) * 2048));
    }
  }
  pend = (u32)((s1q ? WS_FQ + (tb1 * 48 + 32) * 8 : WS_FH + (tb1 * 64 - 10) * 8) + (2 * cg + grp) * 8);
  {
    g.soff = so2 + (grpb ? 256u : 384u) * st2; g.lds = lds0 + (u32)((grpb ? 4 : 7) * 8192 + 4096 * m); g.n = (grpb || heavy) ? 4 : 8;
    u32 p1 = q1 + (grpb ? 0u : 114688u), p2 = q2 + 114688u, p3 = q3 + (grpb ? 114688u : 0u);
    asm volatile("" : "+v"(p1), "+v"(p2), "+v"(p3));
    keep_s(g.lds); keep_s(g.soff); keep_s(g.n); keep_s(pend);
    STAMP(14);
    if (!grpb) wg_barrier();
    STAMP(16);
    u64 xst[5];
    acc = gemm1_block<2, false, true>(xa, L + p1, L + p2, L + p3, bias2, g, grpb, s1q, heavy, pend, tag, ws, xst);
    STAMP(17);
    pending = !s1q;
    blk = 22 + 2 * cg + grp;
  }
  if (heavy && !grpb) {
    vm_wait<0>();
    post_flag(pend, tag, ws);
    store_gelu(acc, gc1, lo16, ws, (u32)(WS_HH + tb1 * HH_TB + blk * 2048));
    pend = (u32)(WS_FH + (tb1 * 64 + blk) * 8);
    blk = 54 + (xcd & 1);
    const u32 t = (u32)(trl ^ ((xcd & 1) * 64));
    wg_barrier();
    STAMP(20);
    acc = gemm1_block<3, false, true>(xa, L + t, L + t + 8192, L + t + 57344, bias3, g, false, false, false, pend, tag, ws);
    STAMP(21);
  }

  {
    int kb[5];
#pragma unroll
    for (int n = 0; n < 5; ++n) kb[n] = tbq - (grp == 0 ? (n == 0 ? 0 : 4 + n) : 1 + n);
    v4u qf[2], kf[5][2], vf[5][2];
    {
      ld8c(fpoll, fo_qk, ws);
      u32 p[8];
      mfma_settle(acc);
      gelu4(p[0], p[1], acc[0], acc[1], acc[2], acc[3], gc1);
      gelu4(p[2], p[3], acc[4], acc[5], acc[6], acc[7], gc1);
      STAMP(24);
      vm_wait<0>();
      pin(fpoll);
      if (pending) post_flag(pend, tag, ws);
      if (!flags_ok(fpoll, lane < 12, tag)) wait_flags(fo_qk, lane < 12, tag, ws);
      STAMP(25);
      const u32 kbase = (u32)(WS_QK + (56 + c0) * 512);
      ld16c<0>(qf[0], lo16, ws, (u32)(WS_QK + tbq * QK_TB + c0 * 512));
      ld16c<1024>(qf[1], lo16, ws, (u32)(WS_QK + tbq * QK_TB + c0 * 512));
#define LDK(n) { const u32 so_ = kbase + (u32)((kb[n] < 0 ? tbq : kb[n]) * QK_TB); ld16c<0>(kf[n][0], lo16, ws, so_); ld16c<1024>(kf[n][1], lo16, ws, so_); }
      LDK(0)
      gelu4(p[4], p[5], acc[8], acc[9], acc[10], acc[11], gc1);
      LDK(1) LDK(2)
      gelu4(p[6], p[7], acc[12], acc[13], acc[14], acc[15], gc1);
      LDK(3)
      if (grp == 0) LDK(4)
#undef LDK
      v4u o0, o1;
      swap_tile(p, o0, o1);
      const u32 so = (u32)(WS_HH + tb1 * HH_TB + blk * 2048);
      st16c<0>(o0, lo16, ws, so);
      st16c<1024>(o1, lo16, ws, so);
      pend = (u32)(WS_FH + (tb1 * 64 + blk) * 8);
    }
    STAMP(26);
    f32x16 md, mw;
    {
      const u32 ninf = grp == 0 ? 0xff800000u : 0u;
      const int dd = r32 - 4 * hh;
#pragma unroll
      for (int i = 0; i < 16; ++i) {
        const u32 t = (u32)((dd - ((i & 3) + 8 * (i >> 2))) >> 31) & ninf;
        md[i] = __builtin_bit_cast(float, t);
        mw[i] = __builtin_bit_cast(float, t ^ 0xff800000u);
      }
    }
    asm volatile("" : "+v"(md), "+v"(mw));
    if (grp == 0 && !heavy) wg_barrier();
    ld8c(fpoll, fo_v, ws);
    STAMP(32);
    CSTAMP(61);
    vm_wait<3>();
    pin(qf[0]); pin(qf[1]);
#pragma unroll
    for (int n = 0; n < 5; ++n) { pin(kf[n][0]); pin(kf[n][1]); }
    STAMP(33);
    {
      const u32 k0 = (odd && hh == 0) ? 0u : ~0u, k1 = (!odd && hh == 1) ? 0u : ~0u;
      qf[0][0] &= k0; qf[0][1] &= k0; qf[1][2] &= k1; qf[1][3] &= k1;
    }
    f32x16 sc[5];
    f32x16 zero;
#pragma unroll
    for (int i = 0; i < 16; ++i) zero[i] = 0.f;
    float mx = -1e30f;
#pragma unroll
    for (int n = 0; n < 4; ++n) {
      sc[n] = mfma(kf[n][0], qf[0], n == 0 ? md : zero); sc[n] = mfma(kf[n][1], qf[1], sc[n]);
      __builtin_amdgcn_sched_barrier(0);
    }
    if (grp == 0) { sc[4] = mfma(kf[4][0], qf[0], mw); sc[4] = mfma(kf[4][1], qf[1], sc[4]); }
    float gate[5];
#pragma unroll
    for (int n = 0; n < 5; ++n) gate[n] = kb[n] < 0 ? -__builtin_inff() : 0.f;
    mfma_settle(sc[0]);
    mx = __builtin_fmaxf(mx, max16(sc[0]) + gate[0]);
    mx = __builtin_fmaxf(mx, max16(sc[1]) + gate[1]);
    mx = __builtin_fmaxf(mx, max16(sc[2]) + gate[2]);
    mfma_settle(sc[3]);
    mx = __builtin_fmaxf(mx, max16(sc[3]) + gate[3]);
    if (grp == 0) { mfma_settle(sc[4]); mx = __builtin_fmaxf(mx, max16(sc[4]) + gate[4]); }
    mx = __builtin_fmaxf(mx, swap32(mx));
    float cs = 0.18898223650461362f * 1.4426950408889634f;
    asm volatile("" : "+v"(cs));
    const float nb = -mx * cs;
    float nbn[5];
#pragma unroll
    for (int n = 0; n < 5; ++n) nbn[n] = nb + gate[n];
    v4u pf[5][2];
    pf[0][0] = exp8(sc[0], 0, cs, nbn[0]); pf[0][1] = exp8(sc[0], 8, cs, nbn[0]);
    pf[1][0] = exp8(sc[1], 0, cs, nbn[1]); pf[1][1] = exp8(sc[1], 8, cs, nbn[1]);
    pin(pf[0][0]); pin(pf[0][1]); pin(pf[1][0]); pin(pf[1][1]);
    STAMP(34);
    vm_wait<0>();
    pin(fpoll);
    post_flag(pend, tag, ws);
    if (!flags_ok(fpoll, lane < 10, tag)) wait_flags(fo_v, lane < 10, tag, ws);
    STAMP(35);
    {
#define LDV(n) { const u32 so_ = (u32)((kb[n] < 0 ? tbq : kb[n]) * VT_TB); \
      ld16c<0>(vf[n][0], mad24(vkeep, so_, vbase), ws, 0u); ld16c<0>(vf[n][1], mad24(vkeep, so_ + 14336u, vbase), ws, 0u); \
      __builtin_amdgcn_sched_barrier(0); }
      LDV(0) LDV(1)
      pf[2][0] = exp8(sc[2], 0, cs, nbn[2]); pin(pf[2][0]);
      LDV(2)
      pf[2][1] = exp8(sc[2], 8, cs, nbn[2]); pin(pf[2][1]);
      LDV(3)
      pf[3][0] = exp8(sc[3], 0, cs, nbn[3]); pin(pf[3][0]);
      if (grp == 0) LDV(4)
      pf[3][1] = exp8(sc[3], 8, cs, nbn[3]); pin(pf[3][1]);
#undef LDV
    }
    if (grp == 0) { pf[4][0] = exp8(sc[4], 0, cs, nbn[4]); pf[4][1] = exp8(sc[4], 8, cs, nbn[4]); pin(pf[4][0]); pin(pf[4][1]); }
    STAMP(36);
    vm_wait<0>();
#pragma unroll
    for (int n = 0; n < 5; ++n) { pin(vf[n][0]); pin(vf[n][1]); }
    f32x16 o = zero;
#pragma unroll
    for (int n = 0; n < 4; ++n) { o = mfma(vf[n][0], pf[n][0], o); o = mfma(vf[n][1], pf[n][1], o); }
    LAS char* sp = L + 17 * 8192 + m * 4096 + lane * 16;
    LAS float* mp = (LAS float*)(L + 6 * 8192 + m * 256 + lane * 4);
    if (grp == 0) {
      o = mfma(vf[4][0], pf[4][0], o); o = mfma(vf[4][1], pf[4][1], o);
#pragma unroll
      for (int q = 0; q < 4; ++q) *(LAS f32x4*)(sp + q * 1024) = f32x4{o[4 * q], o[4 * q + 1], o[4 * q + 2], o[4 * q + 3]};
      *mp = nb;
    }
    STAMP(37);
    wg_barrier();
    STAMP(38);
    if (grp == 1) {
      const float na = *mp;
      const float nm = __builtin_fminf(na, nb);
      const float fa = __builtin_amdgcn_exp2f(nm - na), fb = __builtin_amdgcn_exp2f(nm - nb);
#pragma unroll
      for (int q = 0; q < 4; ++q) {
        const f32x4 t = *(LAS f32x4*)(sp + q * 1024);
#pragma unroll
        for (int j = 0; j < 4; ++j) o[4 * q + j] = o[4 * q + j] * fb + t[j] * fa;
      }
      const float lown = odd ? o[0] : o[12], loth = swap32(lown);
      const float rl = __builtin_amdgcn_rcpf((odd ? hh == 0 : hh == 1) ? lown : loth);
#pragma unroll
      for (int i = 0; i < 16; ++i) o[i] *= rl;
      v4u o0, o1;
      pack_tile(o, o0, o1);
      const u32 so = (u32)(WS_HH + tbq * HH_TB + (224 + c0) * 512);
      if (odd) {
        st16c<1024>(o1, lo16, ws, so);
        if (hh == 1) st16c<0>(o0, lo16, ws, so); else st8c<8>(v2u{o0[2], o0[3]}, lo16, ws, so);
      } else {
        st16c<0>(o0, lo16, ws, so);
        if (hh == 0) st16c<1024>(o1, lo16, ws, so); else st8c<1024>(v2u{o1[0], o1[1]}, lo16, ws, so);
      }
    }
    pend = (u32)(WS_FO + (tbq * 16 + hd) * 8);
    STAMP(39);
  }

  if (heavy) {
    if (grp == 1) { vm_wait<0>(); post_flag(pend, tag, ws); }
    if (b == 255 && w == 0) {
#pragma unroll 1
      for (u32 spins = 0; spins < SPIN_CAP; ++spins) {
        v4u f[8];
        ld16c<0>(f[0], lo16, ws, (u32)WS_FO); ld16c<1024>(f[1], lo16, ws, (u32)WS_FO);
        ld16c<2048>(f[2], lo16, ws, (u32)WS_FO); ld16c<3072>(f[3], lo16, ws, (u32)WS_FO);
        ld16c<0>(f[4], lo16, ws, (u32)(WS_FO + 4096)); ld16c<1024>(f[5], lo16, ws, (u32)(WS_FO + 4096));
        ld16c<2048>(f[6], lo16, ws, (u32)(WS_FO + 4096)); ld16c<3072>(f[7], lo16, ws, (u32)(WS_FO + 4096));
        vm_wait<0>();
        bool ok = true;
#pragma unroll
        for (int i = 0; i < 8; ++i) { pin(f[i]); ok = ok && f[i][0] == tag[0] && f[i][1] == tag[1] && f[i][2] == tag[0] && f[i][3] == tag[1]; }
        if (__all(ok)) break;
        __builtin_amdgcn_s_sleep(8);
      }
      post_flag((u32)WS_E, tag, ws);
    }
    STAMP_OUT();
    return;
  }

  {
    const int kq = m, mh = grp, tb = 2 * tt + mh;
    const __attribute__((address_space(4))) u64* ka = (const __attribute__((address_space(4))) u64*)__builtin_amdgcn_kernarg_segment_ptr();
    asm volatile("" : "+s"(ka));
    const v4i sx2 = make_srd((const void*)ka[0], S_ * D_ * 2), swo = make_srd((const void*)ka[2], D_ * D_ * 2);
    const v4i sb2 = make_srd((const void*)ka[6], D_ * 2), sout = make_srd((const void*)ka[7], S_ * D_ * 2);
    auto cell_of = [&](int pos) { const int i = 4 * pos + kq; return i < 6 ? i : (i < 19 ? i + 1 : 6); };
    const u32 fo_h = (u32)(WS_FH + (tb * 64 + ((lane >> 1) * 4 + kq) * 2 + (lane & 1)) * 8);
    ld8c(fpoll, fo_h, ws);
    load_pieces(lds0 + (u32)(11 * 8192 + w * 1024), goff_2, sw2, (u32)((640 + w * 8) * 896 + ct * 128), 8192u, 64u * 896u, 6);
    v4u xres, bres;
    const u32 oo = (u32)((tb * 32 + r32) * 896 + (ct * 64 + kq * 16 + hh * 8) * 2);
    ld16<0>(xres, oo, sx2, 0u);
    ld16<0>(bres, (u32)(hh * 16), sb2, (u32)((ct * 64 + kq * 16) * 2));
    vm_wait<8>();
    pin(fpoll);
    if (grp == 1) post_flag(pend, tag, ws);
    if (!flags_ok(fpoll, lane < 14, tag)) wait_flags(fo_h, lane < 14, tag, ws);
    STAMP(40);
    v4u hf[35];
    const u32 hb = (u32)(WS_HH + tb * HH_TB + kq * 4096);
#define LDH1(j, s) ld16c<(s) * 1024>(hf[4 * (j) + (s)], lo16, ws, (j) < 8 ? hb + (j) * 16384 : hb + 8 * 16384 - kq * 1024)
#pragma unroll
    for (int j = 0; j < 2; ++j) { LDH1(j, 0); LDH1(j, 1); LDH1(j, 2); LDH1(j, 3); }
    f32x16 acc0, acc1;
#pragma unroll
    for (int i = 0; i < 16; ++i) { acc0[i] = 0.f; acc1[i] = 0.f; }
    const u32 fo_o = (u32)(WS_FO + (tb * 16 + (lane & 15)) * 8);
#define STAGE(j, WAIT) { \
      constexpr int ns = (j) == 8 ? 3 : 4; \
      vm_wait<WAIT>(); \
      pin(hf[4 * (j)]); pin(hf[4 * (j) + 1]); pin(hf[4 * (j) + 2]); if (ns == 4) pin(hf[4 * (j) + (ns - 1)]); \
      if ((j) == 5) { pin(fpoll); if (!flags_ok(fpoll, true, tag)) wait_flags(fo_o, true, tag, ws); } \
      wg_barrier(); \
      STAMP(41 + (j)); \
      LAS char* p0 = L + cell_of((j) % 5) * 8192 + trl; \
      LAS char* p1 = L + cell_of((j) % 5) * 8192 + (trl ^ 64); \
      v4u wa[4], wb[4]; \
      wa[0] = tr_frag(p0); wb[0] = tr_frag(p1); wa[1] = tr_frag(p0 + 2048); wb[1] = tr_frag(p1 + 2048); \
      wa[2] = tr_frag(p0 + 4096); wb[2] = tr_frag(p1 + 4096); \
      if (ns == 4) { wa[3] = tr_frag(p0 + 6144); wb[3] = tr_frag(p1 + 6144); } \
      u32 gl = lds0 + (u32)(cell_of(((j) + 4) % 5) * 8192 + mh * ((j) == 4 ? 3072 : 4096)); \
      u32 gs = (u32)(((j) < 3 ? 256 * ((j) + 4) + 64 * kq + 32 * mh : ((j) == 3 ? 64 * kq + 32 * mh : 256 + 48 * kq + 24 * mh)) * 896 + ct * 128); \
      __builtin_amdgcn_sched_barrier(0); \
      if ((j) <= 6) LDH1(((j) <= 6 ? (j) + 2 : 0), 0); \
      acc0 = mfma(wa[0], hf[4 * (j)], acc0); __builtin_amdgcn_sched_barrier(0); \
      if ((j) <= 6) LDH1(((j) <= 6 ? (j) + 2 : 0), 1); \
      acc1 = mfma(wb[0], hf[4 * (j)], acc1); __builtin_amdgcn_sched_barrier(0); \
      if ((j) <= 6) LDH1(((j) <= 6 ? (j) + 2 : 0), 2); \
      acc0 = mfma(wa[1], hf[4 * (j) + 1], acc0); __builtin_amdgcn_sched_barrier(0); \
      if ((j) <= 5) LDH1(((j) <= 5 ? (j) + 2 : 0), 3); \
      acc1 = mfma(wb[1], hf[4 * (j) + 1], acc1); __builtin_amdgcn_sched_barrier(0); \
      if ((j) <= 4) glds16(gl, goff_2, (j) < 3 ? sw2 : swo, gs); \
      acc0 = mfma(wa[2], hf[4 * (j) + 2], acc0); __builtin_amdgcn_sched_barrier(0); \
      if ((j) <= 4) glds16(gl + 1024, goff_2, (j) < 3 ? sw2 : swo, gs + 7168); \
      acc1 = mfma(wb[2], hf[4 * (j) + 2], acc1); __builtin_amdgcn_sched_barrier(0); \
      if ((j) <= 4) glds16(gl + 2048, goff_2, (j) < 3 ? sw2 : swo, gs + 14336); \
      if (ns == 4) { acc0 = mfma(wa[3], hf[4 * (j) + (ns - 1)], acc0); __builtin_amdgcn_sched_barrier(0); } \
      if ((j) <= 3) glds16(gl + 3072, goff_2, (j) < 3 ? sw2 : swo, gs + 21504); \
      if ((j) == 3) ld8c(fpoll, fo_o, ws); \
      if (ns == 4) { acc1 = mfma(wb[3], hf[4 * (j) + (ns - 1)], acc1); __builtin_amdgcn_sched_barrier(0); } \
    }
    STAGE(0, 4) STAGE(1, 8) STAGE(2, 12) STAGE(3, 12) STAGE(4, 13) STAGE(5, 7) STAGE(6, 7) STAGE(7, 3) STAGE(8, 0)
#undef STAGE
#undef LDH1
    STAMP(50);
    {
      LAS char* rp = L + (w < 6 ? w : w + 1) * 8192 + lane * 16;
#pragma unroll
      for (int q = 0; q < 4; ++q) {
        *(LAS f32x4*)(rp + q * 1024) = f32x4{acc0[4 * q], acc0[4 * q + 1], acc0[4 * q + 2], acc0[4 * q + 3]};
        *(LAS f32x4*)(rp + 4096 + q * 1024) = f32x4{acc1[4 * q], acc1[4 * q + 1], acc1[4 * q + 2], acc1[4 * q + 3]};
      }
    }
    wg_barrier();
    STAMP(51);
    pin(xres); pin(bres);
    f32x4 a = {0.f, 0.f, 0.f, 0.f}, c = {0.f, 0.f, 0.f, 0.f};
#pragma unroll
    for (int k = 0; k < 4; ++k) {
      const int ww = 4 * mh + k;
      LAS char* rp = L + (ww < 6 ? ww : ww + 1) * 8192 + kq * 2048 + hh * 1024 + r32 * 16;
      a += *(LAS f32x4*)rp;
      c += *(LAS f32x4*)(rp + 512);
    }
    auto lo_f = [](u32 d) { return __builtin_bit_cast(float, d << 16); };
    auto hi_f = [](u32 d) { return __builtin_bit_cast(float, d & 0xffff0000u); };
    v4u o;
    o[0] = pk(a[0] + lo_f(xres[0]) + lo_f(bres[0]), a[1] + hi_f(xres[0]) + hi_f(bres[0]));
    o[1] = pk(a[2] + lo_f(xres[1]) + lo_f(bres[1]), a[3] + hi_f(xres[1]) + hi_f(bres[1]));
    o[2] = pk(c[0] + lo_f(xres[2]) + lo_f(bres[2]), c[1] + hi_f(xres[2]) + hi_f(bres[2]));
    o[3] = pk(c[2] + lo_f(xres[3]) + lo_f(bres[3]), c[3] + hi_f(xres[3]) + hi_f(bres[3]));
    st16(o, oo, sout);
    STAMP(52);
    CSTAMP(62);
    STAMP_OUT();
  }
}

extern "C" void kernel_launch(void* const* d_in, const int* in_sizes, int n_in, void* d_out, int out_size, void* d_ws, size_t ws_size,
                              hipStream_t stream) {
  (void)hipFuncSetAttribute((const void*)fused_block, hipFuncAttributeMaxDynamicSharedMemorySize, LDS_BYTES);
  fused_block<<<256, 512, LDS_BYTES, stream>>>((const u16*)d_in[0], (const u16*)d_in[1], (const u16*)d_in[2], (const u16*)d_in[4],
                                               (const u16*)d_in[5], (const u16*)d_in[6], (const u16*)d_in[7], (u16*)d_out, (char*)d_ws);
}
```

```cpp
#include <hip/hip_runtime.h>

typedef unsigned short u16;
typedef unsigned int u32;
typedef unsigned long long u64;
typedef u32 v4u __attribute__((ext_vector_type(4)));
typedef u32 v2u __attribute__((ext_vector_type(2)));
typedef int v4i __attribute__((ext_vector_type(4)));
typedef short s16x4 __attribute__((ext_vector_type(4)));
typedef __bf16 bf16x8 __attribute__((ext_vector_type(8)));
typedef __bf16 bf16x2 __attribute__((ext_vector_type(2)));
typedef float f32x2 __attribute__((ext_vector_type(2)));
typedef float f32x4 __attribute__((ext_vector_type(4)));
typedef float f32x16 __attribute__((ext_vector_type(16)));
#define GAS __attribute__((address_space(1)))
#define LAS __attribute__((address_space(3)))
#define DI __device__ __forceinline__

#define EXP_NOSTREAM 0
#define EXP_XST 0
#define STAMPS 0

constexpr int S_ = 2048, D_ = 448, F_ = 1792;
constexpr int WS_E = 0;
constexpr int WS_FQ = 4096;
constexpr int WS_FH = WS_FQ + 64 * 48 * 8;
constexpr int WS_FO = WS_FH + 64 * 64 * 8;
constexpr int WS_ONE = 1 << 19;
constexpr int WS_QK = 1 << 20;
constexpr int QK_TB = 112 * 512;
constexpr int WS_VT = WS_QK + 64 * QK_TB;
constexpr int VT_TB = 4 * 448 * 16;
constexpr int WS_HH = WS_VT + 64 * VT_TB;
constexpr int HH_TB = 280 * 512;
constexpr int WS_END = WS_HH + 64 * HH_TB;
constexpr int WS_DBG = 32 << 20;
constexpr int LDS_BYTES = 163840;
constexpr u32 SPIN_CAP = 1u << 16;

DI v4i make_srd(const void* p, u32 bytes) {
  const u64 a = (u64)p;
  v4i r;
  r[0] = (int)(u32)a; r[1] = (int)((u32)(a >> 32) & 0xffffu); r[2] = (int)bytes; r[3] = 0x00020000;
  return r;
}
template <int OFF> DI void ld16(v4u& d, u32 voff, v4i srd, u32 soff) {
  asm volatile("buffer_load_dwordx4 %0, %1, %2, %3 offen offset:%4" : "=v"(d) : "v"(voff), "s"(srd), "s"(soff), "i"(OFF) : "memory");
}
template <int OFF> DI void ld16c(v4u& d, u32 voff, v4i srd, u32 soff) {
  asm volatile("buffer_load_dwordx4 %0, %1, %2, %3 offen offset:%4 sc1" : "=v"(d) : "v"(voff), "s"(srd), "s"(soff), "i"(OFF) : "memory");
}
DI void ld8c(v2u& d, u32 voff, v4i srd) {
  asm volatile("buffer_load_dwordx2 %0, %1, %2, 0 offen sc1" : "=v"(d) : "v"(voff), "s"(srd) : "memory");
}
DI void ld2(u32& d, u32 voff, v4i srd, u32 soff) {
  asm volatile("buffer_load_ushort %0, %1, %2, %3 offen" : "=v"(d) : "v"(voff), "s"(srd), "s"(soff) : "memory");
}
DI void glds16(u32 lds, u32 voff, v4i srd, u32 soff) {
  asm volatile("s_mov_b32 m0, %0\n\ts_nop 0\n\tbuffer_load_dwordx4 %1, %2, %3 offen lds" ::"s"(lds), "v"(voff), "s"(srd), "s"(soff) : "memory");
}
DI void load_cells(u32 lds, u32 voff, v4i srd, u32 soff, u32 stride, int n) {
#pragma unroll 1
  for (int c = 0; c < n; ++c) {
    u32 t;
    asm volatile("s_mov_b32 m0, %1\n\ts_add_u32 %0, %4, %5\n\tbuffer_load_dwordx4 %2, %3, %4 offen lds\n\t"
                 "s_add_u32 m0, %1, 0x400\n\ts_nop 0\n\tbuffer_load_dwordx4 %2, %3, %0 offen lds"
                 : "=&s"(t) : "s"(lds), "v"(voff), "s"(srd), "s"(soff), "s"(stride * 8) : "memory", "scc");
    lds += 8192; soff += stride * 64;
  }
}
DI void load_pieces(u32 lds, u32 voff, v4i srd, u32 soff, u32 lstep, u32 sstep, int n) {
#pragma unroll 1
  for (int c = 0; c < n; ++c) { glds16(lds, voff, srd, soff); lds += lstep; soff += sstep; }
}
template <int OFF> DI void st16c(v4u d, u32 voff, v4i srd, u32 soff) {
  asm volatile("buffer_store_dwordx4 %0, %1, %2, %3 offen offset:%4 sc1\n\ts_nop 1" ::"v"(d), "v"(voff), "s"(srd), "s"(soff), "i"(OFF) : "memory");
}
template <int OFF> DI void st8c(v2u d, u32 voff, v4i srd, u32 soff) {
  asm volatile("buffer_store_dwordx2 %0, %1, %2, %3 offen offset:%4 sc1" ::"v"(d), "v"(voff), "s"(srd), "s"(soff), "i"(OFF) : "memory");
}
DI void st16(v4u d, u32 voff, v4i srd) {
  asm volatile("buffer_store_dwordx4 %0, %1, %2, 0 offen\n\ts_nop 1" ::"v"(d), "v"(voff), "s"(srd) : "memory");
}
template <int N> DI void vm_wait() { asm volatile("s_waitcnt vmcnt(%0)" ::"i"(N) : "memory"); }
DI void pin(v4u& a) { asm volatile("" : "+v"(a)); }
DI void pin(v2u& a) { asm volatile("" : "+v"(a)); }
DI void pin(u32& a) { asm volatile("" : "+v"(a)); }
DI void keep_s(u32& a) { a = __builtin_amdgcn_readfirstlane(a); asm volatile("" : "+s"(a)); }
DI void wg_barrier() { asm volatile("s_waitcnt lgkmcnt(0)\n\ts_barrier" ::: "memory"); }
DI f32x16 mfma(v4u a, v4u b, f32x16 c) {
  return __builtin_amdgcn_mfma_f32_32x32x16_bf16(__builtin_bit_cast(bf16x8, a), __builtin_bit_cast(bf16x8, b), c, 0, 0, 0);
}
DI u32 pk(float a, float b) { f32x2 f = {a, b}; return __builtin_bit_cast(u32, __builtin_convertvector(f, bf16x2)); }
DI v4u tr_frag(LAS char* p) {
  const s16x4 a = __builtin_amdgcn_ds_read_tr16_b64_v4i16((LAS s16x4*)p);
  const s16x4 b = __builtin_amdgcn_ds_read_tr16_b64_v4i16((LAS s16x4*)(p + 512));
  const v2u x = __builtin_bit_cast(v2u, a), y = __builtin_bit_cast(v2u, b);
  return v4u{x[0], x[1], y[0], y[1]};
}
template <int C> DI float dpp_add(float x) {
  return x + __builtin_bit_cast(float, __builtin_amdgcn_update_dpp(0, __builtin_bit_cast(int, x), C, 0xf, 0xf, true));
}
DI float red8(float x) { x = dpp_add<0xB1>(x); x = dpp_add<0x4E>(x); return dpp_add<0x141>(x); }
DI float swap32(float x) {
  const u32 u = __builtin_bit_cast(u32, x);
  const auto r = __builtin_amdgcn_permlane32_swap(u, u, false, false);
  return __builtin_bit_cast(float, (threadIdx.x & 32) ? r[0] : r[1]);
}
DI void pack_tile(const f32x16& a, v4u& o0, v4u& o1) {
  u32 p[8];
#pragma unroll
  for (int i = 0; i < 8; ++i) p[i] = pk(a[2 * i], a[2 * i + 1]);
  const auto r0 = __builtin_amdgcn_permlane32_swap(p[0], p[2], false, false);
  const auto r1 = __builtin_amdgcn_permlane32_swap(p[1], p[3], false, false);
  const auto r2 = __builtin_amdgcn_permlane32_swap(p[4], p[6], false, false);
  const auto r3 = __builtin_amdgcn_permlane32_swap(p[5], p[7], false, false);
  o0 = v4u{r0[0], r1[0], r0[1], r1[1]};
  o1 = v4u{r2[0], r3[0], r2[1], r3[1]};
}
DI void wait_flags(u32 voff, bool active, v2u tag, v4i ws) {
#pragma unroll 1
  for (u32 spins = 0; spins < SPIN_CAP; ++spins) {
    v2u f;
    ld8c(f, voff, ws);
    vm_wait<0>();
    pin(f);
    const bool ok = !active || (f[0] == tag[0] && f[1] == tag[1]);
    if (__all(ok)) break;
    __builtin_amdgcn_s_sleep(2);
  }
}
DI void post_flag(u32 off, v2u tag, v4i ws) {
  asm volatile("s_mov_b64 exec, 1\n\tbuffer_store_dwordx2 %0, off, %1, %2 sc1\n\ts_mov_b64 exec, -1" ::"v"(tag), "s"(ws), "s"(off) : "memory");
}
#define STAMP(i) do {} while (0)
#define STAMP_DECL
#define XSTAMP(i) do {} while (0)
#define CSTAMP(i) do {} while (0)
#define STAMP_OUT() do {} while (0)

struct Stream { v4i srd; u32 voff, soff, s8, lds, n; };
template <int P, bool COND> DI void stream_piece(const Stream& g) {
  u32 t;
  if constexpr (COND)
    asm volatile("s_cmp_gt_u32 %6, %9\n\ts_cbranch_scc0 1f\n\ts_mul_i32 %0, %5, %8\n\ts_add_u32 m0, %1, %7\n\ts_add_u32 %0, %0, %2\n\t"
                 "buffer_load_dwordx4 %3, %4, %0 offen lds\n1:"
                 : "=&s"(t) : "s"(g.lds), "s"(g.soff), "v"(g.voff), "s"(g.srd), "s"(g.s8), "s"(g.n),
                   "i"((P >> 2) * 16384 + (P & 3) * 1024), "i"((P >> 2) * 16 + (P & 3)), "i"(P) : "memory", "scc");
  else
    asm volatile("s_mul_i32 %0, %5, %7\n\ts_add_u32 m0, %1, %6\n\ts_add_u32 %0, %0, %2\n\tbuffer_load_dwordx4 %3, %4, %0 offen lds"
                 : "=&s"(t) : "s"(g.lds), "s"(g.soff), "v"(g.voff), "s"(g.srd), "s"(g.s8),
                   "i"((P >> 2) * 16384 + (P & 3) * 1024), "i"((P >> 2) * 16 + (P & 3)) : "memory", "scc");
}
template <int LO, int HI> DI void stream_run(const Stream& g) {
  if constexpr (LO < HI) { stream_piece<LO, false>(g); stream_run<LO + 1, HI>(g); }
}
DI u32 mad24(u32 a, u32 s, u32 c) { u32 d; asm("v_mad_u32_u24 %0, %1, %2, %3" : "=v"(d) : "v"(a), "s"(s), "v"(c)); return d; }
DI bool flags_ok(v2u f, bool active, v2u tag) { return __all(!active || (f[0] == tag[0] && f[1] == tag[1])); }

template <int SLOT, bool SWAP, bool BIAS>
DI f32x16 gemm1_block(const v4u (&xa)[28], LAS char* p1, LAS char* p2, LAS char* p3, u32 bias, const Stream& g, bool grpb, bool post, bool heavy,
                      u32 pend, v2u tag, v4i ws, u64* xst = nullptr) {
  constexpr int PD = 5;
  constexpr int NP = SLOT == 0 ? 12 : (SLOT == 1 ? 4 : (SLOT == 2 ? 8 : 0)), NU = SLOT == 0 ? 0 : 4;
  auto rd = [&](int s) { return s < 4 ? tr_frag(p1 + s * 2048) : (s < 24 ? tr_frag(p2 + (s - 4) * 2048) : tr_frag(p3 + (s - 24) * 2048)); };
  __builtin_amdgcn_s_setprio(1);
  v4u wf[PD];
#pragma unroll
  for (int s = 0; s < PD; ++s) wf[s] = rd(s);
  f32x16 acc;
#pragma unroll
  for (int i = 0; i < 16; ++i) acc[i] = 0.f;
  if constexpr (BIAS) {
    const v4u bf = {bias, 0u, 0u, 0u};
    const v4u one = {(threadIdx.x & 32) ? 0u : 0x3f80u, 0u, 0u, 0u};
    acc = mfma(bf, one, acc);
  }
  __builtin_amdgcn_sched_barrier(0);
#define G1STEP(s) { \
    const v4u cur = wf[(s) % PD]; \
    if ((s) == 19) { \
      if (SLOT == 0) { if (!grpb) { vm_wait<0>(); asm volatile("s_barrier" ::: "memory"); } } \
      else if (SLOT == 1) { vm_wait<4>(); post_flag(pend, tag, ws); } \
      else if (SLOT == 2) { if (post) { vm_wait<4>(); post_flag(pend, tag, ws); } } \
    } \
    if ((s) == 24 && SLOT < 3) { \
      if (grpb) { \
        if (SLOT == 0) vm_wait<12>(); \
        if (SLOT == 2) { if (heavy) vm_wait<0>(); } \
        asm volatile("s_barrier" ::: "memory"); \
        __builtin_amdgcn_s_setprio(2); \
      } \
    } \
    acc = SWAP ? mfma(xa[s], cur, acc) : mfma(cur, xa[s], acc); \
    if ((s) + PD < 28) wf[(s) % PD] = rd((s) + PD); \
    if (((s) & 1) == 0 && (s) / 2 < NP && !(EXP_NOSTREAM && SLOT > 0)) stream_piece<(s) / 2, ((s) / 2 >= NU)>(g); \
    if (SLOT == 2 && EXP_XST && ((s) == 0 || (s) == 6 || (s) == 12 || (s) == 18 || (s) == 23)) XSTAMP(53 + ((s) + 1) / 6); \
    __builtin_amdgcn_sched_barrier(0); }
  G1STEP(0) G1STEP(1) G1STEP(2) G1STEP(3) G1STEP(4) G1STEP(5) G1STEP(6) G1STEP(7) G1STEP(8) G1STEP(9)
  G1STEP(10) G1STEP(11) G1STEP(12) G1STEP(13) G1STEP(14) G1STEP(15) G1STEP(16) G1STEP(17) G1STEP(18) G1STEP(19)
  G1STEP(20) G1STEP(21) G1STEP(22) G1STEP(23) G1STEP(24) G1STEP(25) G1STEP(26) G1STEP(27)
#undef G1STEP
  __builtin_amdgcn_s_setprio(0);
  return acc;
}

DI void gelu4(u32& o0, u32& o1, float a, float b, float c, float d, float c1) {
  float t0, t1, t2, t3;
  asm volatile(
      "v_mul_f32 %2, %6, %6\n\tv_mul_f32 %3, %7, %7\n\tv_mul_f32 %4, %8, %8\n\tv_mul_f32 %5, %9, %9\n\t"
      "v_fmaak_f32 %2, %2, %10, 0xc0135761\n\tv_fmaak_f32 %3, %3, %10, 0xc0135761\n\t"
      "v_fmaak_f32 %4, %4, %10, 0xc0135761\n\tv_fmaak_f32 %5, %5, %10, 0xc0135761\n\t"
      "v_mul_f32 %2, %2, %6\n\tv_mul_f32 %3, %3, %7\n\tv_mul_f32 %4, %4, %8\n\tv_mul_f32 %5, %5, %9\n\t"
      "v_exp_f32 %2, %2\n\tv_exp_f32 %3, %3\n\tv_exp_f32 %4, %4\n\tv_exp_f32 %5, %5\n\t"
      "v_add_f32 %2, 1.0, %2\n\tv_add_f32 %3, 1.0, %3\n\tv_add_f32 %4, 1.0, %4\n\tv_add_f32 %5, 1.0, %5\n\t"
      "v_rcp_f32 %2, %2\n\tv_rcp_f32 %3, %3\n\tv_rcp_f32 %4, %4\n\tv_rcp_f32 %5, %5\n\t"
      "v_mul_f32 %2, %2, %6\n\tv_mul_f32 %3, %3, %7\n\tv_mul_f32 %4, %4, %8\n\tv_mul_f32 %5, %5, %9\n\t"
      "v_cvt_pk_bf16_f32 %0, %2, %3\n\tv_cvt_pk_bf16_f32 %1, %4, %5\n\ts_nop 1"
      : "=&v"(o0), "=&v"(o1), "=&v"(t0), "=&v"(t1), "=&v"(t2), "=&v"(t3) : "v"(a), "v"(b), "v"(c), "v"(d), "v"(c1));
}
DI void swap_tile(const u32 (&p)[8], v4u& o0, v4u& o1) {
  const auto r0 = __builtin_amdgcn_permlane32_swap(p[0], p[2], false, false);
  const auto r1 = __builtin_amdgcn_permlane32_swap(p[1], p[3], false, false);
  const auto r2 = __builtin_amdgcn_permlane32_swap(p[4], p[6], false, false);
  const auto r3 = __builtin_amdgcn_permlane32_swap(p[5], p[7], false, false);
  o0 = v4u{r0[0], r1[0], r0[1], r1[1]};
  o1 = v4u{r2[0], r3[0], r2[1], r3[1]};
}
DI void mfma_settle(const f32x16& a) { asm volatile("s_nop 7\n\ts_nop 4" ::"v"(a)); }
DI void store_v(const f32x16& a, u32 vo, v4i ws, u32 so) {
  st16c<0>(v4u{pk(a[0], a[1]), pk(a[2], a[3]), pk(a[4], a[5]), pk(a[6], a[7])}, vo, ws, so);
  st16c<0>(v4u{pk(a[8], a[9]), pk(a[10], a[11]), pk(a[12], a[13]), pk(a[14], a[15])}, vo, ws, so + 14336u);
}
DI void store_gelu(const f32x16& a, float gc1, u32 lo16, v4i ws, u32 so) {
  u32 p[8];
  mfma_settle(a);
#pragma unroll
  for (int i = 0; i < 4; ++i) gelu4(p[2 * i], p[2 * i + 1], a[4 * i], a[4 * i + 1], a[4 * i + 2], a[4 * i + 3], gc1);
  v4u o0, o1;
  swap_tile(p, o0, o1);
  st16c<0>(o0, lo16, ws, so);
  st16c<1024>(o1, lo16, ws, so);
}
DI float max16(const f32x16& s) {
  float a, b;
  asm volatile("v_max3_f32 %0, %2, %3, %4\n\tv_max3_f32 %1, %5, %6, %7\n\tv_max3_f32 %0, %0, %8, %9\n\tv_max3_f32 %1, %1, %10, %11\n\t"
               "v_max3_f32 %0, %0, %12, %13\n\tv_max3_f32 %1, %1, %14, %15\n\tv_max3_f32 %0, %0, %16, %17\n\tv_max_f32 %0, %0, %1"
               : "=&v"(a), "=&v"(b) : "v"(s[0]), "v"(s[1]), "v"(s[2]), "v"(s[3]), "v"(s[4]), "v"(s[5]), "v"(s[6]), "v"(s[7]), "v"(s[8]),
                 "v"(s[9]), "v"(s[10]), "v"(s[11]), "v"(s[12]), "v"(s[13]), "v"(s[14]), "v"(s[15]));
  return a;
}
DI v4u exp8(const f32x16& s, int o, float cs, float nb) {
  v4u p;
#pragma unroll
  for (int j = 0; j < 4; ++j)
    p[j] = pk(__builtin_amdgcn_exp2f(__builtin_fmaf(s[o + 2 * j], cs, nb)), __builtin_amdgcn_exp2f(__builtin_fmaf(s[o + 2 * j + 1], cs, nb)));
  return p;
}

__global__ void __launch_bounds__(512, 2)
fused_block(const u16* __restrict__ x, const u16* __restrict__ wqkv, const u16* __restrict__ wo, const u16* __restrict__ w1,
            const u16* __restrict__ b1, const u16* __restrict__ w2, const u16* __restrict__ b2, u16* __restrict__ out, char* wsp) {
  extern __shared__ __attribute__((aligned(16))) char smem_[];
  LAS char* const L = (LAS char*)smem_;
  const u32 lds0 = (u32)(size_t)L;
  const int lane = threadIdx.x & 63;
  const int w = __builtin_amdgcn_readfirstlane(threadIdx.x >> 6);
  const int m = w & 3, grp = w >> 2;
  const int b = blockIdx.x, xcd = b & 7, r = b >> 3;
  const int r32 = lane & 31, hh = lane >> 5;
  const v4i ws = make_srd(wsp, WS_END);
  const v4i sx = make_srd(x, S_ * D_ * 2);
  const v4i sqkv = make_srd(wqkv, D_ * 3 * D_ * 2);
  const v4i sw1 = make_srd(w1, D_ * F_ * 2);
  const v4i sw2 = make_srd(w2, F_ * D_ * 2);
  STAMP_DECL;
  STAMP(0);
  CSTAMP(60);

  const int rt = 4 * (xcd >> 1) + (r & 3);
  const int cg = 8 * (xcd & 1) + (r >> 2);
  const bool heavy = r >= 28;
  const int tt = 4 * xcd + (r & 3), ct = r >> 2;
  const bool s1q = cg < 5;
  const int r8 = lane >> 3, c8 = lane & 7;

  v2u tag;
  v4u xr[14];
  {
    const u32 xo = (u32)((rt * 128 + 16 * w + r8) * 896 + c8 * 16);
#define LDX(g, i) ld16<(i) * 128>(xr[(g) * 7 + (i)], xo, sx, (u32)((g) * 7168))
    LDX(0, 0); LDX(0, 1); LDX(0, 2); LDX(0, 3); LDX(0, 4); LDX(0, 5); LDX(0, 6);
    LDX(1, 0); LDX(1, 1); LDX(1, 2); LDX(1, 3); LDX(1, 4); LDX(1, 5); LDX(1, 6);
#undef LDX
  }
  ld8c(tag, 0u, ws);
  u32 cpf = 0;
  {
    u64 pc; asm volatile("s_getpc_b64 %0" : "=s"(pc));
    const v4i sc = make_srd((const void*)(pc & ~127ull), 23000);
    asm volatile("buffer_load_dword %0, %1, %2, 0 offen" : "=v"(cpf) : "v"((u32)((m * 64 + lane) * 128)), "s"(sc) : "memory");
  }
  u32 bias1, bias2, bias3;
  {
    const v4i sb1 = make_srd(b1, F_ * 2);
    const u32 bo = (u32)(r32 * 2);
    ld2(bias1, bo, sb1, (u32)(((s1q ? 0 : cg - 5) * 64 + grp * 32) * 2));
    ld2(bias2, bo, sb1, (u32)(((11 + cg) * 64 + grp * 32) * 2));
    ld2(bias3, bo, sb1, (u32)((27 * 64 + (xcd & 1) * 32) * 2));
  }
  const u32 gsw = (u32)((c8 ^ (((r8 >> 1) & 1) << 2)) * 16);
  u32 goff_q = (u32)(r8 * 2688) + gsw, goff_1 = (u32)(r8 * 3584) + gsw, goff_2 = (u32)(r8 * 896) + gsw;
  const int tq = (lane & 15) >> 2, tp = lane & 3, tg = lane >> 4;
  int trl = ((tg >> 1) * 8 + tq) * 128 + (((2 * (tg & 1) + (tp >> 1)) ^ (((tq >> 1) & 1) << 2)) << 4) + (tp & 1) * 8;
  const int tb1 = 4 * rt + m;
  u32 lo16 = (u32)(lane * 16);
  float gc1 = -0.1029432397f;
  asm volatile("" : "+v"(gc1));
  const int hd = 2 * xcd + (r & 1), tbq = 4 * (15 - (r >> 1)) + m;
  const int c0 = (28 * hd) >> 3;
  const bool odd = hd & 1;
  u32 fo_qk, fo_v;
  {
    const int n = lane >> 1;
    int kbl = tbq - (grp == 0 ? (n == 0 ? 0 : 4 + n) : 1 + n);
    kbl = (kbl < 0 || n >= 5 - grp) ? tbq : kbl;
    const int fbl = (8 * c0 + (lane & 1) * 31) >> 5;
    fo_v = (u32)(WS_FQ + (kbl * 48 + fbl + 28) * 8);
    fo_qk = (u32)(WS_FQ + (kbl * 48 + fbl + (n < 5 ? 14 : 0)) * 8);
  }

  u32 vkeep = r32 == (odd ? 0 : 28) ? 0u : 1u;
  u32 vbase = vkeep ? (u32)(WS_VT + hh * 7168 + (8 * c0 + r32) * 16) : (u32)(WS_ONE + b * 128);
  asm volatile("" : "+v"(vkeep), "+v"(vbase));
  u32 xb[4];
#pragma unroll
  for (int j = 0; j < 4; ++j) xb[j] = (u32)(65536 - grp * 16384 + m * 4096 + (2 * j + hh) * 512 + ((r32 ^ (2 * j + hh)) << 4));
  asm volatile("" : "+v"(goff_q), "+v"(goff_1), "+v"(goff_2), "+v"(trl), "+v"(lo16), "+v"(fo_qk), "+v"(fo_v));
  asm volatile("" : "+v"(xb[0]), "+v"(xb[1]), "+v"(xb[2]), "+v"(xb[3]));
  {
    u32 sel = ((lane & 15) == 4 * (lane >> 4)) ? 0x3f803f80u : 0u;
    v4u asum = {sel, sel, sel, sel};
    const int l3 = lane & 3;
    u32 dm0 = l3 == 0 ? 0xffffu : (l3 == 1 ? 0xffff0000u : 0u), dm1 = l3 == 2 ? 0xffffu : (l3 == 3 ? 0xffff0000u : 0u);
    u32 dsto[2][2];
#pragma unroll
    for (int g = 0; g < 2; ++g) {
      dsto[g][0] = (u32)(49152 + (w >> 1) * 4096 + c8 * 512 + (((16 * (w & 1) + 8 * g + r8) ^ c8) << 4));
      dsto[g][1] = dsto[g][0] + 65536u;
    }
    asm volatile("" : "+v"(asum), "+v"(dm0), "+v"(dm1), "+v"(dsto[0][0]), "+v"(dsto[0][1]), "+v"(dsto[1][0]), "+v"(dsto[1][1]));
#pragma unroll
    for (int g = 0; g < 2; ++g) {
      if (g == 0) vm_wait<12>(); else vm_wait<5>();
#pragma unroll
      for (int i = 0; i < 7; ++i) pin(xr[g * 7 + i]);
      if (g == 0) STAMP(1); else STAMP(2);
      f32x4 s1 = {0.f, 0.f, 0.f, 0.f}, qa = s1, qb = s1;
#pragma unroll
      for (int i = 0; i < 7; ++i) {
        const v4u d = xr[g * 7 + i];
        const s16x4 lo = __builtin_bit_cast(s16x4, v2u{d[0], d[1]}), hi = __builtin_bit_cast(s16x4, v2u{d[2], d[3]});
        s1 = __builtin_amdgcn_mfma_f32_16x16x32_bf16(__builtin_bit_cast(bf16x8, asum), __builtin_bit_cast(bf16x8, d), s1, 0, 0, 0);
        qa = __builtin_amdgcn_mfma_f32_4x4x4bf16_1k(lo, lo, qa, 0, 0, 0);
        qb = __builtin_amdgcn_mfma_f32_4x4x4bf16_1k(hi, hi, qb, 0, 0, 0);
      }
      asm volatile("" : "+v"(s1), "+v"(qa), "+v"(qb));
      qa += qb;
      const float q01 = (l3 & 1) ? qa[1] : qa[0], q23 = (l3 & 1) ? qa[3] : qa[2];
      const float mean = red8(s1[0]) * (1.f / 448.f);
      const float ex2 = red8((l3 & 2) ? q23 : q01) * (1.f / 448.f);
      const float var = __builtin_fmaxf(__builtin_fmaf(-mean, mean, ex2), 0.f);
      const float rstd = __builtin_amdgcn_rsqf(var + 1e-5f);
      const u32 rr = pk(rstd, rstd);
      const float nc = -mean * __builtin_bit_cast(float, rr << 16);
      const f32x4 cc = {nc, nc, nc, nc};
      const s16x4 an = __builtin_bit_cast(s16x4, v2u{rr & dm0, rr & dm1});
#pragma unroll
      for (int i = 0; i < 7; ++i) {
        const v4u d = xr[g * 7 + i];
        const f32x4 y0 = __builtin_amdgcn_mfma_f32_4x4x4bf16_1k(an, __builtin_bit_cast(s16x4, v2u{d[0], d[1]}), cc, 0, 0, 0);
        const f32x4 y1 = __builtin_amdgcn_mfma_f32_4x4x4bf16_1k(an, __builtin_bit_cast(s16x4, v2u{d[2], d[3]}), cc, 0, 0, 0);
        *(LAS v4u*)(L + (i == 6 ? dsto[g][0] : (i < 3 ? dsto[g][0] + 16384 * (i + 1) : dsto[g][1] + 16384 * (i - 3)))) = v4u{pk(y0[0], y0[1]), pk(y0[2], y0[3]), pk(y1[0], y1[1]), pk(y1[2], y1[3])};
      }
    }
  }
  STAMP(3);
  if (grp == 0) {
#pragma unroll 1
    for (int i = 0; i < 3; ++i)
      load_pieces(lds0 + (u32)(16384 * i + 4096 * m), goff_q, sqkv, (u32)((128 * i + 32 * m) * 2688 + cg * 128), 1024u, 8u * 2688u, 4);
  }
  if (threadIdx.x == 256) st16c<0>(v4u{0x3f803f80u, 0x3f803f80u, 0x3f803f80u, 0x3f803f80u}, 0u, ws, (u32)(WS_ONE + b * 128));
  Stream g;
  wg_barrier();
  STAMP(4);
  v4u xa[28];
  const int xlast = grp == 0 ? -16384 : 6 * 16384;
#define RDX(s) xa[s] = *(LAS v4u*)(L + xb[(s) & 3] + (((s) >> 2) == 6 ? xlast : 16384 * ((s) >> 2)))
#pragma unroll
  for (int s = 0; s < 4; ++s) RDX(s);
  const u32 st1 = s1q ? 2688u : 3584u;
  const int strip1 = s1q ? 16 + cg : cg - 5;
  g.srd = s1q ? sqkv : sw1; g.voff = s1q ? goff_q : goff_1; g.s8 = 8 * st1;
  if (grp == 1) {
    vm_wait<0>();
    wg_barrier();
    load_cells(lds0 + (u32)(57344 + m * 2048), g.voff, g.srd, (u32)(m * 16) * st1 + (u32)(strip1 * 128), st1, 1);
  }
#pragma unroll
  for (int s = 4; s < 16; ++s) RDX(s);
  if (grp == 1) asm volatile("s_waitcnt lgkmcnt(0)" ::: "memory");
#pragma unroll
  for (int s = 16; s < 28; ++s) RDX(s);
#undef RDX
  STAMP(5);
  const bool grpb = grp == 1;
  u32 q1 = (u32)((trl ^ (grp * 64)) + (grpb ? 49152 : 0)), q2 = (u32)((trl ^ (grp * 64)) + (grpb ? 0 : 8192)),
      q3 = (u32)((trl ^ (grp * 64)) + (grpb ? 40960 : 49152));
  u32 vst = (u32)(hh * 7168 + r32 * 16);
  Stream g0;
  g0.srd = sw1; g0.voff = goff_1; g0.s8 = 8 * 3584; g0.lds = lds0 + (u32)(114688 + 4096 * m);
  g0.soff = (u32)(32 * m * 3584 + (11 + cg) * 128); g0.n = grpb ? 12 : 0;
  asm volatile("" : "+v"(q1), "+v"(q2), "+v"(q3), "+v"(vst));
  keep_s(g0.lds); keep_s(g0.soff); keep_s(g0.n);
  if (!grpb) {
    vm_wait<0>();
    STAMP(6);
    wg_barrier();
    load_cells(lds0 + (u32)(49152 + m * 2048), goff_q, sqkv, (u32)((384 + m * 16) * 2688 + cg * 128), 2688u, 1);
  } else {
    g.soff = (u32)(64 + 32 * m) * st1 + (u32)(strip1 * 128); g.lds = lds0 + (u32)(65536 + 4096 * m); g.n = 12;
    stream_run<0, 6>(g);
    STAMP(6);
    wg_barrier();
    stream_run<6, 12>(g);
  }
  pin(cpf); pin(tag); pin(bias1); pin(bias2); pin(bias3);
  STAMP(8);
  f32x16 acc;
  v2u fpoll;
  u32 pend;
  bool pending;
  int blk;
  if (cg >= 14) {
    acc = gemm1_block<0, true, false>(xa, L + q1, L + q2, L + q3, 0u, g0, grpb, false, false, 0u, tag, ws);
    STAMP(9);
    store_v(acc, vst, ws, (u32)(WS_VT + tb1 * VT_TB + (2 * cg + grp - 28) * 512));
  } else {
    acc = gemm1_block<0, false, false>(xa, L + q1, L + q2, L + q3, 0u, g0, grpb, false, false, 0u, tag, ws);
    STAMP(9);
    v4u o0, o1;
    pack_tile(acc, o0, o1);
    const u32 so = (u32)(WS_QK + tb1 * QK_TB + (2 * cg + grp) * 2048);
    st16c<0>(o0, lo16, ws, so);
    st16c<1024>(o1, lo16, ws, so);
  }
  { const u64 t = (((u64)tag[1] << 32) | tag[0]) + 1; tag[0] = (u32)t; tag[1] = (u32)(t >> 32); }
  pend = (u32)(WS_FQ + (tb1 * 48 + 2 * cg + grp) * 8);
  const u32 st2 = heavy ? 3584u : 896u;
  g.srd = heavy ? sw1 : sw2; g.voff = heavy ? goff_1 : goff_2; g.s8 = 8 * st2;
  const u32 so2 = (u32)(32 * m) * st2 + (u32)((heavy ? 27 : ct) * 128);
  g.soff = so2 + (u32)(grp * 128) * st2; g.lds = lds0 + (u32)(grp * 16384 + 4096 * m); g.n = 4;
  {
    u32 p1 = q1 + 57344u, p2 = q2 + 57344u, p3 = q3 + 57344u;
    u32 tl = lds0 + (u32)(49152 + m * 2048), ts = (u32)((384 + m * 16) * 3584 + (11 + cg) * 128);
    asm volatile("" : "+v"(p1), "+v"(p2), "+v"(p3), "+v"(tag), "+v"(g.voff));
    keep_s(g.lds); keep_s(g.soff); keep_s(g.s8); keep_s(pend); keep_s(tl); keep_s(ts);
    STAMP(10);
    if (!grpb) {
      wg_barrier();
      load_cells(tl, goff_1, sw1, ts, 3584u, 1);
    }
    STAMP(12);
    if (s1q) {
      acc = gemm1_block<1, true, false>(xa, L + p1, L + p2, L + p3, 0u, g, grpb, true, false, pend, tag, ws);
      STAMP(13);
      store_v(acc, vst, ws, (u32)(WS_VT + tb1 * VT_TB + (2 * cg + grp + 4) * 512));
    } else {
      acc = gemm1_block<1, false, true>(xa, L + p1, L + p2, L + p3, bias1, g, grpb, true, false, pend, tag, ws);
      STAMP(13);
      store_gelu(acc, gc1, lo16, ws, (u32)(WS_HH + tb1 * HH_TB + (2 * cg - 10 + grp) * 2048));
    }
  }
  pend = (u32)((s1q ? WS_FQ + (tb1 * 48 + 32) * 8 : WS_FH + (tb1 * 64 - 10) * 8) + (2 * cg + grp) * 8);
  {
    g.soff = so2 + (grpb ? 256u : 384u) * st2; g.lds = lds0 + (u32)((grpb ? 4 : 7) * 8192 + 4096 * m); g.n = (grpb || heavy) ? 4 : 8;
    u32 p1 = q1 + (grpb ? 0u : 114688u), p2 = q2 + 114688u, p3 = q3 + (grpb ? 114688u : 0u);
    asm volatile("" : "+v"(p1), "+v"(p2), "+v"(p3));
    keep_s(g.lds); keep_s(g.soff); keep_s(g.n); keep_s(pend);
    STAMP(14);
    if (!grpb) wg_barrier();
    STAMP(16);
    u64 xst[5];
    acc = gemm1_block<2, false, true>(xa, L + p1, L + p2, L + p3, bias2, g, grpb, s1q, heavy, pend, tag, ws, xst);
    STAMP(17);
    pending = !s1q;
    blk = 22 + 2 * cg + grp;
  }
  if (heavy && !grpb) {
    vm_wait<0>();
    post_flag(pend, tag, ws);
    store_gelu(acc, gc1, lo16, ws, (u32)(WS_HH + tb1 * HH_TB + blk * 2048));
    pend = (u32)(WS_FH + (tb1 * 64 + blk) * 8);
    blk = 54 + (xcd & 1);
    const u32 t = (u32)(trl ^ ((xcd & 1) * 64));
    wg_barrier();
    STAMP(20);
    acc = gemm1_block<3, false, true>(xa, L + t, L + t + 8192, L + t + 57344, bias3, g, false, false, false, pend, tag, ws);
    STAMP(21);
  }

  {
    int kb[5];
#pragma unroll
    for (int n = 0; n < 5; ++n) kb[n] = tbq - (grp == 0 ? (n == 0 ? 0 : 4 + n) : 1 + n);
    v4u qf[2], kf[5][2], vf[5][2];
    {
      ld8c(fpoll, fo_qk, ws);
      u32 p[8];
      mfma_settle(acc);
      gelu4(p[0], p[1], acc[0], acc[1], acc[2], acc[3], gc1);
      gelu4(p[2], p[3], acc[4], acc[5], acc[6], acc[7], gc1);
      STAMP(24);
      vm_wait<0>();
      pin(fpoll);
      if (pending) post_flag(pend, tag, ws);
      if (!flags_ok(fpoll, lane < 12, tag)) wait_flags(fo_qk, lane < 12, tag, ws);
      STAMP(25);
      const u32 kbase = (u32)(WS_QK + (56 + c0) * 512);
      ld16c<0>(qf[0], lo16, ws, (u32)(WS_QK + tbq * QK_TB + c0 * 512));
      ld16c<1024>(qf[1], lo16, ws, (u32)(WS_QK + tbq * QK_TB + c0 * 512));
#define LDK(n) { const u32 so_ = kbase + (u32)((kb[n] < 0 ? tbq : kb[n]) * QK_TB); ld16c<0>(kf[n][0], lo16, ws, so_); ld16c<1024>(kf[n][1], lo16, ws, so_); }
      LDK(0)
      gelu4(p[4], p[5], acc[8], acc[9], acc[10], acc[11], gc1);
      LDK(1) LDK(2)
      gelu4(p[6], p[7], acc[12], acc[13], acc[14], acc[15], gc1);
      LDK(3)
      if (grp == 0) LDK(4)
#undef LDK
      v4u o0, o1;
      swap_tile(p, o0, o1);
      const u32 so = (u32)(WS_HH + tb1 * HH_TB + blk * 2048);
      st16c<0>(o0, lo16, ws, so);
      st16c<1024>(o1, lo16, ws, so);
      pend = (u32)(WS_FH + (tb1 * 64 + blk) * 8);
    }
    STAMP(26);
    f32x16 md, mw;
    {
      const u32 ninf = grp == 0 ? 0xff800000u : 0u;
      const int dd = r32 - 4 * hh;
#pragma unroll
      for (int i = 0; i < 16; ++i) {
        const u32 t = (u32)((dd - ((i & 3) + 8 * (i >> 2))) >> 31) & ninf;
        md[i] = __builtin_bit_cast(float, t);
        mw[i] = __builtin_bit_cast(float, t ^ 0xff800000u);
      }
    }
    asm volatile("" : "+v"(md), "+v"(mw));
    if (grp == 0 && !heavy) wg_barrier();
    ld8c(fpoll, fo_v, ws);
    STAMP(32);
    CSTAMP(61);
    vm_wait<3>();
    pin(qf[0]); pin(qf[1]);
#pragma unroll
    for (int n = 0; n < 5; ++n) { pin(kf[n][0]); pin(kf[n][1]); }
    STAMP(33);
    {
      const u32 k0 = (odd && hh == 0) ? 0u : ~0u, k1 = (!odd && hh == 1) ? 0u : ~0u;
      qf[0][0] &= k0; qf[0][1] &= k0; qf[1][2] &= k1; qf[1][3] &= k1;
    }
    f32x16 sc[5];
    f32x16 zero;
#pragma unroll
    for (int i = 0; i < 16; ++i) zero[i] = 0.f;
    float mx = -1e30f;
#pragma unroll
    for (int n = 0; n < 4; ++n) {
      sc[n] = mfma(kf[n][0], qf[0], n == 0 ? md : zero); sc[n] = mfma(kf[n][1], qf[1], sc[n]);
      __builtin_amdgcn_sched_barrier(0);
    }
    if (grp == 0) { sc[4] = mfma(kf[4][0], qf[0], mw); sc[4] = mfma(kf[4][1], qf[1], sc[4]); }
    float gate[5];
#pragma unroll
    for (int n = 0; n < 5; ++n) gate[n] = kb[n] < 0 ? -__builtin_inff() : 0.f;
    mfma_settle(sc[0]);
    mx = __builtin_fmaxf(mx, max16(sc[0]) + gate[0]);
    mx = __builtin_fmaxf(mx, max16(sc[1]) + gate[1]);
    mx = __builtin_fmaxf(mx, max16(sc[2]) + gate[2]);
    mfma_settle(sc[3]);
    mx = __builtin_fmaxf(mx, max16(sc[3]) + gate[3]);
    if (grp == 0) { mfma_settle(sc[4]); mx = __builtin_fmaxf(mx, max16(sc[4]) + gate[4]); }
    mx = __builtin_fmaxf(mx, swap32(mx));
    float cs = 0.18898223650461362f * 1.4426950408889634f;
    asm volatile("" : "+v"(cs));
    const float nb = -mx * cs;
    float nbn[5];
#pragma unroll
    for (int n = 0; n < 5; ++n) nbn[n] = nb + gate[n];
    v4u pf[5][2];
    pf[0][0] = exp8(sc[0], 0, cs, nbn[0]); pf[0][1] = exp8(sc[0], 8, cs, nbn[0]);
    pf[1][0] = exp8(sc[1], 0, cs, nbn[1]); pf[1][1] = exp8(sc[1], 8, cs, nbn[1]);
    pin(pf[0][0]); pin(pf[0][1]); pin(pf[1][0]); pin(pf[1][1]);
    STAMP(34);
    vm_wait<0>();
    pin(fpoll);
    post_flag(pend, tag, ws);
    if (!flags_ok(fpoll, lane < 10, tag)) wait_flags(fo_v, lane < 10, tag, ws);
    STAMP(35);
    {
#define LDV(n) { const u32 so_ = (u32)((kb[n] < 0 ? tbq : kb[n]) * VT_TB); \
      ld16c<0>(vf[n][0], mad24(vkeep, so_, vbase), ws, 0u); ld16c<0>(vf[n][1], mad24(vkeep, so_ + 14336u, vbase), ws, 0u); \
      __builtin_amdgcn_sched_barrier(0); }
      LDV(0) LDV(1)
      pf[2][0] = exp8(sc[2], 0, cs, nbn[2]); pin(pf[2][0]);
      LDV(2)
      pf[2][1] = exp8(sc[2], 8, cs, nbn[2]); pin(pf[2][1]);
      LDV(3)
      pf[3][0] = exp8(sc[3], 0, cs, nbn[3]); pin(pf[3][0]);
      if (grp == 0) LDV(4)
      pf[3][1] = exp8(sc[3], 8, cs, nbn[3]); pin(pf[3][1]);
#undef LDV
    }
    if (grp == 0) { pf[4][0] = exp8(sc[4], 0, cs, nbn[4]); pf[4][1] = exp8(sc[4], 8, cs, nbn[4]); pin(pf[4][0]); pin(pf[4][1]); }
    STAMP(36);
    vm_wait<0>();
#pragma unroll
    for (int n = 0; n < 5; ++n) { pin(vf[n][0]); pin(vf[n][1]); }
    f32x16 o = zero;
#pragma unroll
    for (int n = 0; n < 4; ++n) { o = mfma(vf[n][0], pf[n][0], o); o = mfma(vf[n][1], pf[n][1], o); }
    LAS char* sp = L + 17 * 8192 + m * 4096 + lane * 16;
    LAS float* mp = (LAS float*)(L + 6 * 8192 + m * 256 + lane * 4);
    if (grp == 0) {
      o = mfma(vf[4][0], pf[4][0], o); o = mfma(vf[4][1], pf[4][1], o);
#pragma unroll
      for (int q = 0; q < 4; ++q) *(LAS f32x4*)(sp + q * 1024) = f32x4{o[4 * q], o[4 * q + 1], o[4 * q + 2], o[4 * q + 3]};
      *mp = nb;
    }
    STAMP(37);
    wg_barrier();
    STAMP(38);
    if (grp == 1) {
      const float na = *mp;
      const float nm = __builtin_fminf(na, nb);
      const float fa = __builtin_amdgcn_exp2f(nm - na), fb = __builtin_amdgcn_exp2f(nm - nb);
#pragma unroll
      for (int q = 0; q < 4; ++q) {
        const f32x4 t = *(LAS f32x4*)(sp + q * 1024);
#pragma unroll
        for (int j = 0; j < 4; ++j) o[4 * q + j] = o[4 * q + j] * fb + t[j] * fa;
      }
      const float lown = odd ? o[0] : o[12], loth = swap32(lown);
      const float rl = __builtin_amdgcn_rcpf((odd ? hh == 0 : hh == 1) ? lown : loth);
#pragma unroll
      for (int i = 0; i < 16; ++i) o[i] *= rl;
      v4u o0, o1;
      pack_tile(o, o0, o1);
      const u32 so = (u32)(WS_HH + tbq * HH_TB + (224 + c0) * 512);
      if (odd) {
        st16c<1024>(o1, lo16, ws, so);
        if (hh == 1) st16c<0>(o0, lo16, ws, so); else st8c<8>(v2u{o0[2], o0[3]}, lo16, ws, so);
      } else {
        st16c<0>(o0, lo16, ws, so);
        if (hh == 0) st16c<1024>(o1, lo16, ws, so); else st8c<1024>(v2u{o1[0], o1[1]}, lo16, ws, so);
      }
    }
    pend = (u32)(WS_FO + (tbq * 16 + hd) * 8);
    STAMP(39);
  }

  if (heavy) {
    if (grp == 1) { vm_wait<0>(); post_flag(pend, tag, ws); }
    if (b == 255 && w == 0) {
#pragma unroll 1
      for (u32 spins = 0; spins < SPIN_CAP; ++spins) {
        v4u f[8];
        ld16c<0>(f[0], lo16, ws, (u32)WS_FO); ld16c<1024>(f[1], lo16, ws, (u32)WS_FO);
        ld16c<2048>(f[2], lo16, ws, (u32)WS_FO); ld16c<3072>(f[3], lo16, ws, (u32)WS_FO);
        ld16c<0>(f[4], lo16, ws, (u32)(WS_FO + 4096)); ld16c<1024>(f[5], lo16, ws, (u32)(WS_FO + 4096));
        ld16c<2048>(f[6], lo16, ws, (u32)(WS_FO + 4096)); ld16c<3072>(f[7], lo16, ws, (u32)(WS_FO + 4096));
        vm_wait<0>();
        bool ok = true;
#pragma unroll
        for (int i = 0; i < 8; ++i) { pin(f[i]); ok = ok && f[i][0] == tag[0] && f[i][1] == tag[1] && f[i][2] == tag[0] && f[i][3] == tag[1]; }
        if (__all(ok)) break;
        __builtin_amdgcn_s_sleep(8);
      }
      post_flag((u32)WS_E, tag, ws);
    }
    STAMP_OUT();
    return;
  }

  {
    const int kq = m, mh = grp, tb = 2 * tt + mh;
    const __attribute__((address_space(4))) u64* ka = (const __attribute__((address_space(4))) u64*)__builtin_amdgcn_kernarg_segment_ptr();
    asm volatile("" : "+s"(ka));
    const v4i sx2 = make_srd((const void*)ka[0], S_ * D_ * 2), swo = make_srd((const void*)ka[2], D_ * D_ * 2);
    const v4i sb2 = make_srd((const void*)ka[6], D_ * 2), sout = make_srd((const void*)ka[7], S_ * D_ * 2);
    auto cell_of = [&](int pos) { const int i = 4 * pos + kq; return i < 6 ? i : (i < 19 ? i + 1 : 6); };
    const u32 fo_h = (u32)(WS_FH + (tb * 64 + ((lane >> 1) * 4 + kq) * 2 + (lane & 1)) * 8);
    ld8c(fpoll, fo_h, ws);
    load_pieces(lds0 + (u32)(11 * 8192 + w * 1024), goff_2, sw2, (u32)((640 + w * 8) * 896 + ct * 128), 8192u, 64u * 896u, 6);
    v4u xres, bres;
    const u32 oo = (u32)((tb * 32 + r32) * 896 + (ct * 64 + kq * 16 + hh * 8) * 2);
    ld16<0>(xres, oo, sx2, 0u);
    ld16<0>(bres, (u32)(hh * 16), sb2, (u32)((ct * 64 + kq * 16) * 2));
    vm_wait<8>();
    pin(fpoll);
    if (grp == 1) post_flag(pend, tag, ws);
    if (!flags_ok(fpoll, lane < 14, tag)) wait_flags(fo_h, lane < 14, tag, ws);
    STAMP(40);
    v4u hf[35];
    const u32 hb = (u32)(WS_HH + tb * HH_TB + kq * 4096);
#define LDH1(j, s) ld16c<(s) * 1024>(hf[4 * (j) + (s)], lo16, ws, (j) < 8 ? hb + (j) * 16384 : hb + 8 * 16384 - kq * 1024)
#pragma unroll
    for (int j = 0; j < 2; ++j) { LDH1(j, 0); LDH1(j, 1); LDH1(j, 2); LDH1(j, 3); }
    f32x16 acc0, acc1;
#pragma unroll
    for (int i = 0; i < 16; ++i) { acc0[i] = 0.f; acc1[i] = 0.f; }
    const u32 fo_o = (u32)(WS_FO + (tb * 16 + (lane & 15)) * 8);
#define STAGE(j, WAIT) { \
      constexpr int ns = (j) == 8 ? 3 : 4; \
      vm_wait<WAIT>(); \
      pin(hf[4 * (j)]); pin(hf[4 * (j) + 1]); pin(hf[4 * (j) + 2]); if (ns == 4) pin(hf[4 * (j) + (ns - 1)]); \
      if ((j) == 5) { pin(fpoll); if (!flags_ok(fpoll, true, tag)) wait_flags(fo_o, true, tag, ws); } \
      wg_barrier(); \
      STAMP(41 + (j)); \
      LAS char* p0 = L + cell_of((j) % 5) * 8192 + trl; \
      LAS char* p1 = L + cell_of((j) % 5) * 8192 + (trl ^ 64); \
      v4u wa[4], wb[4]; \
      wa[0] = tr_frag(p0); wb[0] = tr_frag(p1); wa[1] = tr_frag(p0 + 2048); wb[1] = tr_frag(p1 + 2048); \
      wa[2] = tr_frag(p0 + 4096); wb[2] = tr_frag(p1 + 4096); \
      if (ns == 4) { wa[3] = tr_frag(p0 + 6144); wb[3] = tr_frag(p1 + 6144); } \
      u32 gl = lds0 + (u32)(cell_of(((j) + 4) % 5) * 8192 + mh * ((j) == 4 ? 3072 : 4096)); \
      u32 gs = (u32)(((j) < 3 ? 256 * ((j) + 4) + 64 * kq + 32 * mh : ((j) == 3 ? 64 * kq + 32 * mh : 256 + 48 * kq + 24 * mh)) * 896 + ct * 128); \
      __builtin_amdgcn_sched_barrier(0); \
      if ((j) <= 6) LDH1(((j) <= 6 ? (j) + 2 : 0), 0); \
      acc0 = mfma(wa[0], hf[4 * (j)], acc0); __builtin_amdgcn_sched_barrier(0); \
      if ((j) <= 6) LDH1(((j) <= 6 ? (j) + 2 : 0), 1); \
      acc1 = mfma(wb[0], hf[4 * (j)], acc1); __builtin_amdgcn_sched_barrier(0); \
      if ((j) <= 6) LDH1(((j) <= 6 ? (j) + 2 : 0), 2); \
      acc0 = mfma(wa[1], hf[4 * (j) + 1], acc0); __builtin_amdgcn_sched_barrier(0); \
      if ((j) <= 5) LDH1(((j) <= 5 ? (j) + 2 : 0), 3); \
      acc1 = mfma(wb[1], hf[4 * (j) + 1], acc1); __builtin_amdgcn_sched_barrier(0); \
      if ((j) <= 4) glds16(gl, goff_2, (j) < 3 ? sw2 : swo, gs); \
      acc0 = mfma(wa[2], hf[4 * (j) + 2], acc0); __builtin_amdgcn_sched_barrier(0); \
      if ((j) <= 4) glds16(gl + 1024, goff_2, (j) < 3 ? sw2 : swo, gs + 7168); \
      acc1 = mfma(wb[2], hf[4 * (j) + 2], acc1); __builtin_amdgcn_sched_barrier(0); \
      if ((j) <= 4) glds16(gl + 2048, goff_2, (j) < 3 ? sw2 : swo, gs + 14336); \
      if (ns == 4) { acc0 = mfma(wa[3], hf[4 * (j) + (ns - 1)], acc0); __builtin_amdgcn_sched_barrier(0); } \
      if ((j) <= 3) glds16(gl + 3072, goff_2, (j) < 3 ? sw2 : swo, gs + 21504); \
      if ((j) == 3) ld8c(fpoll, fo_o, ws); \
      if (ns == 4) { acc1 = mfma(wb[3], hf[4 * (j) + (ns - 1)], acc1); __builtin_amdgcn_sched_barrier(0); } \
    }
    STAGE(0, 4) STAGE(1, 8) STAGE(2, 12) STAGE(3, 12) STAGE(4, 13) STAGE(5, 7) STAGE(6, 7) STAGE(7, 3) STAGE(8, 0)
#undef STAGE
#undef LDH1
    STAMP(50);
    {
      LAS char* rp = L + (w < 6 ? w : w + 1) * 8192 + lane * 16;
#pragma unroll
      for (int q = 0; q < 4; ++q) {
        *(LAS f32x4*)(rp + q * 1024) = f32x4{acc0[4 * q], acc0[4 * q + 1], acc0[4 * q + 2], acc0[4 * q + 3]};
        *(LAS f32x4*)(rp + 4096 + q * 1024) = f32x4{acc1[4 * q], acc1[4 * q + 1], acc1[4 * q + 2], acc1[4 * q + 3]};
      }
    }
    wg_barrier();
    STAMP(51);
    pin(xres); pin(bres);
    f32x4 a = {0.f, 0.f, 0.f, 0.f}, c = {0.f, 0.f, 0.f, 0.f};
#pragma unroll
    for (int k = 0; k < 4; ++k) {
      const int ww = 4 * mh + k;
      LAS char* rp = L + (ww < 6 ? ww : ww + 1) * 8192 + kq * 2048 + hh * 1024 + r32 * 16;
      a += *(LAS f32x4*)rp;
      c += *(LAS f32x4*)(rp + 512);
    }
    auto lo_f = [](u32 d) { return __builtin_bit_cast(float, d << 16); };
    auto hi_f = [](u32 d) { return __builtin_bit_cast(float, d & 0xffff0000u); };
    v4u o;
    o[0] = pk(a[0] + lo_f(xres[0]) + lo_f(bres[0]), a[1] + hi_f(xres[0]) + hi_f(bres[0]));
    o[1] = pk(a[2] + lo_f(xres[1]) + lo_f(bres[1]), a[3] + hi_f(xres[1]) + hi_f(bres[1]));
    o[2] = pk(c[0] + lo_f(xres[2]) + lo_f(bres[2]), c[1] + hi_f(xres[2]) + hi_f(bres[2]));
    o[3] = pk(c[2] + lo_f(xres[3]) + lo_f(bres[3]), c[3] + hi_f(xres[3]) + hi_f(bres[3]));
    st16(o, oo, sout);
    STAMP(52);
    CSTAMP(62);
    STAMP_OUT();
  }
}

extern "C" void kernel_launch(void* const* d_in, const int* in_sizes, int n_in, void* d_out, int out_size, void* d_ws, size_t ws_size,
                              hipStream_t stream) {
  (void)hipFuncSetAttribute((const void*)fused_block, hipFuncAttributeMaxDynamicSharedMemorySize, LDS_BYTES);
  fused_block<<<256, 512, LDS_BYTES, stream>>>((const u16*)d_in[0], (const u16*)d_in[1], (const u16*)d_in[2], (const u16*)d_in[4],
                                               (const u16*)d_in[5], (const u16*)d_in[6], (const u16*)d_in[7], (u16*)d_out, (char*)d_ws);
}
```

```cpp
#define STAMPS 1
#define PROBE_I 0
#define PROBE_MODE 2
#define PROBE_K 0
#define PROBE_W 4
#include <hip/hip_runtime.h>

typedef unsigned short u16;
typedef unsigned int u32;
typedef unsigned long long u64;
typedef u32 v4u __attribute__((ext_vector_type(4)));
typedef u32 v2u __attribute__((ext_vector_type(2)));
typedef int v4i __attribute__((ext_vector_type(4)));
typedef short s16x4 __attribute__((ext_vector_type(4)));
typedef __bf16 bf16x8 __attribute__((ext_vector_type(8)));
typedef __bf16 bf16x2 __attribute__((ext_vector_type(2)));
typedef float f32x2 __attribute__((ext_vector_type(2)));
typedef float f32x4 __attribute__((ext_vector_type(4)));
typedef float f32x16 __attribute__((ext_vector_type(16)));
#define GAS __attribute__((address_space(1)))
#define LAS __attribute__((address_space(3)))
#define DI __device__ __forceinline__

#define EXP_NOSTREAM 0
#define EXP_XST 0

constexpr int S_ = 2048, D_ = 448, F_ = 1792;
constexpr int WS_E = 0;
constexpr int WS_FQ = 4096;
constexpr int WS_FH = WS_FQ + 64 * 48 * 8;
constexpr int WS_FO = WS_FH + 64 * 64 * 8;
constexpr int WS_ONE = 1 << 19;
constexpr int WS_QK = 1 << 20;
constexpr int QK_TB = 112 * 512;
constexpr int WS_VT = WS_QK + 64 * QK_TB;
constexpr int VT_TB = 4 * 448 * 16;
constexpr int WS_HH = WS_VT + 64 * VT_TB;
constexpr int HH_TB = 280 * 512;
constexpr int WS_END = WS_HH + 64 * HH_TB;
constexpr int WS_DBG = 32 << 20;
constexpr int LDS_BYTES = 163840;
constexpr u32 SPIN_CAP = 1u << 16;

DI v4i make_srd(const void* p, u32 bytes) {
  const u64 a = (u64)p;
  v4i r;
  r[0] = (int)(u32)a; r[1] = (int)((u32)(a >> 32) & 0xffffu); r[2] = (int)bytes; r[3] = 0x00020000;
  return r;
}
template <int OFF> DI void ld16(v4u& d, u32 voff, v4i srd, u32 soff) {
  asm volatile("buffer_load_dwordx4 %0, %1, %2, %3 offen offset:%4" : "=v"(d) : "v"(voff), "s"(srd), "s"(soff), "i"(OFF) : "memory");
}
template <int OFF> DI void ld16c(v4u& d, u32 voff, v4i srd, u32 soff) {
  asm volatile("buffer_load_dwordx4 %0, %1, %2, %3 offen offset:%4 sc1" : "=v"(d) : "v"(voff), "s"(srd), "s"(soff), "i"(OFF) : "memory");
}
DI void ld8c(v2u& d, u32 voff, v4i srd) {
  asm volatile("buffer_load_dwordx2 %0, %1, %2, 0 offen sc1" : "=v"(d) : "v"(voff), "s"(srd) : "memory");
}
DI void ld2(u32& d, u32 voff, v4i srd, u32 soff) {
  asm volatile("buffer_load_ushort %0, %1, %2, %3 offen" : "=v"(d) : "v"(voff), "s"(srd), "s"(soff) : "memory");
}
DI void glds16(u32 lds, u32 voff, v4i srd, u32 soff) {
  asm volatile("s_mov_b32 m0, %0\n\ts_nop 0\n\tbuffer_load_dwordx4 %1, %2, %3 offen lds" ::"s"(lds), "v"(voff), "s"(srd), "s"(soff) : "memory");
}
DI void load_cells(u32 lds, u32 voff, v4i srd, u32 soff, u32 stride, int n) {
#pragma unroll 1
  for (int c = 0; c < n; ++c) {
    u32 t;
    asm volatile("s_mov_b32 m0, %1\n\ts_add_u32 %0, %4, %5\n\tbuffer_load_dwordx4 %2, %3, %4 offen lds\n\t"
                 "s_add_u32 m0, %1, 0x400\n\ts_nop 0\n\tbuffer_load_dwordx4 %2, %3, %0 offen lds"
                 : "=&s"(t) : "s"(lds), "v"(voff), "s"(srd), "s"(soff), "s"(stride * 8) : "memory", "scc");
    lds += 8192; soff += stride * 64;
  }
}
DI void load_pieces(u32 lds, u32 voff, v4i srd, u32 soff, u32 lstep, u32 sstep, int n) {
#pragma unroll 1
  for (int c = 0; c < n; ++c) { glds16(lds, voff, srd, soff); lds += lstep; soff += sstep; }
}
template <int OFF> DI void st16c(v4u d, u32 voff, v4i srd, u32 soff) {
  asm volatile("buffer_store_dwordx4 %0, %1, %2, %3 offen offset:%4 sc1\n\ts_nop 1" ::"v"(d), "v"(voff), "s"(srd), "s"(soff), "i"(OFF) : "memory");
}
template <int OFF> DI void st8c(v2u d, u32 voff, v4i srd, u32 soff) {
  asm volatile("buffer_store_dwordx2 %0, %1, %2, %3 offen offset:%4 sc1" ::"v"(d), "v"(voff), "s"(srd), "s"(soff), "i"(OFF) : "memory");
}
DI void st16(v4u d, u32 voff, v4i srd) {
  asm volatile("buffer_store_dwordx4 %0, %1, %2, 0 offen\n\ts_nop 1" ::"v"(d), "v"(voff), "s"(srd) : "memory");
}
template <int N> DI void vm_wait() { asm volatile("s_waitcnt vmcnt(%0)" ::"i"(N) : "memory"); }
DI void pin(v4u& a) { asm volatile("" : "+v"(a)); }
DI void pin(v2u& a) { asm volatile("" : "+v"(a)); }
DI void pin(u32& a) { asm volatile("" : "+v"(a)); }
DI void keep_s(u32& a) { a = __builtin_amdgcn_readfirstlane(a); asm volatile("" : "+s"(a)); }
DI void wg_barrier() { asm volatile("s_waitcnt lgkmcnt(0)\n\ts_barrier" ::: "memory"); }
DI f32x16 mfma(v4u a, v4u b, f32x16 c) {
  return __builtin_amdgcn_mfma_f32_32x32x16_bf16(__builtin_bit_cast(bf16x8, a), __builtin_bit_cast(bf16x8, b), c, 0, 0, 0);
}
DI u32 pk(float a, float b) { f32x2 f = {a, b}; return __builtin_bit_cast(u32, __builtin_convertvector(f, bf16x2)); }
DI v4u tr_frag(LAS char* p) {
  const s16x4 a = __builtin_amdgcn_ds_read_tr16_b64_v4i16((LAS s16x4*)p);
  const s16x4 b = __builtin_amdgcn_ds_read_tr16_b64_v4i16((LAS s16x4*)(p + 512));
  const v2u x = __builtin_bit_cast(v2u, a), y = __builtin_bit_cast(v2u, b);
  return v4u{x[0], x[1], y[0], y[1]};
}
template <int C> DI float dpp_add(float x) {
  return x + __builtin_bit_cast(float, __builtin_amdgcn_update_dpp(0, __builtin_bit_cast(int, x), C, 0xf, 0xf, true));
}
DI float red8(float x) { x = dpp_add<0xB1>(x); x = dpp_add<0x4E>(x); return dpp_add<0x141>(x); }
DI float swap32(float x) {
  const u32 u = __builtin_bit_cast(u32, x);
  const auto r = __builtin_amdgcn_permlane32_swap(u, u, false, false);
  return __builtin_bit_cast(float, (threadIdx.x & 32) ? r[0] : r[1]);
}
DI void pack_tile(const f32x16& a, v4u& o0, v4u& o1) {
  u32 p[8];
#pragma unroll
  for (int i = 0; i < 8; ++i) p[i] = pk(a[2 * i], a[2 * i + 1]);
  const auto r0 = __builtin_amdgcn_permlane32_swap(p[0], p[2], false, false);
  const auto r1 = __builtin_amdgcn_permlane32_swap(p[1], p[3], false, false);
  const auto r2 = __builtin_amdgcn_permlane32_swap(p[4], p[6], false, false);
  const auto r3 = __builtin_amdgcn_permlane32_swap(p[5], p[7], false, false);
  o0 = v4u{r0[0], r1[0], r0[1], r1[1]};
  o1 = v4u{r2[0], r3[0], r2[1], r3[1]};
}
DI void wait_flags(u32 voff, bool active, v2u tag, v4i ws) {
#pragma unroll 1
  for (u32 spins = 0; spins < SPIN_CAP; ++spins) {
    v2u f;
    ld8c(f, voff, ws);
    vm_wait<0>();
    pin(f);
    const bool ok = !active || (f[0] == tag[0] && f[1] == tag[1]);
    if (__all(ok)) break;
    __builtin_amdgcn_s_sleep(2);
  }
}
DI void post_flag(u32 off, v2u tag, v4i ws) {
  asm volatile("s_mov_b64 exec, 1\n\tbuffer_store_dwordx2 %0, off, %1, %2 sc1\n\ts_mov_b64 exec, -1" ::"v"(tag), "s"(ws), "s"(off) : "memory");
}
#define STAMP(i) do { u64 t_; __builtin_amdgcn_sched_barrier(0); asm volatile("s_memrealtime %0\n\ts_waitcnt lgkmcnt(0)" : "=s"(t_) :: "memory"); \
  asm volatile("s_mov_b32 m0, %2\n\ts_nop 0\n\tv_writelane_b32 %0, %1, m0" : "+v"(stv_) : "s"((u32)t_), "s"((u32)(i))); __builtin_amdgcn_sched_barrier(0); } while (0)
#define CSTAMP(i) do { u64 t_; asm volatile("s_memtime %0\n\ts_waitcnt lgkmcnt(0)" : "=s"(t_) :: "memory"); \
  asm volatile("s_mov_b32 m0, %2\n\ts_nop 0\n\tv_writelane_b32 %0, %1, m0" : "+v"(stv_) : "s"((u32)t_), "s"((u32)(i))); } while (0)
#define XSTAMP(i) asm volatile("s_memtime %0" : "=s"(xst[(i) - 53]) :: "memory")
#define STAMP_DECL u32 stv_ = 0
#define STAMP_OUT() do { if (blockIdx.x == 100 && w == PROBE_W) { \
  const u32 own_ = (u32)__builtin_amdgcn_readlane(stv_, 0); \
  int mn_ = 1 << 30, mx_ = -(1 << 30); \
  for (int q_ = 0; q_ < 4; ++q_) { const int v_ = (int)(__hip_atomic_load((GAS u32*)(wsp + 262144) + q_ * 64 + lane, __ATOMIC_RELAXED, __HIP_MEMORY_SCOPE_AGENT) - own_); mn_ = min(mn_, v_); mx_ = max(mx_, v_); } \
  for (int o_ = 32; o_ >= 1; o_ >>= 1) { mn_ = min(mn_, __shfl_xor(mn_, o_)); mx_ = max(mx_, __shfl_xor(mx_, o_)); } \
  const u32 d_ = __builtin_amdgcn_readfirstlane(PROBE_MODE == 0 ? -mn_ : (PROBE_MODE == 1 ? mx_ : 100)); \
  const u32 t0_ = (u32)__builtin_amdgcn_s_memrealtime(); \
  for (int i_ = 0; i_ < 1000000; ++i_) { if ((u32)__builtin_amdgcn_s_memrealtime() - t0_ >= 1000 + PROBE_K * d_) break; __builtin_amdgcn_s_sleep(1); } } } while (0)

struct Stream { v4i srd; u32 voff, soff, s8, lds, n; };
template <int P, bool COND> DI void stream_piece(const Stream& g) {
  u32 t;
  if constexpr (COND)
    asm volatile("s_cmp_gt_u32 %6, %9\n\ts_cbranch_scc0 1f\n\ts_mul_i32 %0, %5, %8\n\ts_add_u32 m0, %1, %7\n\ts_add_u32 %0, %0, %2\n\t"
                 "buffer_load_dwordx4 %3, %4, %0 offen lds\n1:"
                 : "=&s"(t) : "s"(g.lds), "s"(g.soff), "v"(g.voff), "s"(g.srd), "s"(g.s8), "s"(g.n),
                   "i"((P >> 2) * 16384 + (P & 3) * 1024), "i"((P >> 2) * 16 + (P & 3)), "i"(P) : "memory", "scc");
  else
    asm volatile("s_mul_i32 %0, %5, %7\n\ts_add_u32 m0, %1, %6\n\ts_add_u32 %0, %0, %2\n\tbuffer_load_dwordx4 %3, %4, %0 offen lds"
                 : "=&s"(t) : "s"(g.lds), "s"(g.soff), "v"(g.voff), "s"(g.srd), "s"(g.s8),
                   "i"((P >> 2) * 16384 + (P & 3) * 1024), "i"((P >> 2) * 16 + (P & 3)) : "memory", "scc");
}
template <int LO, int HI> DI void stream_run(const Stream& g) {
  if constexpr (LO < HI) { stream_piece<LO, false>(g); stream_run<LO + 1, HI>(g); }
}
DI u32 mad24(u32 a, u32 s, u32 c) { u32 d; asm("v_mad_u32_u24 %0, %1, %2, %3" : "=v"(d) : "v"(a), "s"(s), "v"(c)); return d; }
DI bool flags_ok(v2u f, bool active, v2u tag) { return __all(!active || (f[0] == tag[0] && f[1] == tag[1])); }

template <int SLOT, bool SWAP, bool BIAS>
DI f32x16 gemm1_block(const v4u (&xa)[28], LAS char* p1, LAS char* p2, LAS char* p3, u32 bias, const Stream& g, bool grpb, bool post, bool heavy,
                      u32 pend, v2u tag, v4i ws, u64* xst = nullptr) {
  constexpr int PD = 5;
  constexpr int NP = SLOT == 0 ? 12 : (SLOT == 1 ? 4 : (SLOT == 2 ? 8 : 0)), NU = SLOT == 0 ? 0 : 4;
  auto rd = [&](int s) { return s < 4 ? tr_frag(p1 + s * 2048) : (s < 24 ? tr_frag(p2 + (s - 4) * 2048) : tr_frag(p3 + (s - 24) * 2048)); };
  __builtin_amdgcn_s_setprio(1);
  v4u wf[PD];
#pragma unroll
  for (int s = 0; s < PD; ++s) wf[s] = rd(s);
  f32x16 acc;
#pragma unroll
  for (int i = 0; i < 16; ++i) acc[i] = 0.f;
  if constexpr (BIAS) {
    const v4u bf = {bias, 0u, 0u, 0u};
    const v4u one = {(threadIdx.x & 32) ? 0u : 0x3f80u, 0u, 0u, 0u};
    acc = mfma(bf, one, acc);
  }
  __builtin_amdgcn_sched_barrier(0);
#define G1STEP(s) { \
    const v4u cur = wf[(s) % PD]; \
    if ((s) == 19) { \
      if (SLOT == 0) { if (!grpb) { vm_wait<0>(); asm volatile("s_barrier" ::: "memory"); } } \
      else if (SLOT == 1) { vm_wait<4>(); post_flag(pend, tag, ws); } \
      else if (SLOT == 2) { if (post) { vm_wait<4>(); post_flag(pend, tag, ws); } } \
    } \
    if ((s) == 24 && SLOT < 3) { \
      if (grpb) { \
        if (SLOT == 0) vm_wait<12>(); \
        if (SLOT == 2) { if (heavy) vm_wait<0>(); } \
        asm volatile("s_barrier" ::: "memory"); \
        __builtin_amdgcn_s_setprio(2); \
      } \
    } \
    acc = SWAP ? mfma(xa[s], cur, acc) : mfma(cur, xa[s], acc); \
    if ((s) + PD < 28) wf[(s) % PD] = rd((s) + PD); \
    if (((s) & 1) == 0 && (s) / 2 < NP && !(EXP_NOSTREAM && SLOT > 0)) stream_piece<(s) / 2, ((s) / 2 >= NU)>(g); \
    if (SLOT == 2 && EXP_XST && ((s) == 0 || (s) == 6 || (s) == 12 || (s) == 18 || (s) == 23)) XSTAMP(53 + ((s) + 1) / 6); \
    __builtin_amdgcn_sched_barrier(0); }
  G1STEP(0) G1STEP(1) G1STEP(2) G1STEP(3) G1STEP(4) G1STEP(5) G1STEP(6) G1STEP(7) G1STEP(8) G1STEP(9)
  G1STEP(10) G1STEP(11) G1STEP(12) G1STEP(13) G1STEP(14) G1STEP(15) G1STEP(16) G1STEP(17) G1STEP(18) G1STEP(19)
  G1STEP(20) G1STEP(21) G1STEP(22) G1STEP(23) G1STEP(24) G1STEP(25) G1STEP(26) G1STEP(27)
#undef G1STEP
  __builtin_amdgcn_s_setprio(0);
  return acc;
}

DI void gelu4(u32& o0, u32& o1, float a, float b, float c, float d, float c1) {
  float t0, t1, t2, t3;
  asm volatile(
      "v_mul_f32 %2, %6, %6\n\tv_mul_f32 %3, %7, %7\n\tv_mul_f32 %4, %8, %8\n\tv_mul_f32 %5, %9, %9\n\t"
      "v_fmaak_f32 %2, %2, %10, 0xc0135761\n\tv_fmaak_f32 %3, %3, %10, 0xc0135761\n\t"
      "v_fmaak_f32 %4, %4, %10, 0xc0135761\n\tv_fmaak_f32 %5, %5, %10, 0xc0135761\n\t"
      "v_mul_f32 %2, %2, %6\n\tv_mul_f32 %3, %3, %7\n\tv_mul_f32 %4, %4, %8\n\tv_mul_f32 %5, %5, %9\n\t"
      "v_exp_f32 %2, %2\n\tv_exp_f32 %3, %3\n\tv_exp_f32 %4, %4\n\tv_exp_f32 %5, %5\n\t"
      "v_add_f32 %2, 1.0, %2\n\tv_add_f32 %3, 1.0, %3\n\tv_add_f32 %4, 1.0, %4\n\tv_add_f32 %5, 1.0, %5\n\t"
      "v_rcp_f32 %2, %2\n\tv_rcp_f32 %3, %3\n\tv_rcp_f32 %4, %4\n\tv_rcp_f32 %5, %5\n\t"
      "v_mul_f32 %2, %2, %6\n\tv_mul_f32 %3, %3, %7\n\tv_mul_f32 %4, %4, %8\n\tv_mul_f32 %5, %5, %9\n\t"
      "v_cvt_pk_bf16_f32 %0, %2, %3\n\tv_cvt_pk_bf16_f32 %1, %4, %5\n\ts_nop 1"
      : "=&v"(o0), "=&v"(o1), "=&v"(t0), "=&v"(t1), "=&v"(t2), "=&v"(t3) : "v"(a), "v"(b), "v"(c), "v"(d), "v"(c1));
}
DI void swap_tile(const u32 (&p)[8], v4u& o0, v4u& o1) {
  const auto r0 = __builtin_amdgcn_permlane32_swap(p[0], p[2], false, false);
  const auto r1 = __builtin_amdgcn_permlane32_swap(p[1], p[3], false, false);
  const auto r2 = __builtin_amdgcn_permlane32_swap(p[4], p[6], false, false);
  const auto r3 = __builtin_amdgcn_permlane32_swap(p[5], p[7], false, false);
  o0 = v4u{r0[0], r1[0], r0[1], r1[1]};
  o1 = v4u{r2[0], r3[0], r2[1], r3[1]};
}
DI void mfma_settle(const f32x16& a) { asm volatile("s_nop 7\n\ts_nop 4" ::"v"(a)); }
DI void store_v(const f32x16& a, u32 vo, v4i ws, u32 so) {
  st16c<0>(v4u{pk(a[0], a[1]), pk(a[2], a[3]), pk(a[4], a[5]), pk(a[6], a[7])}, vo, ws, so);
  st16c<0>(v4u{pk(a[8], a[9]), pk(a[10], a[11]), pk(a[12], a[13]), pk(a[14], a[15])}, vo, ws, so + 14336u);
}
DI void store_gelu(const f32x16& a, float gc1, u32 lo16, v4i ws, u32 so) {
  u32 p[8];
  mfma_settle(a);
#pragma unroll
  for (int i = 0; i < 4; ++i) gelu4(p[2 * i], p[2 * i + 1], a[4 * i], a[4 * i + 1], a[4 * i + 2], a[4 * i + 3], gc1);
  v4u o0, o1;
  swap_tile(p, o0, o1);
  st16c<0>(o0, lo16, ws, so);
  st16c<1024>(o1, lo16, ws, so);
}
DI float max16(const f32x16& s) {
  float a, b;
  asm volatile("v_max3_f32 %0, %2, %3, %4\n\tv_max3_f32 %1, %5, %6, %7\n\tv_max3_f32 %0, %0, %8, %9\n\tv_max3_f32 %1, %1, %10, %11\n\t"
               "v_max3_f32 %0, %0, %12, %13\n\tv_max3_f32 %1, %1, %14, %15\n\tv_max3_f32 %0, %0, %16, %17\n\tv_max_f32 %0, %0, %1"
               : "=&v"(a), "=&v"(b) : "v"(s[0]), "v"(s[1]), "v"(s[2]), "v"(s[3]), "v"(s[4]), "v"(s[5]), "v"(s[6]), "v"(s[7]), "v"(s[8]),
                 "v"(s[9]), "v"(s[10]), "v"(s[11]), "v"(s[12]), "v"(s[13]), "v"(s[14]), "v"(s[15]));
  return a;
}
DI v4u exp8(const f32x16& s, int o, float cs, float nb) {
  v4u p;
#pragma unroll
  for (int j = 0; j < 4; ++j)
    p[j] = pk(__builtin_amdgcn_exp2f(__builtin_fmaf(s[o + 2 * j], cs, nb)), __builtin_amdgcn_exp2f(__builtin_fmaf(s[o + 2 * j + 1], cs, nb)));
  return p;
}

__global__ void __launch_bounds__(512, 2)
fused_block(const u16* __restrict__ x, const u16* __restrict__ wqkv, const u16* __restrict__ wo, const u16* __restrict__ w1,
            const u16* __restrict__ b1, const u16* __restrict__ w2, const u16* __restrict__ b2, u16* __restrict__ out, char* wsp) {
  extern __shared__ __attribute__((aligned(16))) char smem_[];
  LAS char* const L = (LAS char*)smem_;
  const u32 lds0 = (u32)(size_t)L;
  const int lane = threadIdx.x & 63;
  const int w = __builtin_amdgcn_readfirstlane(threadIdx.x >> 6);
  const int m = w & 3, grp = w >> 2;
  const int b = blockIdx.x, xcd = b & 7, r = b >> 3;
  const int r32 = lane & 31, hh = lane >> 5;
  const v4i ws = make_srd(wsp, WS_END);
  const v4i sx = make_srd(x, S_ * D_ * 2);
  const v4i sqkv = make_srd(wqkv, D_ * 3 * D_ * 2);
  const v4i sw1 = make_srd(w1, D_ * F_ * 2);
  const v4i sw2 = make_srd(w2, F_ * D_ * 2);
  STAMP_DECL;
  STAMP(0);
  CSTAMP(60);
  if (threadIdx.x == 0) { const u32 t_ = (u32)__builtin_amdgcn_s_memrealtime(); __hip_atomic_store((GAS u32*)(wsp + 262144) + blockIdx.x, t_, __ATOMIC_RELAXED, __HIP_MEMORY_SCOPE_AGENT); }

  const int rt = 4 * (xcd >> 1) + (r & 3);
  const int cg = 8 * (xcd & 1) + (r >> 2);
  const bool heavy = r >= 28;
  const int tt = 4 * xcd + (r & 3), ct = r >> 2;
  const bool s1q = cg < 5;
  const int r8 = lane >> 3, c8 = lane & 7;

  v2u tag;
  v4u xr[14];
  {
    const u32 xo = (u32)((rt * 128 + 16 * w + r8) * 896 + c8 * 16);
#define LDX(g, i) ld16<(i) * 128>(xr[(g) * 7 + (i)], xo, sx, (u32)((g) * 7168))
    LDX(0, 0); LDX(0, 1); LDX(0, 2); LDX(0, 3); LDX(0, 4); LDX(0, 5); LDX(0, 6);
    LDX(1, 0); LDX(1, 1); LDX(1, 2); LDX(1, 3); LDX(1, 4); LDX(1, 5); LDX(1, 6);
#undef LDX
  }
  ld8c(tag, 0u, ws);
  u32 bias1, bias2, bias3;
  {
    const v4i sb1 = make_srd(b1, F_ * 2);
    const u32 bo = (u32)(r32 * 2);
    ld2(bias1, bo, sb1, (u32)(((s1q ? 0 : cg - 5) * 64 + grp * 32) * 2));
    ld2(bias2, bo, sb1, (u32)(((11 + cg) * 64 + grp * 32) * 2));
    ld2(bias3, bo, sb1, (u32)((27 * 64 + (xcd & 1) * 32) * 2));
  }
  const u32 gsw = (u32)((c8 ^ (((r8 >> 1) & 1) << 2)) * 16);
  u32 goff_q = (u32)(r8 * 2688) + gsw, goff_1 = (u32)(r8 * 3584) + gsw, goff_2 = (u32)(r8 * 896) + gsw;
  const int tq = (lane & 15) >> 2, tp = lane & 3, tg = lane >> 4;
  int trl = ((tg >> 1) * 8 + tq) * 128 + (((2 * (tg & 1) + (tp >> 1)) ^ (((tq >> 1) & 1) << 2)) << 4) + (tp & 1) * 8;
  const int tb1 = 4 * rt + m;
  u32 lo16 = (u32)(lane * 16);
  float gc1 = -0.1029432397f;
  asm volatile("" : "+v"(gc1));
  const int hd = 2 * xcd + (r & 1), tbq = 4 * (15 - (r >> 1)) + m;
  const int c0 = (28 * hd) >> 3;
  const bool odd = hd & 1;
  u32 fo_qk, fo_v;
  {
    const int n = lane >> 1;
    int kbl = tbq - (grp == 0 ? (n == 0 ? 0 : 4 + n) : 1 + n);
    kbl = (kbl < 0 || n >= 5 - grp) ? tbq : kbl;
    const int fbl = (8 * c0 + (lane & 1) * 31) >> 5;
    fo_v = (u32)(WS_FQ + (kbl * 48 + fbl + 28) * 8);
    fo_qk = (u32)(WS_FQ + (kbl * 48 + fbl + (n < 5 ? 14 : 0)) * 8);
  }

  u32 vkeep = r32 == (odd ? 0 : 28) ? 0u : 1u;
  u32 vbase = vkeep ? (u32)(WS_VT + hh * 7168 + (8 * c0 + r32) * 16) : (u32)(WS_ONE + b * 128);
  asm volatile("" : "+v"(vkeep), "+v"(vbase));
  u32 xb[4];
#pragma unroll
  for (int j = 0; j < 4; ++j) xb[j] = (u32)(65536 - grp * 16384 + m * 4096 + (2 * j + hh) * 512 + ((r32 ^ (2 * j + hh)) << 4));
  asm volatile("" : "+v"(goff_q), "+v"(goff_1), "+v"(goff_2), "+v"(trl), "+v"(lo16), "+v"(fo_qk), "+v"(fo_v));
  asm volatile("" : "+v"(xb[0]), "+v"(xb[1]), "+v"(xb[2]), "+v"(xb[3]));
  {
    u32 sel = ((lane & 15) == 4 * (lane >> 4)) ? 0x3f803f80u : 0u;
    v4u asum = {sel, sel, sel, sel};
    const int l3 = lane & 3;
    u32 dm0 = l3 == 0 ? 0xffffu : (l3 == 1 ? 0xffff0000u : 0u), dm1 = l3 == 2 ? 0xffffu : (l3 == 3 ? 0xffff0000u : 0u);
    u32 dsto[2][2];
#pragma unroll
    for (int g = 0; g < 2; ++g) {
      dsto[g][0] = (u32)(49152 + (w >> 1) * 4096 + c8 * 512 + (((16 * (w & 1) + 8 * g + r8) ^ c8) << 4));
      dsto[g][1] = dsto[g][0] + 65536u;
    }
    asm volatile("" : "+v"(asum), "+v"(dm0), "+v"(dm1), "+v"(dsto[0][0]), "+v"(dsto[0][1]), "+v"(dsto[1][0]), "+v"(dsto[1][1]));
#pragma unroll
    for (int g = 0; g < 2; ++g) {
      if (g == 0) vm_wait<11>(); else vm_wait<4>();
#pragma unroll
      for (int i = 0; i < 7; ++i) pin(xr[g * 7 + i]);
      if (g == 0) STAMP(1); else STAMP(2);
      f32x4 s1 = {0.f, 0.f, 0.f, 0.f}, qa = s1, qb = s1;
#pragma unroll
      for (int i = 0; i < 7; ++i) {
        const v4u d = xr[g * 7 + i];
        const s16x4 lo = __builtin_bit_cast(s16x4, v2u{d[0], d[1]}), hi = __builtin_bit_cast(s16x4, v2u{d[2], d[3]});
        s1 = __builtin_amdgcn_mfma_f32_16x16x32_bf16(__builtin_bit_cast(bf16x8, asum), __builtin_bit_cast(bf16x8, d), s1, 0, 0, 0);
        qa = __builtin_amdgcn_mfma_f32_4x4x4bf16_1k(lo, lo, qa, 0, 0, 0);
        qb = __builtin_amdgcn_mfma_f32_4x4x4bf16_1k(hi, hi, qb, 0, 0, 0);
      }
      asm volatile("" : "+v"(s1), "+v"(qa), "+v"(qb));
      qa += qb;
      const float q01 = (l3 & 1) ? qa[1] : qa[0], q23 = (l3 & 1) ? qa[3] : qa[2];
      const float mean = red8(s1[0]) * (1.f / 448.f);
      const float ex2 = red8((l3 & 2) ? q23 : q01) * (1.f / 448.f);
      const float var = __builtin_fmaxf(__builtin_fmaf(-mean, mean, ex2), 0.f);
      const float rstd = __builtin_amdgcn_rsqf(var + 1e-5f);
      const u32 rr = pk(rstd, rstd);
      const float nc = -mean * __builtin_bit_cast(float, rr << 16);
      const f32x4 cc = {nc, nc, nc, nc};
      const s16x4 an = __builtin_bit_cast(s16x4, v2u{rr & dm0, rr & dm1});
#pragma unroll
      for (int i = 0; i < 7; ++i) {
        const v4u d = xr[g * 7 + i];
        const f32x4 y0 = __builtin_amdgcn_mfma_f32_4x4x4bf16_1k(an, __builtin_bit_cast(s16x4, v2u{d[0], d[1]}), cc, 0, 0, 0);
        const f32x4 y1 = __builtin_amdgcn_mfma_f32_4x4x4bf16_1k(an, __builtin_bit_cast(s16x4, v2u{d[2], d[3]}), cc, 0, 0, 0);
        *(LAS v4u*)(L + (i == 6 ? dsto[g][0] : (i < 3 ? dsto[g][0] + 16384 * (i + 1) : dsto[g][1] + 16384 * (i - 3)))) = v4u{pk(y0[0], y0[1]), pk(y0[2], y0[3]), pk(y1[0], y1[1]), pk(y1[2], y1[3])};
      }
    }
  }
  STAMP(3);
  if (grp == 0) {
#pragma unroll 1
    for (int i = 0; i < 3; ++i)
      load_pieces(lds0 + (u32)(16384 * i + 4096 * m), goff_q, sqkv, (u32)((128 * i + 32 * m) * 2688 + cg * 128), 1024u, 8u * 2688u, 4);
  }
  if (threadIdx.x == 256) st16c<0>(v4u{0x3f803f80u, 0x3f803f80u, 0x3f803f80u, 0x3f803f80u}, 0u, ws, (u32)(WS_ONE + b * 128));
  Stream g;
  wg_barrier();
  STAMP(4);
  v4u xa[28];
  const int xlast = grp == 0 ? -16384 : 6 * 16384;
#define RDX(s) xa[s] = *(LAS v4u*)(L + xb[(s) & 3] + (((s) >> 2) == 6 ? xlast : 16384 * ((s) >> 2)))
#pragma unroll
  for (int s = 0; s < 4; ++s) RDX(s);
  const u32 st1 = s1q ? 2688u : 3584u;
  const int strip1 = s1q ? 16 + cg : cg - 5;
  g.srd = s1q ? sqkv : sw1; g.voff = s1q ? goff_q : goff_1; g.s8 = 8 * st1;
  if (grp == 1) {
    vm_wait<0>();
    wg_barrier();
    load_cells(lds0 + (u32)(57344 + m * 2048), g.voff, g.srd, (u32)(m * 16) * st1 + (u32)(strip1 * 128), st1, 1);
  }
#pragma unroll
  for (int s = 4; s < 16; ++s) RDX(s);
  if (grp == 1) asm volatile("s_waitcnt lgkmcnt(0)" ::: "memory");
#pragma unroll
  for (int s = 16; s < 28; ++s) RDX(s);
#undef RDX
  STAMP(5);
  const bool grpb = grp == 1;
  u32 q1 = (u32)((trl ^ (grp * 64)) + (grpb ? 49152 : 0)), q2 = (u32)((trl ^ (grp * 64)) + (grpb ? 0 : 8192)),
      q3 = (u32)((trl ^ (grp * 64)) + (grpb ? 40960 : 49152));
  u32 vst = (u32)(hh * 7168 + r32 * 16);
  Stream g0;
  g0.srd = sw1; g0.voff = goff_1; g0.s8 = 8 * 3584; g0.lds = lds0 + (u32)(114688 + 4096 * m);
  g0.soff = (u32)(32 * m * 3584 + (11 + cg) * 128); g0.n = grpb ? 12 : 0;
  asm volatile("" : "+v"(q1), "+v"(q2), "+v"(q3), "+v"(vst));
  keep_s(g0.lds); keep_s(g0.soff); keep_s(g0.n);
  if (!grpb) {
    vm_wait<0>();
    STAMP(6);
    wg_barrier();
    load_cells(lds0 + (u32)(49152 + m * 2048), goff_q, sqkv, (u32)((384 + m * 16) * 2688 + cg * 128), 2688u, 1);
  } else {
    g.soff = (u32)(64 + 32 * m) * st1 + (u32)(strip1 * 128); g.lds = lds0 + (u32)(65536 + 4096 * m); g.n = 12;
    stream_run<0, 6>(g);
    STAMP(6);
    wg_barrier();
    stream_run<6, 12>(g);
  }
  pin(tag); pin(bias1); pin(bias2); pin(bias3);
  STAMP(8);
  f32x16 acc;
  v2u fpoll;
  u32 pend;
  bool pending;
  int blk;
  if (cg >= 14) {
    acc = gemm1_block<0, true, false>(xa, L + q1, L + q2, L + q3, 0u, g0, grpb, false, false, 0u, tag, ws);
    STAMP(9);
    store_v(acc, vst, ws, (u32)(WS_VT + tb1 * VT_TB + (2 * cg + grp - 28) * 512));
  } else {
    acc = gemm1_block<0, false, false>(xa, L + q1, L + q2, L + q3, 0u, g0, grpb, false, false, 0u, tag, ws);
    STAMP(9);
    v4u o0, o1;
    pack_tile(acc, o0, o1);
    const u32 so = (u32)(WS_QK + tb1 * QK_TB + (2 * cg + grp) * 2048);
    st16c<0>(o0, lo16, ws, so);
    st16c<1024>(o1, lo16, ws, so);
  }
  { const u64 t = (((u64)tag[1] << 32) | tag[0]) + 1; tag[0] = (u32)t; tag[1] = (u32)(t >> 32); }
  pend = (u32)(WS_FQ + (tb1 * 48 + 2 * cg + grp) * 8);
  const u32 st2 = heavy ? 3584u : 896u;
  g.srd = heavy ? sw1 : sw2; g.voff = heavy ? goff_1 : goff_2; g.s8 = 8 * st2;
  const u32 so2 = (u32)(32 * m) * st2 + (u32)((heavy ? 27 : ct) * 128);
  g.soff = so2 + (u32)(grp * 128) * st2; g.lds = lds0 + (u32)(grp * 16384 + 4096 * m); g.n = 4;
  {
    u32 p1 = q1 + 57344u, p2 = q2 + 57344u, p3 = q3 + 57344u;
    u32 tl = lds0 + (u32)(49152 + m * 2048), ts = (u32)((384 + m * 16) * 3584 + (11 + cg) * 128);
    asm volatile("" : "+v"(p1), "+v"(p2), "+v"(p3), "+v"(tag), "+v"(g.voff));
    keep_s(g.lds); keep_s(g.soff); keep_s(g.s8); keep_s(pend); keep_s(tl); keep_s(ts);
    STAMP(10);
    if (!grpb) {
      wg_barrier();
      load_cells(tl, goff_1, sw1, ts, 3584u, 1);
    }
    STAMP(12);
    if (s1q) {
      acc = gemm1_block<1, true, false>(xa, L + p1, L + p2, L + p3, 0u, g, grpb, true, false, pend, tag, ws);
      STAMP(13);
      store_v(acc, vst, ws, (u32)(WS_VT + tb1 * VT_TB + (2 * cg + grp + 4) * 512));
    } else {
      acc = gemm1_block<1, false, true>(xa, L + p1, L + p2, L + p3, bias1, g, grpb, true, false, pend, tag, ws);
      STAMP(13);
      store_gelu(acc, gc1, lo16, ws, (u32)(WS_HH + tb1 * HH_TB + (2 * cg - 10 + grp) * 2048));
    }
  }
  pend = (u32)((s1q ? WS_FQ + (tb1 * 48 + 32) * 8 : WS_FH + (tb1 * 64 - 10) * 8) + (2 * cg + grp) * 8);
  {
    g.soff = so2 + (grpb ? 256u : 384u) * st2; g.lds = lds0 + (u32)((grpb ? 4 : 7) * 8192 + 4096 * m); g.n = (grpb || heavy) ? 4 : 8;
    u32 p1 = q1 + (grpb ? 0u : 114688u), p2 = q2 + 114688u, p3 = q3 + (grpb ? 114688u : 0u);
    asm volatile("" : "+v"(p1), "+v"(p2), "+v"(p3));
    keep_s(g.lds); keep_s(g.soff); keep_s(g.n); keep_s(pend);
    STAMP(14);
    if (!grpb) wg_barrier();
    STAMP(16);
    u64 xst[5];
    acc = gemm1_block<2, false, true>(xa, L + p1, L + p2, L + p3, bias2, g, grpb, s1q, heavy, pend, tag, ws, xst);
    STAMP(17);
    pending = !s1q;
    blk = 22 + 2 * cg + grp;
  }
  if (heavy && !grpb) {
    vm_wait<0>();
    post_flag(pend, tag, ws);
    store_gelu(acc, gc1, lo16, ws, (u32)(WS_HH + tb1 * HH_TB + blk * 2048));
    pend = (u32)(WS_FH + (tb1 * 64 + blk) * 8);
    blk = 54 + (xcd & 1);
    const u32 t = (u32)(trl ^ ((xcd & 1) * 64));
    wg_barrier();
    STAMP(20);
    acc = gemm1_block<3, false, true>(xa, L + t, L + t + 8192, L + t + 57344, bias3, g, false, false, false, pend, tag, ws);
    STAMP(21);
  }

  {
    int kb[5];
#pragma unroll
    for (int n = 0; n < 5; ++n) kb[n] = tbq - (grp == 0 ? (n == 0 ? 0 : 4 + n) : 1 + n);
    v4u qf[2], kf[5][2], vf[5][2];
    {
      ld8c(fpoll, fo_qk, ws);
      u32 p[8];
      mfma_settle(acc);
      gelu4(p[0], p[1], acc[0], acc[1], acc[2], acc[3], gc1);
      gelu4(p[2], p[3], acc[4], acc[5], acc[6], acc[7], gc1);
      STAMP(24);
      vm_wait<0>();
      pin(fpoll);
      if (pending) post_flag(pend, tag, ws);
      if (!flags_ok(fpoll, lane < 12, tag)) wait_flags(fo_qk, lane < 12, tag, ws);
      STAMP(25);
      const u32 kbase = (u32)(WS_QK + (56 + c0) * 512);
      ld16c<0>(qf[0], lo16, ws, (u32)(WS_QK + tbq * QK_TB + c0 * 512));
      ld16c<1024>(qf[1], lo16, ws, (u32)(WS_QK + tbq * QK_TB + c0 * 512));
#define LDK(n) { const u32 so_ = kbase + (u32)((kb[n] < 0 ? tbq : kb[n]) * QK_TB); ld16c<0>(kf[n][0], lo16, ws, so_); ld16c<1024>(kf[n][1], lo16, ws, so_); }
      LDK(0)
      gelu4(p[4], p[5], acc[8], acc[9], acc[10], acc[11], gc1);
      LDK(1) LDK(2)
      gelu4(p[6], p[7], acc[12], acc[13], acc[14], acc[15], gc1);
      LDK(3)
      if (grp == 0) LDK(4)
#undef LDK
      v4u o0, o1;
      swap_tile(p, o0, o1);
      const u32 so = (u32)(WS_HH + tb1 * HH_TB + blk * 2048);
      st16c<0>(o0, lo16, ws, so);
      st16c<1024>(o1, lo16, ws, so);
      pend = (u32)(WS_FH + (tb1 * 64 + blk) * 8);
    }
    STAMP(26);
    f32x16 md, mw;
    {
      const u32 ninf = grp == 0 ? 0xff800000u : 0u;
      const int dd = r32 - 4 * hh;
#pragma unroll
      for (int i = 0; i < 16; ++i) {
        const u32 t = (u32)((dd - ((i & 3) + 8 * (i >> 2))) >> 31) & ninf;
        md[i] = __builtin_bit_cast(float, t);
        mw[i] = __builtin_bit_cast(float, t ^ 0xff800000u);
      }
    }
    asm volatile("" : "+v"(md), "+v"(mw));
    if (grp == 0 && !heavy) wg_barrier();
    ld8c(fpoll, fo_v, ws);
    STAMP(32);
    CSTAMP(61);
    vm_wait<3>();
    pin(qf[0]); pin(qf[1]);
#pragma unroll
    for (int n = 0; n < 5; ++n) { pin(kf[n][0]); pin(kf[n][1]); }
    STAMP(33);
    {
      const u32 k0 = (odd && hh == 0) ? 0u : ~0u, k1 = (!odd && hh == 1) ? 0u : ~0u;
      qf[0][0] &= k0; qf[0][1] &= k0; qf[1][2] &= k1; qf[1][3] &= k1;
    }
    f32x16 sc[5];
    f32x16 zero;
#pragma unroll
    for (int i = 0; i < 16; ++i) zero[i] = 0.f;
    float mx = -1e30f;
#pragma unroll
    for (int n = 0; n < 4; ++n) {
      sc[n] = mfma(kf[n][0], qf[0], n == 0 ? md : zero); sc[n] = mfma(kf[n][1], qf[1], sc[n]);
      __builtin_amdgcn_sched_barrier(0);
    }
    if (grp == 0) { sc[4] = mfma(kf[4][0], qf[0], mw); sc[4] = mfma(kf[4][1], qf[1], sc[4]); }
    float gate[5];
#pragma unroll
    for (int n = 0; n < 5; ++n) gate[n] = kb[n] < 0 ? -__builtin_inff() : 0.f;
    mfma_settle(sc[0]);
    mx = __builtin_fmaxf(mx, max16(sc[0]) + gate[0]);
    mx = __builtin_fmaxf(mx, max16(sc[1]) + gate[1]);
    mx = __builtin_fmaxf(mx, max16(sc[2]) + gate[2]);
    mfma_settle(sc[3]);
    mx = __builtin_fmaxf(mx, max16(sc[3]) + gate[3]);
    if (grp == 0) { mfma_settle(sc[4]); mx = __builtin_fmaxf(mx, max16(sc[4]) + gate[4]); }
    mx = __builtin_fmaxf(mx, swap32(mx));
    float cs = 0.18898223650461362f * 1.4426950408889634f;
    asm volatile("" : "+v"(cs));
    const float nb = -mx * cs;
    float nbn[5];
#pragma unroll
    for (int n = 0; n < 5; ++n) nbn[n] = nb + gate[n];
    v4u pf[5][2];
    pf[0][0] = exp8(sc[0], 0, cs, nbn[0]); pf[0][1] = exp8(sc[0], 8, cs, nbn[0]);
    pf[1][0] = exp8(sc[1], 0, cs, nbn[1]); pf[1][1] = exp8(sc[1], 8, cs, nbn[1]);
    pin(pf[0][0]); pin(pf[0][1]); pin(pf[1][0]); pin(pf[1][1]);
    STAMP(34);
    vm_wait<0>();
    pin(fpoll);
    post_flag(pend, tag, ws);
    if (!flags_ok(fpoll, lane < 10, tag)) wait_flags(fo_v, lane < 10, tag, ws);
    STAMP(35);
    {
#define LDV(n) { const u32 so_ = (u32)((kb[n] < 0 ? tbq : kb[n]) * VT_TB); \
      ld16c<0>(vf[n][0], mad24(vkeep, so_, vbase), ws, 0u); ld16c<0>(vf[n][1], mad24(vkeep, so_ + 14336u, vbase), ws, 0u); \
      __builtin_amdgcn_sched_barrier(0); }
      LDV(0) LDV(1)
      pf[2][0] = exp8(sc[2], 0, cs, nbn[2]); pin(pf[2][0]);
      LDV(2)
      pf[2][1] = exp8(sc[2], 8, cs, nbn[2]); pin(pf[2][1]);
      LDV(3)
      pf[3][0] = exp8(sc[3], 0, cs, nbn[3]); pin(pf[3][0]);
      if (grp == 0) LDV(4)
      pf[3][1] = exp8(sc[3], 8, cs, nbn[3]); pin(pf[3][1]);
#undef LDV
    }
    if (grp == 0) { pf[4][0] = exp8(sc[4], 0, cs, nbn[4]); pf[4][1] = exp8(sc[4], 8, cs, nbn[4]); pin(pf[4][0]); pin(pf[4][1]); }
    STAMP(36);
    vm_wait<0>();
#pragma unroll
    for (int n = 0; n < 5; ++n) { pin(vf[n][0]); pin(vf[n][1]); }
    f32x16 o = zero;
#pragma unroll
    for (int n = 0; n < 4; ++n) { o = mfma(vf[n][0], pf[n][0], o); o = mfma(vf[n][1], pf[n][1], o); }
    LAS char* sp = L + 17 * 8192 + m * 4096 + lane * 16;
    LAS float* mp = (LAS float*)(L + 6 * 8192 + m * 256 + lane * 4);
    if (grp == 0) {
      o = mfma(vf[4][0], pf[4][0], o); o = mfma(vf[4][1], pf[4][1], o);
#pragma unroll
      for (int q = 0; q < 4; ++q) *(LAS f32x4*)(sp + q * 1024) = f32x4{o[4 * q], o[4 * q + 1], o[4 * q + 2], o[4 * q + 3]};
      *mp = nb;
    }
    STAMP(37);
    wg_barrier();
    STAMP(38);
    if (grp == 1) {
      const float na = *mp;
      const float nm = __builtin_fminf(na, nb);
      const float fa = __builtin_amdgcn_exp2f(nm - na), fb = __builtin_amdgcn_exp2f(nm - nb);
#pragma unroll
      for (int q = 0; q < 4; ++q) {
        const f32x4 t = *(LAS f32x4*)(sp + q * 1024);
#pragma unroll
        for (int j = 0; j < 4; ++j) o[4 * q + j] = o[4 * q + j] * fb + t[j] * fa;
      }
      const float lown = odd ? o[0] : o[12], loth = swap32(lown);
      const float rl = __builtin_amdgcn_rcpf((odd ? hh == 0 : hh == 1) ? lown : loth);
#pragma unroll
      for (int i = 0; i < 16; ++i) o[i] *= rl;
      v4u o0, o1;
      pack_tile(o, o0, o1);
      const u32 so = (u32)(WS_HH + tbq * HH_TB + (224 + c0) * 512);
      if (odd) {
        st16c<1024>(o1, lo16, ws, so);
        if (hh == 1) st16c<0>(o0, lo16, ws, so); else st8c<8>(v2u{o0[2], o0[3]}, lo16, ws, so);
      } else {
        st16c<0>(o0, lo16, ws, so);
        if (hh == 0) st16c<1024>(o1, lo16, ws, so); else st8c<1024>(v2u{o1[0], o1[1]}, lo16, ws, so);
      }
    }
    pend = (u32)(WS_FO + (tbq * 16 + hd) * 8);
    STAMP(39);
  }

  if (heavy) {
    if (grp == 1) { vm_wait<0>(); post_flag(pend, tag, ws); }
    if (b == 255 && w == 0) {
#pragma unroll 1
      for (u32 spins = 0; spins < SPIN_CAP; ++spins) {
        v4u f[8];
        ld16c<0>(f[0], lo16, ws, (u32)WS_FO); ld16c<1024>(f[1], lo16, ws, (u32)WS_FO);
        ld16c<2048>(f[2], lo16, ws, (u32)WS_FO); ld16c<3072>(f[3], lo16, ws, (u32)WS_FO);
        ld16c<0>(f[4], lo16, ws, (u32)(WS_FO + 4096)); ld16c<1024>(f[5], lo16, ws, (u32)(WS_FO + 4096));
        ld16c<2048>(f[6], lo16, ws, (u32)(WS_FO + 4096)); ld16c<3072>(f[7], lo16, ws, (u32)(WS_FO + 4096));
        vm_wait<0>();
        bool ok = true;
#pragma unroll
        for (int i = 0; i < 8; ++i) { pin(f[i]); ok = ok && f[i][0] == tag[0] && f[i][1] == tag[1] && f[i][2] == tag[0] && f[i][3] == tag[1]; }
        if (__all(ok)) break;
        __builtin_amdgcn_s_sleep(8);
      }
      post_flag((u32)WS_E, tag, ws);
    }
    STAMP_OUT();
    return;
  }

  {
    const int kq = m, mh = grp, tb = 2 * tt + mh;
    const __attribute__((address_space(4))) u64* ka = (const __attribute__((address_space(4))) u64*)__builtin_amdgcn_kernarg_segment_ptr();
    asm volatile("" : "+s"(ka));
    const v4i sx2 = make_srd((const void*)ka[0], S_ * D_ * 2), swo = make_srd((const void*)ka[2], D_ * D_ * 2);
    const v4i sb2 = make_srd((const void*)ka[6], D_ * 2), sout = make_srd((const void*)ka[7], S_ * D_ * 2);
    auto cell_of = [&](int pos) { const int i = 4 * pos + kq; return i < 6 ? i : (i < 19 ? i + 1 : 6); };
    const u32 fo_h = (u32)(WS_FH + (tb * 64 + ((lane >> 1) * 4 + kq) * 2 + (lane & 1)) * 8);
    ld8c(fpoll, fo_h, ws);
    load_pieces(lds0 + (u32)(11 * 8192 + w * 1024), goff_2, sw2, (u32)((640 + w * 8) * 896 + ct * 128), 8192u, 64u * 896u, 6);
    v4u xres, bres;
    const u32 oo = (u32)((tb * 32 + r32) * 896 + (ct * 64 + kq * 16 + hh * 8) * 2);
    ld16<0>(xres, oo, sx2, 0u);
    ld16<0>(bres, (u32)(hh * 16), sb2, (u32)((ct * 64 + kq * 16) * 2));
    vm_wait<8>();
    pin(fpoll);
    if (grp == 1) post_flag(pend, tag, ws);
    if (!flags_ok(fpoll, lane < 14, tag)) wait_flags(fo_h, lane < 14, tag, ws);
    STAMP(40);
    v4u hf[35];
    const u32 hb = (u32)(WS_HH + tb * HH_TB + kq * 4096);
#define LDH1(j, s) ld16c<(s) * 1024>(hf[4 * (j) + (s)], lo16, ws, (j) < 8 ? hb + (j) * 16384 : hb + 8 * 16384 - kq * 1024)
#pragma unroll
    for (int j = 0; j < 2; ++j) { LDH1(j, 0); LDH1(j, 1); LDH1(j, 2); LDH1(j, 3); }
    f32x16 acc0, acc1;
#pragma unroll
    for (int i = 0; i < 16; ++i) { acc0[i] = 0.f; acc1[i] = 0.f; }
    const u32 fo_o = (u32)(WS_FO + (tb * 16 + (lane & 15)) * 8);
#define STAGE(j, WAIT) { \
      constexpr int ns = (j) == 8 ? 3 : 4; \
      vm_wait<WAIT>(); \
      pin(hf[4 * (j)]); pin(hf[4 * (j) + 1]); pin(hf[4 * (j) + 2]); if (ns == 4) pin(hf[4 * (j) + (ns - 1)]); \
      if ((j) == 5) { pin(fpoll); if (!flags_ok(fpoll, true, tag)) wait_flags(fo_o, true, tag, ws); } \
      wg_barrier(); \
      STAMP(41 + (j)); \
      LAS char* p0 = L + cell_of((j) % 5) * 8192 + trl; \
      LAS char* p1 = L + cell_of((j) % 5) * 8192 + (trl ^ 64); \
      v4u wa[4], wb[4]; \
      wa[0] = tr_frag(p0); wb[0] = tr_frag(p1); wa[1] = tr_frag(p0 + 2048); wb[1] = tr_frag(p1 + 2048); \
      wa[2] = tr_frag(p0 + 4096); wb[2] = tr_frag(p1 + 4096); \
      if (ns == 4) { wa[3] = tr_frag(p0 + 6144); wb[3] = tr_frag(p1 + 6144); } \
      u32 gl = lds0 + (u32)(cell_of(((j) + 4) % 5) * 8192 + mh * ((j) == 4 ? 3072 : 4096)); \
      u32 gs = (u32)(((j) < 3 ? 256 * ((j) + 4) + 64 * kq + 32 * mh : ((j) == 3 ? 64 * kq + 32 * mh : 256 + 48 * kq + 24 * mh)) * 896 + ct * 128); \
      __builtin_amdgcn_sched_barrier(0); \
      if ((j) <= 6) LDH1(((j) <= 6 ? (j) + 2 : 0), 0); \
      acc0 = mfma(wa[0], hf[4 * (j)], acc0); __builtin_amdgcn_sched_barrier(0); \
      if ((j) <= 6) LDH1(((j) <= 6 ? (j) + 2 : 0), 1); \
      acc1 = mfma(wb[0], hf[4 * (j)], acc1); __builtin_amdgcn_sched_barrier(0); \
      if ((j) <= 6) LDH1(((j) <= 6 ? (j) + 2 : 0), 2); \
      acc0 = mfma(wa[1], hf[4 * (j) + 1], acc0); __builtin_amdgcn_sched_barrier(0); \
      if ((j) <= 5) LDH1(((j) <= 5 ? (j) + 2 : 0), 3); \
      acc1 = mfma(wb[1], hf[4 * (j) + 1], acc1); __builtin_amdgcn_sched_barrier(0); \
      if ((j) <= 4) glds16(gl, goff_2, (j) < 3 ? sw2 : swo, gs); \
      acc0 = mfma(wa[2], hf[4 * (j) + 2], acc0); __builtin_amdgcn_sched_barrier(0); \
      if ((j) <= 4) glds16(gl + 1024, goff_2, (j) < 3 ? sw2 : swo, gs + 7168); \
      acc1 = mfma(wb[2], hf[4 * (j) + 2], acc1); __builtin_amdgcn_sched_barrier(0); \
      if ((j) <= 4) glds16(gl + 2048, goff_2, (j) < 3 ? sw2 : swo, gs + 14336); \
      if (ns == 4) { acc0 = mfma(wa[3], hf[4 * (j) + (ns - 1)], acc0); __builtin_amdgcn_sched_barrier(0); } \
      if ((j) <= 3) glds16(gl + 3072, goff_2, (j) < 3 ? sw2 : swo, gs + 21504); \
      if ((j) == 3) ld8c(fpoll, fo_o, ws); \
      if (ns == 4) { acc1 = mfma(wb[3], hf[4 * (j) + (ns - 1)], acc1); __builtin_amdgcn_sched_barrier(0); } \
    }
    STAGE(0, 4) STAGE(1, 8) STAGE(2, 12) STAGE(3, 12) STAGE(4, 13) STAGE(5, 7) STAGE(6, 7) STAGE(7, 3) STAGE(8, 0)
#undef STAGE
#undef LDH1
    STAMP(50);
    {
      LAS char* rp = L + (w < 6 ? w : w + 1) * 8192 + lane * 16;
#pragma unroll
      for (int q = 0; q < 4; ++q) {
        *(LAS f32x4*)(rp + q * 1024) = f32x4{acc0[4 * q], acc0[4 * q + 1], acc0[4 * q + 2], acc0[4 * q + 3]};
        *(LAS f32x4*)(rp + 4096 + q * 1024) = f32x4{acc1[4 * q], acc1[4 * q + 1], acc1[4 * q + 2], acc1[4 * q + 3]};
      }
    }
    wg_barrier();
    STAMP(51);
    pin(xres); pin(bres);
    f32x4 a = {0.f, 0.f, 0.f, 0.f}, c = {0.f, 0.f, 0.f, 0.f};
#pragma unroll
    for (int k = 0; k < 4; ++k) {
      const int ww = 4 * mh + k;
      LAS char* rp = L + (ww < 6 ? ww : ww + 1) * 8192 + kq * 2048 + hh * 1024 + r32 * 16;
      a += *(LAS f32x4*)rp;
      c += *(LAS f32x4*)(rp + 512);
    }
    auto lo_f = [](u32 d) { return __builtin_bit_cast(float, d << 16); };
    auto hi_f = [](u32 d) { return __builtin_bit_cast(float, d & 0xffff0000u); };
    v4u o;
    o[0] = pk(a[0] + lo_f(xres[0]) + lo_f(bres[0]), a[1] + hi_f(xres[0]) + hi_f(bres[0]));
    o[1] = pk(a[2] + lo_f(xres[1]) + lo_f(bres[1]), a[3] + hi_f(xres[1]) + hi_f(bres[1]));
    o[2] = pk(c[0] + lo_f(xres[2]) + lo_f(bres[2]), c[1] + hi_f(xres[2]) + hi_f(bres[2]));
    o[3] = pk(c[2] + lo_f(xres[3]) + lo_f(bres[3]), c[3] + hi_f(xres[3]) + hi_f(bres[3]));
    st16(o, oo, sout);
    STAMP(52);
    CSTAMP(62);
    STAMP_OUT();
  }
}

extern "C" void kernel_launch(void* const* d_in, const int* in_sizes, int n_in, void* d_out, int out_size, void* d_ws, size_t ws_size,
                              hipStream_t stream) {
  (void)hipFuncSetAttribute((const void*)fused_block, hipFuncAttributeMaxDynamicSharedMemorySize, LDS_BYTES);
  fused_block<<<256, 512, LDS_BYTES, stream>>>((const u16*)d_in[0], (const u16*)d_in[1], (const u16*)d_in[2], (const u16*)d_in[4],
                                               (const u16*)d_in[5], (const u16*)d_in[6], (const u16*)d_in[7], (u16*)d_out, (char*)d_ws);
}
```

```cpp
#define STAMPS 1
#define PROBE_I 59
#define PROBE_J 0
#define PROBE_K 10
#define PROBE_W 0
#include <hip/hip_runtime.h>

typedef unsigned short u16;
typedef unsigned int u32;
typedef unsigned long long u64;
typedef u32 v4u __attribute__((ext_vector_type(4)));
typedef u32 v2u __attribute__((ext_vector_type(2)));
typedef int v4i __attribute__((ext_vector_type(4)));
typedef short s16x4 __attribute__((ext_vector_type(4)));
typedef __bf16 bf16x8 __attribute__((ext_vector_type(8)));
typedef __bf16 bf16x2 __attribute__((ext_vector_type(2)));
typedef float f32x2 __attribute__((ext_vector_type(2)));
typedef float f32x4 __attribute__((ext_vector_type(4)));
typedef float f32x16 __attribute__((ext_vector_type(16)));
#define GAS __attribute__((address_space(1)))
#define LAS __attribute__((address_space(3)))
#define DI __device__ __forceinline__

#define EXP_NOSTREAM 0
#define EXP_XST 0

constexpr int S_ = 2048, D_ = 448, F_ = 1792;
constexpr int WS_E = 0;
constexpr int WS_FQ = 4096;
constexpr int WS_FH = WS_FQ + 64 * 48 * 8;
constexpr int WS_FO = WS_FH + 64 * 64 * 8;
constexpr int WS_ONE = 1 << 19;
constexpr int WS_QK = 1 << 20;
constexpr int QK_TB = 112 * 512;
constexpr int WS_VT = WS_QK + 64 * QK_TB;
constexpr int VT_TB = 4 * 448 * 16;
constexpr int WS_HH = WS_VT + 64 * VT_TB;
constexpr int HH_TB = 280 * 512;
constexpr int WS_END = WS_HH + 64 * HH_TB;
constexpr int WS_DBG = 32 << 20;
constexpr int LDS_BYTES = 163840;
constexpr u32 SPIN_CAP = 1u << 16;

DI v4i make_srd(const void* p, u32 bytes) {
  const u64 a = (u64)p;
  v4i r;
  r[0] = (int)(u32)a; r[1] = (int)((u32)(a >> 32) & 0xffffu); r[2] = (int)bytes; r[3] = 0x00020000;
  return r;
}
template <int OFF> DI void ld16(v4u& d, u32 voff, v4i srd, u32 soff) {
  asm volatile("buffer_load_dwordx4 %0, %1, %2, %3 offen offset:%4" : "=v"(d) : "v"(voff), "s"(srd), "s"(soff), "i"(OFF) : "memory");
}
template <int OFF> DI void ld16c(v4u& d, u32 voff, v4i srd, u32 soff) {
  asm volatile("buffer_load_dwordx4 %0, %1, %2, %3 offen offset:%4 sc1" : "=v"(d) : "v"(voff), "s"(srd), "s"(soff), "i"(OFF) : "memory");
}
DI void ld8c(v2u& d, u32 voff, v4i srd) {
  asm volatile("buffer_load_dwordx2 %0, %1, %2, 0 offen sc1" : "=v"(d) : "v"(voff), "s"(srd) : "memory");
}
DI void ld2(u32& d, u32 voff, v4i srd, u32 soff) {
  asm volatile("buffer_load_ushort %0, %1, %2, %3 offen" : "=v"(d) : "v"(voff), "s"(srd), "s"(soff) : "memory");
}
DI void glds16(u32 lds, u32 voff, v4i srd, u32 soff) {
  asm volatile("s_mov_b32 m0, %0\n\ts_nop 0\n\tbuffer_load_dwordx4 %1, %2, %3 offen lds" ::"s"(lds), "v"(voff), "s"(srd), "s"(soff) : "memory");
}
DI void load_cells(u32 lds, u32 voff, v4i srd, u32 soff, u32 stride, int n) {
#pragma unroll 1
  for (int c = 0; c < n; ++c) {
    u32 t;
    asm volatile("s_mov_b32 m0, %1\n\ts_add_u32 %0, %4, %5\n\tbuffer_load_dwordx4 %2, %3, %4 offen lds\n\t"
                 "s_add_u32 m0, %1, 0x400\n\ts_nop 0\n\tbuffer_load_dwordx4 %2, %3, %0 offen lds"
                 : "=&s"(t) : "s"(lds), "v"(voff), "s"(srd), "s"(soff), "s"(stride * 8) : "memory", "scc");
    lds += 8192; soff += stride * 64;
  }
}
DI void load_pieces(u32 lds, u32 voff, v4i srd, u32 soff, u32 lstep, u32 sstep, int n) {
#pragma unroll 1
  for (int c = 0; c < n; ++c) { glds16(lds, voff, srd, soff); lds += lstep; soff += sstep; }
}
template <int OFF> DI void st16c(v4u d, u32 voff, v4i srd, u32 soff) {
  asm volatile("buffer_store_dwordx4 %0, %1, %2, %3 offen offset:%4 sc1\n\ts_nop 1" ::"v"(d), "v"(voff), "s"(srd), "s"(soff), "i"(OFF) : "memory");
}
template <int OFF> DI void st8c(v2u d, u32 voff, v4i srd, u32 soff) {
  asm volatile("buffer_store_dwordx2 %0, %1, %2, %3 offen offset:%4 sc1" ::"v"(d), "v"(voff), "s"(srd), "s"(soff), "i"(OFF) : "memory");
}
DI void st16(v4u d, u32 voff, v4i srd) {
  asm volatile("buffer_store_dwordx4 %0, %1, %2, 0 offen\n\ts_nop 1" ::"v"(d), "v"(voff), "s"(srd) : "memory");
}
template <int N> DI void vm_wait() { asm volatile("s_waitcnt vmcnt(%0)" ::"i"(N) : "memory"); }
DI void pin(v4u& a) { asm volatile("" : "+v"(a)); }
DI void pin(v2u& a) { asm volatile("" : "+v"(a)); }
DI void pin(u32& a) { asm volatile("" : "+v"(a)); }
DI void keep_s(u32& a) { a = __builtin_amdgcn_readfirstlane(a); asm volatile("" : "+s"(a)); }
DI void wg_barrier() { asm volatile("s_waitcnt lgkmcnt(0)\n\ts_barrier" ::: "memory"); }
DI f32x16 mfma(v4u a, v4u b, f32x16 c) {
  return __builtin_amdgcn_mfma_f32_32x32x16_bf16(__builtin_bit_cast(bf16x8, a), __builtin_bit_cast(bf16x8, b), c, 0, 0, 0);
}
DI u32 pk(float a, float b) { f32x2 f = {a, b}; return __builtin_bit_cast(u32, __builtin_convertvector(f, bf16x2)); }
DI v4u tr_frag(LAS char* p) {
  const s16x4 a = __builtin_amdgcn_ds_read_tr16_b64_v4i16((LAS s16x4*)p);
  const s16x4 b = __builtin_amdgcn_ds_read_tr16_b64_v4i16((LAS s16x4*)(p + 512));
  const v2u x = __builtin_bit_cast(v2u, a), y = __builtin_bit_cast(v2u, b);
  return v4u{x[0], x[1], y[0], y[1]};
}
template <int C> DI float dpp_add(float x) {
  return x + __builtin_bit_cast(float, __builtin_amdgcn_update_dpp(0, __builtin_bit_cast(int, x), C, 0xf, 0xf, true));
}
DI float red8(float x) { x = dpp_add<0xB1>(x); x = dpp_add<0x4E>(x); return dpp_add<0x141>(x); }
DI float swap32(float x) {
  const u32 u = __builtin_bit_cast(u32, x);
  const auto r = __builtin_amdgcn_permlane32_swap(u, u, false, false);
  return __builtin_bit_cast(float, (threadIdx.x & 32) ? r[0] : r[1]);
}
DI void pack_tile(const f32x16& a, v4u& o0, v4u& o1) {
  u32 p[8];
#pragma unroll
  for (int i = 0; i < 8; ++i) p[i] = pk(a[2 * i], a[2 * i + 1]);
  const auto r0 = __builtin_amdgcn_permlane32_swap(p[0], p[2], false, false);
  const auto r1 = __builtin_amdgcn_permlane32_swap(p[1], p[3], false, false);
  const auto r2 = __builtin_amdgcn_permlane32_swap(p[4], p[6], false, false);
  const auto r3 = __builtin_amdgcn_permlane32_swap(p[5], p[7], false, false);
  o0 = v4u{r0[0], r1[0], r0[1], r1[1]};
  o1 = v4u{r2[0], r3[0], r2[1], r3[1]};
}
DI void wait_flags(u32 voff, bool active, v2u tag, v4i ws) {
#pragma unroll 1
  for (u32 spins = 0; spins < SPIN_CAP; ++spins) {
    v2u f;
    ld8c(f, voff, ws);
    vm_wait<0>();
    pin(f);
    const bool ok = !active || (f[0] == tag[0] && f[1] == tag[1]);
    if (__all(ok)) break;
    __builtin_amdgcn_s_sleep(2);
  }
}
DI void post_flag(u32 off, v2u tag, v4i ws) {
  asm volatile("s_mov_b64 exec, 1\n\tbuffer_store_dwordx2 %0, off, %1, %2 sc1\n\ts_mov_b64 exec, -1" ::"v"(tag), "s"(ws), "s"(off) : "memory");
}
#define STAMP(i) do { u64 t_; __builtin_amdgcn_sched_barrier(0); asm volatile("s_memrealtime %0\n\ts_waitcnt lgkmcnt(0)" : "=s"(t_) :: "memory"); \
  asm volatile("s_mov_b32 m0, %2\n\ts_nop 0\n\tv_writelane_b32 %0, %1, m0" : "+v"(stv_) : "s"((u32)t_), "s"((u32)(i))); __builtin_amdgcn_sched_barrier(0); } while (0)
#define CSTAMP(i) do { u64 t_; asm volatile("s_memtime %0\n\ts_waitcnt lgkmcnt(0)" : "=s"(t_) :: "memory"); \
  asm volatile("s_mov_b32 m0, %2\n\ts_nop 0\n\tv_writelane_b32 %0, %1, m0" : "+v"(stv_) : "s"((u32)t_), "s"((u32)(i))); } while (0)
#define XSTAMP(i) asm volatile("s_memtime %0" : "=s"(xst[(i) - 53]) :: "memory")
#define STAMP_DECL u32 stv_ = 0
#define STAMP_OUT() do { if (blockIdx.x == 100 && w == PROBE_W) { \
  const u32 d_ = (u32)__builtin_amdgcn_readlane(stv_, PROBE_J) - (u32)__builtin_amdgcn_readlane(stv_, PROBE_I); \
  const u32 t0_ = (u32)__builtin_amdgcn_s_memrealtime(); \
  for (int i_ = 0; i_ < 1000000; ++i_) { if ((u32)__builtin_amdgcn_s_memrealtime() - t0_ >= 1000 + PROBE_K * d_) break; __builtin_amdgcn_s_sleep(1); } } } while (0)

struct Stream { v4i srd; u32 voff, soff, s8, lds, n; };
template <int P, bool COND> DI void stream_piece(const Stream& g) {
  u32 t;
  if constexpr (COND)
    asm volatile("s_cmp_gt_u32 %6, %9\n\ts_cbranch_scc0 1f\n\ts_mul_i32 %0, %5, %8\n\ts_add_u32 m0, %1, %7\n\ts_add_u32 %0, %0, %2\n\t"
                 "buffer_load_dwordx4 %3, %4, %0 offen lds\n1:"
                 : "=&s"(t) : "s"(g.lds), "s"(g.soff), "v"(g.voff), "s"(g.srd), "s"(g.s8), "s"(g.n),
                   "i"((P >> 2) * 16384 + (P & 3) * 1024), "i"((P >> 2) * 16 + (P & 3)), "i"(P) : "memory", "scc");
  else
    asm volatile("s_mul_i32 %0, %5, %7\n\ts_add_u32 m0, %1, %6\n\ts_add_u32 %0, %0, %2\n\tbuffer_load_dwordx4 %3, %4, %0 offen lds"
                 : "=&s"(t) : "s"(g.lds), "s"(g.soff), "v"(g.voff), "s"(g.srd), "s"(g.s8),
                   "i"((P >> 2) * 16384 + (P & 3) * 1024), "i"((P >> 2) * 16 + (P & 3)) : "memory", "scc");
}
template <int LO, int HI> DI void stream_run(const Stream& g) {
  if constexpr (LO < HI) { stream_piece<LO, false>(g); stream_run<LO + 1, HI>(g); }
}
DI u32 mad24(u32 a, u32 s, u32 c) { u32 d; asm("v_mad_u32_u24 %0, %1, %2, %3" : "=v"(d) : "v"(a), "s"(s), "v"(c)); return d; }
DI bool flags_ok(v2u f, bool active, v2u tag) { return __all(!active || (f[0] == tag[0] && f[1] == tag[1])); }

template <int SLOT, bool SWAP, bool BIAS>
DI f32x16 gemm1_block(const v4u (&xa)[28], LAS char* p1, LAS char* p2, LAS char* p3, u32 bias, const Stream& g, bool grpb, bool post, bool heavy,
                      u32 pend, v2u tag, v4i ws, u64* xst = nullptr) {
  constexpr int PD = 5;
  constexpr int NP = SLOT == 0 ? 12 : (SLOT == 1 ? 4 : (SLOT == 2 ? 8 : 0)), NU = SLOT == 0 ? 0 : 4;
  auto rd = [&](int s) { return s < 4 ? tr_frag(p1 + s * 2048) : (s < 24 ? tr_frag(p2 + (s - 4) * 2048) : tr_frag(p3 + (s - 24) * 2048)); };
  __builtin_amdgcn_s_setprio(1);
  v4u wf[PD];
#pragma unroll
  for (int s = 0; s < PD; ++s) wf[s] = rd(s);
  f32x16 acc;
#pragma unroll
  for (int i = 0; i < 16; ++i) acc[i] = 0.f;
  if constexpr (BIAS) {
    const v4u bf = {bias, 0u, 0u, 0u};
    const v4u one = {(threadIdx.x & 32) ? 0u : 0x3f80u, 0u, 0u, 0u};
    acc = mfma(bf, one, acc);
  }
  __builtin_amdgcn_sched_barrier(0);
#define G1STEP(s) { \
    const v4u cur = wf[(s) % PD]; \
    if ((s) == 19) { \
      if (SLOT == 0) { if (!grpb) { vm_wait<0>(); asm volatile("s_barrier" ::: "memory"); } } \
      else if (SLOT == 1) { vm_wait<4>(); post_flag(pend, tag, ws); } \
      else if (SLOT == 2) { if (post) { vm_wait<4>(); post_flag(pend, tag, ws); } } \
    } \
    if ((s) == 24 && SLOT < 3) { \
      if (grpb) { \
        if (SLOT == 0) vm_wait<12>(); \
        if (SLOT == 2) { if (heavy) vm_wait<0>(); } \
        asm volatile("s_barrier" ::: "memory"); \
        __builtin_amdgcn_s_setprio(2); \
      } \
    } \
    acc = SWAP ? mfma(xa[s], cur, acc) : mfma(cur, xa[s], acc); \
    if ((s) + PD < 28) wf[(s) % PD] = rd((s) + PD); \
    if (((s) & 1) == 0 && (s) / 2 < NP && !(EXP_NOSTREAM && SLOT > 0)) stream_piece<(s) / 2, ((s) / 2 >= NU)>(g); \
    if (SLOT == 2 && EXP_XST && ((s) == 0 || (s) == 6 || (s) == 12 || (s) == 18 || (s) == 23)) XSTAMP(53 + ((s) + 1) / 6); \
    __builtin_amdgcn_sched_barrier(0); }
  G1STEP(0) G1STEP(1) G1STEP(2) G1STEP(3) G1STEP(4) G1STEP(5) G1STEP(6) G1STEP(7) G1STEP(8) G1STEP(9)
  G1STEP(10) G1STEP(11) G1STEP(12) G1STEP(13) G1STEP(14) G1STEP(15) G1STEP(16) G1STEP(17) G1STEP(18) G1STEP(19)
  G1STEP(20) G1STEP(21) G1STEP(22) G1STEP(23) G1STEP(24) G1STEP(25) G1STEP(26) G1STEP(27)
#undef G1STEP
  __builtin_amdgcn_s_setprio(0);
  return acc;
}

DI void gelu4(u32& o0, u32& o1, float a, float b, float c, float d, float c1) {
  float t0, t1, t2, t3;
  asm volatile(
      "v_mul_f32 %2, %6, %6\n\tv_mul_f32 %3, %7, %7\n\tv_mul_f32 %4, %8, %8\n\tv_mul_f32 %5, %9, %9\n\t"
      "v_fmaak_f32 %2, %2, %10, 0xc0135761\n\tv_fmaak_f32 %3, %3, %10, 0xc0135761\n\t"
      "v_fmaak_f32 %4, %4, %10, 0xc0135761\n\tv_fmaak_f32 %5, %5, %10, 0xc0135761\n\t"
      "v_mul_f32 %2, %2, %6\n\tv_mul_f32 %3, %3, %7\n\tv_mul_f32 %4, %4, %8\n\tv_mul_f32 %5, %5, %9\n\t"
      "v_exp_f32 %2, %2\n\tv_exp_f32 %3, %3\n\tv_exp_f32 %4, %4\n\tv_exp_f32 %5, %5\n\t"
      "v_add_f32 %2, 1.0, %2\n\tv_add_f32 %3, 1.0, %3\n\tv_add_f32 %4, 1.0, %4\n\tv_add_f32 %5, 1.0, %5\n\t"
      "v_rcp_f32 %2, %2\n\tv_rcp_f32 %3, %3\n\tv_rcp_f32 %4, %4\n\tv_rcp_f32 %5, %5\n\t"
      "v_mul_f32 %2, %2, %6\n\tv_mul_f32 %3, %3, %7\n\tv_mul_f32 %4, %4, %8\n\tv_mul_f32 %5, %5, %9\n\t"
      "v_cvt_pk_bf16_f32 %0, %2, %3\n\tv_cvt_pk_bf16_f32 %1, %4, %5\n\ts_nop 1"
      : "=&v"(o0), "=&v"(o1), "=&v"(t0), "=&v"(t1), "=&v"(t2), "=&v"(t3) : "v"(a), "v"(b), "v"(c), "v"(d), "v"(c1));
}
DI void swap_tile(const u32 (&p)[8], v4u& o0, v4u& o1) {
  const auto r0 = __builtin_amdgcn_permlane32_swap(p[0], p[2], false, false);
  const auto r1 = __builtin_amdgcn_permlane32_swap(p[1], p[3], false, false);
  const auto r2 = __builtin_amdgcn_permlane32_swap(p[4], p[6], false, false);
  const auto r3 = __builtin_amdgcn_permlane32_swap(p[5], p[7], false, false);
  o0 = v4u{r0[0], r1[0], r0[1], r1[1]};
  o1 = v4u{r2[0], r3[0], r2[1], r3[1]};
}
DI void mfma_settle(const f32x16& a) { asm volatile("s_nop 7\n\ts_nop 4" ::"v"(a)); }
DI void store_v(const f32x16& a, u32 vo, v4i ws, u32 so) {
  st16c<0>(v4u{pk(a[0], a[1]), pk(a[2], a[3]), pk(a[4], a[5]), pk(a[6], a[7])}, vo, ws, so);
  st16c<0>(v4u{pk(a[8], a[9]), pk(a[10], a[11]), pk(a[12], a[13]), pk(a[14], a[15])}, vo, ws, so + 14336u);
}
DI void store_gelu(const f32x16& a, float gc1, u32 lo16, v4i ws, u32 so) {
  u32 p[8];
  mfma_settle(a);
#pragma unroll
  for (int i = 0; i < 4; ++i) gelu4(p[2 * i], p[2 * i + 1], a[4 * i], a[4 * i + 1], a[4 * i + 2], a[4 * i + 3], gc1);
  v4u o0, o1;
  swap_tile(p, o0, o1);
  st16c<0>(o0, lo16, ws, so);
  st16c<1024>(o1, lo16, ws, so);
}
DI float max16(const f32x16& s) {
  float a, b;
  asm volatile("v_max3_f32 %0, %2, %3, %4\n\tv_max3_f32 %1, %5, %6, %7\n\tv_max3_f32 %0, %0, %8, %9\n\tv_max3_f32 %1, %1, %10, %11\n\t"
               "v_max3_f32 %0, %0, %12, %13\n\tv_max3_f32 %1, %1, %14, %15\n\tv_max3_f32 %0, %0, %16, %17\n\tv_max_f32 %0, %0, %1"
               : "=&v"(a), "=&v"(b) : "v"(s[0]), "v"(s[1]), "v"(s[2]), "v"(s[3]), "v"(s[4]), "v"(s[5]), "v"(s[6]), "v"(s[7]), "v"(s[8]),
                 "v"(s[9]), "v"(s[10]), "v"(s[11]), "v"(s[12]), "v"(s[13]), "v"(s[14]), "v"(s[15]));
  return a;
}
DI v4u exp8(const f32x16& s, int o, float cs, float nb) {
  v4u p;
#pragma unroll
  for (int j = 0; j < 4; ++j)
    p[j] = pk(__builtin_amdgcn_exp2f(__builtin_fmaf(s[o + 2 * j], cs, nb)), __builtin_amdgcn_exp2f(__builtin_fmaf(s[o + 2 * j + 1], cs, nb)));
  return p;
}

__global__ void __launch_bounds__(512, 2)
fused_block(const u16* __restrict__ x_, const u16* __restrict__ wqkv_, const u16* __restrict__ wo_, const u16* __restrict__ w1_,
            const u16* __restrict__ b1_, const u16* __restrict__ w2_, const u16* __restrict__ b2_, u16* __restrict__ out_, char* wsp_) {
  const __attribute__((address_space(4))) u64* ka0 = (const __attribute__((address_space(4))) u64*)__builtin_amdgcn_kernarg_segment_ptr();
  u64 tfirst; asm volatile("s_memrealtime %0\n\ts_waitcnt lgkmcnt(0)" : "=s"(tfirst), "+s"(ka0) :: "memory");
  const u16* x = (const u16*)ka0[0]; const u16* wqkv = (const u16*)ka0[1]; const u16* w1 = (const u16*)ka0[3]; const u16* b1 = (const u16*)ka0[4];
  const u16* w2 = (const u16*)ka0[5]; char* wsp = (char*)ka0[8];
  asm volatile("s_waitcnt lgkmcnt(0)" :: "s"(x), "s"(wsp) : "memory");
  extern __shared__ __attribute__((aligned(16))) char smem_[];
  LAS char* const L = (LAS char*)smem_;
  const u32 lds0 = (u32)(size_t)L;
  const int lane = threadIdx.x & 63;
  const int w = __builtin_amdgcn_readfirstlane(threadIdx.x >> 6);
  const int m = w & 3, grp = w >> 2;
  const int b = blockIdx.x, xcd = b & 7, r = b >> 3;
  const int r32 = lane & 31, hh = lane >> 5;
  const v4i ws = make_srd(wsp, WS_END);
  const v4i sx = make_srd(x, S_ * D_ * 2);
  const v4i sqkv = make_srd(wqkv, D_ * 3 * D_ * 2);
  const v4i sw1 = make_srd(w1, D_ * F_ * 2);
  const v4i sw2 = make_srd(w2, F_ * D_ * 2);
  STAMP_DECL;
  STAMP(0);
  asm volatile("s_mov_b32 m0, %2\n\ts_nop 0\n\tv_writelane_b32 %0, %1, m0" : "+v"(stv_) : "s"((u32)tfirst), "s"((u32)(59)));
  CSTAMP(60);

  const int rt = 4 * (xcd >> 1) + (r & 3);
  const int cg = 8 * (xcd & 1) + (r >> 2);
  const bool heavy = r >= 28;
  const int tt = 4 * xcd + (r & 3), ct = r >> 2;
  const bool s1q = cg < 5;
  const int r8 = lane >> 3, c8 = lane & 7;

  v2u tag;
  v4u xr[14];
  {
    const u32 xo = (u32)((rt * 128 + 16 * w + r8) * 896 + c8 * 16);
#define LDX(g, i) ld16<(i) * 128>(xr[(g) * 7 + (i)], xo, sx, (u32)((g) * 7168))
    LDX(0, 0); LDX(0, 1); LDX(0, 2); LDX(0, 3); LDX(0, 4); LDX(0, 5); LDX(0, 6);
    LDX(1, 0); LDX(1, 1); LDX(1, 2); LDX(1, 3); LDX(1, 4); LDX(1, 5); LDX(1, 6);
#undef LDX
  }
  ld8c(tag, 0u, ws);
  u32 bias1, bias2, bias3;
  {
    const v4i sb1 = make_srd(b1, F_ * 2);
    const u32 bo = (u32)(r32 * 2);
    ld2(bias1, bo, sb1, (u32)(((s1q ? 0 : cg - 5) * 64 + grp * 32) * 2));
    ld2(bias2, bo, sb1, (u32)(((11 + cg) * 64 + grp * 32) * 2));
    ld2(bias3, bo, sb1, (u32)((27 * 64 + (xcd & 1) * 32) * 2));
  }
  const u32 gsw = (u32)((c8 ^ (((r8 >> 1) & 1) << 2)) * 16);
  u32 goff_q = (u32)(r8 * 2688) + gsw, goff_1 = (u32)(r8 * 3584) + gsw, goff_2 = (u32)(r8 * 896) + gsw;
  const int tq = (lane & 15) >> 2, tp = lane & 3, tg = lane >> 4;
  int trl = ((tg >> 1) * 8 + tq) * 128 + (((2 * (tg & 1) + (tp >> 1)) ^ (((tq >> 1) & 1) << 2)) << 4) + (tp & 1) * 8;
  const int tb1 = 4 * rt + m;
  u32 lo16 = (u32)(lane * 16);
  float gc1 = -0.1029432397f;
  asm volatile("" : "+v"(gc1));
  const int hd = 2 * xcd + (r & 1), tbq = 4 * (15 - (r >> 1)) + m;
  const int c0 = (28 * hd) >> 3;
  const bool odd = hd & 1;
  u32 fo_qk, fo_v;
  {
    const int n = lane >> 1;
    int kbl = tbq - (grp == 0 ? (n == 0 ? 0 : 4 + n) : 1 + n);
    kbl = (kbl < 0 || n >= 5 - grp) ? tbq : kbl;
    const int fbl = (8 * c0 + (lane & 1) * 31) >> 5;
    fo_v = (u32)(WS_FQ + (kbl * 48 + fbl + 28) * 8);
    fo_qk = (u32)(WS_FQ + (kbl * 48 + fbl + (n < 5 ? 14 : 0)) * 8);
  }

  u32 vkeep = r32 == (odd ? 0 : 28) ? 0u : 1u;
  u32 vbase = vkeep ? (u32)(WS_VT + hh * 7168 + (8 * c0 + r32) * 16) : (u32)(WS_ONE + b * 128);
  asm volatile("" : "+v"(vkeep), "+v"(vbase));
  u32 xb[4];
#pragma unroll
  for (int j = 0; j < 4; ++j) xb[j] = (u32)(65536 - grp * 16384 + m * 4096 + (2 * j + hh) * 512 + ((r32 ^ (2 * j + hh)) << 4));
  asm volatile("" : "+v"(goff_q), "+v"(goff_1), "+v"(goff_2), "+v"(trl), "+v"(lo16), "+v"(fo_qk), "+v"(fo_v));
  asm volatile("" : "+v"(xb[0]), "+v"(xb[1]), "+v"(xb[2]), "+v"(xb[3]));
  {
    u32 sel = ((lane & 15) == 4 * (lane >> 4)) ? 0x3f803f80u : 0u;
    v4u asum = {sel, sel, sel, sel};
    const int l3 = lane & 3;
    u32 dm0 = l3 == 0 ? 0xffffu : (l3 == 1 ? 0xffff0000u : 0u), dm1 = l3 == 2 ? 0xffffu : (l3 == 3 ? 0xffff0000u : 0u);
    u32 dsto[2][2];
#pragma unroll
    for (int g = 0; g < 2; ++g) {
      dsto[g][0] = (u32)(49152 + (w >> 1) * 4096 + c8 * 512 + (((16 * (w & 1) + 8 * g + r8) ^ c8) << 4));
      dsto[g][1] = dsto[g][0] + 65536u;
    }
    asm volatile("" : "+v"(asum), "+v"(dm0), "+v"(dm1), "+v"(dsto[0][0]), "+v"(dsto[0][1]), "+v"(dsto[1][0]), "+v"(dsto[1][1]));
#pragma unroll
    for (int g = 0; g < 2; ++g) {
      if (g == 0) vm_wait<11>(); else vm_wait<4>();
#pragma unroll
      for (int i = 0; i < 7; ++i) pin(xr[g * 7 + i]);
      if (g == 0) STAMP(1); else STAMP(2);
      f32x4 s1 = {0.f, 0.f, 0.f, 0.f}, qa = s1, qb = s1;
#pragma unroll
      for (int i = 0; i < 7; ++i) {
        const v4u d = xr[g * 7 + i];
        const s16x4 lo = __builtin_bit_cast(s16x4, v2u{d[0], d[1]}), hi = __builtin_bit_cast(s16x4, v2u{d[2], d[3]});
        s1 = __builtin_amdgcn_mfma_f32_16x16x32_bf16(__builtin_bit_cast(bf16x8, asum), __builtin_bit_cast(bf16x8, d), s1, 0, 0, 0);
        qa = __builtin_amdgcn_mfma_f32_4x4x4bf16_1k(lo, lo, qa, 0, 0, 0);
        qb = __builtin_amdgcn_mfma_f32_4x4x4bf16_1k(hi, hi, qb, 0, 0, 0);
      }
      asm volatile("" : "+v"(s1), "+v"(qa), "+v"(qb));
      qa += qb;
      const float q01 = (l3 & 1) ? qa[1] : qa[0], q23 = (l3 & 1) ? qa[3] : qa[2];
      const float mean = red8(s1[0]) * (1.f / 448.f);
      const float ex2 = red8((l3 & 2) ? q23 : q01) * (1.f / 448.f);
      const float var = __builtin_fmaxf(__builtin_fmaf(-mean, mean, ex2), 0.f);
      const float rstd = __builtin_amdgcn_rsqf(var + 1e-5f);
      const u32 rr = pk(rstd, rstd);
      const float nc = -mean * __builtin_bit_cast(float, rr << 16);
      const f32x4 cc = {nc, nc, nc, nc};
      const s16x4 an = __builtin_bit_cast(s16x4, v2u{rr & dm0, rr & dm1});
#pragma unroll
      for (int i = 0; i < 7; ++i) {
        const v4u d = xr[g * 7 + i];
        const f32x4 y0 = __builtin_amdgcn_mfma_f32_4x4x4bf16_1k(an, __builtin_bit_cast(s16x4, v2u{d[0], d[1]}), cc, 0, 0, 0);
        const f32x4 y1 = __builtin_amdgcn_mfma_f32_4x4x4bf16_1k(an, __builtin_bit_cast(s16x4, v2u{d[2], d[3]}), cc, 0, 0, 0);
        *(LAS v4u*)(L + (i == 6 ? dsto[g][0] : (i < 3 ? dsto[g][0] + 16384 * (i + 1) : dsto[g][1] + 16384 * (i - 3)))) = v4u{pk(y0[0], y0[1]), pk(y0[2], y0[3]), pk(y1[0], y1[1]), pk(y1[2], y1[3])};
      }
    }
  }
  STAMP(3);
  if (grp == 0) {
#pragma unroll 1
    for (int i = 0; i < 3; ++i)
      load_pieces(lds0 + (u32)(16384 * i + 4096 * m), goff_q, sqkv, (u32)((128 * i + 32 * m) * 2688 + cg * 128), 1024u, 8u * 2688u, 4);
  }
  if (threadIdx.x == 256) st16c<0>(v4u{0x3f803f80u, 0x3f803f80u, 0x3f803f80u, 0x3f803f80u}, 0u, ws, (u32)(WS_ONE + b * 128));
  Stream g;
  wg_barrier();
  STAMP(4);
  v4u xa[28];
  const int xlast = grp == 0 ? -16384 : 6 * 16384;
#define RDX(s) xa[s] = *(LAS v4u*)(L + xb[(s) & 3] + (((s) >> 2) == 6 ? xlast : 16384 * ((s) >> 2)))
#pragma unroll
  for (int s = 0; s < 4; ++s) RDX(s);
  const u32 st1 = s1q ? 2688u : 3584u;
  const int strip1 = s1q ? 16 + cg : cg - 5;
  g.srd = s1q ? sqkv : sw1; g.voff = s1q ? goff_q : goff_1; g.s8 = 8 * st1;
  if (grp == 1) {
    vm_wait<0>();
    wg_barrier();
    load_cells(lds0 + (u32)(57344 + m * 2048), g.voff, g.srd, (u32)(m * 16) * st1 + (u32)(strip1 * 128), st1, 1);
  }
#pragma unroll
  for (int s = 4; s < 16; ++s) RDX(s);
  if (grp == 1) asm volatile("s_waitcnt lgkmcnt(0)" ::: "memory");
#pragma unroll
  for (int s = 16; s < 28; ++s) RDX(s);
#undef RDX
  STAMP(5);
  const bool grpb = grp == 1;
  u32 q1 = (u32)((trl ^ (grp * 64)) + (grpb ? 49152 : 0)), q2 = (u32)((trl ^ (grp * 64)) + (grpb ? 0 : 8192)),
      q3 = (u32)((trl ^ (grp * 64)) + (grpb ? 40960 : 49152));
  u32 vst = (u32)(hh * 7168 + r32 * 16);
  Stream g0;
  g0.srd = sw1; g0.voff = goff_1; g0.s8 = 8 * 3584; g0.lds = lds0 + (u32)(114688 + 4096 * m);
  g0.soff = (u32)(32 * m * 3584 + (11 + cg) * 128); g0.n = grpb ? 12 : 0;
  asm volatile("" : "+v"(q1), "+v"(q2), "+v"(q3), "+v"(vst));
  keep_s(g0.lds); keep_s(g0.soff); keep_s(g0.n);
  if (!grpb) {
    vm_wait<0>();
    STAMP(6);
    wg_barrier();
    load_cells(lds0 + (u32)(49152 + m * 2048), goff_q, sqkv, (u32)((384 + m * 16) * 2688 + cg * 128), 2688u, 1);
  } else {
    g.soff = (u32)(64 + 32 * m) * st1 + (u32)(strip1 * 128); g.lds = lds0 + (u32)(65536 + 4096 * m); g.n = 12;
    stream_run<0, 6>(g);
    STAMP(6);
    wg_barrier();
    stream_run<6, 12>(g);
  }
  pin(tag); pin(bias1); pin(bias2); pin(bias3);
  STAMP(8);
  f32x16 acc;
  v2u fpoll;
  u32 pend;
  bool pending;
  int blk;
  if (cg >= 14) {
    acc = gemm1_block<0, true, false>(xa, L + q1, L + q2, L + q3, 0u, g0, grpb, false, false, 0u, tag, ws);
    STAMP(9);
    store_v(acc, vst, ws, (u32)(WS_VT + tb1 * VT_TB + (2 * cg + grp - 28) * 512));
  } else {
    acc = gemm1_block<0, false, false>(xa, L + q1, L + q2, L + q3, 0u, g0, grpb, false, false, 0u, tag, ws);
    STAMP(9);
    v4u o0, o1;
    pack_tile(acc, o0, o1);
    const u32 so = (u32)(WS_QK + tb1 * QK_TB + (2 * cg + grp) * 2048);
    st16c<0>(o0, lo16, ws, so);
    st16c<1024>(o1, lo16, ws, so);
  }
  { const u64 t = (((u64)tag[1] << 32) | tag[0]) + 1; tag[0] = (u32)t; tag[1] = (u32)(t >> 32); }
  pend = (u32)(WS_FQ + (tb1 * 48 + 2 * cg + grp) * 8);
  const u32 st2 = heavy ? 3584u : 896u;
  g.srd = heavy ? sw1 : sw2; g.voff = heavy ? goff_1 : goff_2; g.s8 = 8 * st2;
  const u32 so2 = (u32)(32 * m) * st2 + (u32)((heavy ? 27 : ct) * 128);
  g.soff = so2 + (u32)(grp * 128) * st2; g.lds = lds0 + (u32)(grp * 16384 + 4096 * m); g.n = 4;
  {
    u32 p1 = q1 + 57344u, p2 = q2 + 57344u, p3 = q3 + 57344u;
    u32 tl = lds0 + (u32)(49152 + m * 2048), ts = (u32)((384 + m * 16) * 3584 + (11 + cg) * 128);
    asm volatile("" : "+v"(p1), "+v"(p2), "+v"(p3), "+v"(tag), "+v"(g.voff));
    keep_s(g.lds); keep_s(g.soff); keep_s(g.s8); keep_s(pend); keep_s(tl); keep_s(ts);
    STAMP(10);
    if (!grpb) {
      wg_barrier();
      load_cells(tl, goff_1, sw1, ts, 3584u, 1);
    }
    STAMP(12);
    if (s1q) {
      acc = gemm1_block<1, true, false>(xa, L + p1, L + p2, L + p3, 0u, g, grpb, true, false, pend, tag, ws);
      STAMP(13);
      store_v(acc, vst, ws, (u32)(WS_VT + tb1 * VT_TB + (2 * cg + grp + 4) * 512));
    } else {
      acc = gemm1_block<1, false, true>(xa, L + p1, L + p2, L + p3, bias1, g, grpb, true, false, pend, tag, ws);
      STAMP(13);
      store_gelu(acc, gc1, lo16, ws, (u32)(WS_HH + tb1 * HH_TB + (2 * cg - 10 + grp) * 2048));
    }
  }
  pend = (u32)((s1q ? WS_FQ + (tb1 * 48 + 32) * 8 : WS_FH + (tb1 * 64 - 10) * 8) + (2 * cg + grp) * 8);
  {
    g.soff = so2 + (grpb ? 256u : 384u) * st2; g.lds = lds0 + (u32)((grpb ? 4 : 7) * 8192 + 4096 * m); g.n = (grpb || heavy) ? 4 : 8;
    u32 p1 = q1 + (grpb ? 0u : 114688u), p2 = q2 + 114688u, p3 = q3 + (grpb ? 114688u : 0u);
    asm volatile("" : "+v"(p1), "+v"(p2), "+v"(p3));
    keep_s(g.lds); keep_s(g.soff); keep_s(g.n); keep_s(pend);
    STAMP(14);
    if (!grpb) wg_barrier();
    STAMP(16);
    u64 xst[5];
    acc = gemm1_block<2, false, true>(xa, L + p1, L + p2, L + p3, bias2, g, grpb, s1q, heavy, pend, tag, ws, xst);
    STAMP(17);
    pending = !s1q;
    blk = 22 + 2 * cg + grp;
  }
  if (heavy && !grpb) {
    vm_wait<0>();
    post_flag(pend, tag, ws);
    store_gelu(acc, gc1, lo16, ws, (u32)(WS_HH + tb1 * HH_TB + blk * 2048));
    pend = (u32)(WS_FH + (tb1 * 64 + blk) * 8);
    blk = 54 + (xcd & 1);
    const u32 t = (u32)(trl ^ ((xcd & 1) * 64));
    wg_barrier();
    STAMP(20);
    acc = gemm1_block<3, false, true>(xa, L + t, L + t + 8192, L + t + 57344, bias3, g, false, false, false, pend, tag, ws);
    STAMP(21);
  }

  {
    int kb[5];
#pragma unroll
    for (int n = 0; n < 5; ++n) kb[n] = tbq - (grp == 0 ? (n == 0 ? 0 : 4 + n) : 1 + n);
    v4u qf[2], kf[5][2], vf[5][2];
    {
      ld8c(fpoll, fo_qk, ws);
      u32 p[8];
      mfma_settle(acc);
      gelu4(p[0], p[1], acc[0], acc[1], acc[2], acc[3], gc1);
      gelu4(p[2], p[3], acc[4], acc[5], acc[6], acc[7], gc1);
      STAMP(24);
      vm_wait<0>();
      pin(fpoll);
      if (pending) post_flag(pend, tag, ws);
      if (!flags_ok(fpoll, lane < 12, tag)) wait_flags(fo_qk, lane < 12, tag, ws);
      STAMP(25);
      const u32 kbase = (u32)(WS_QK + (56 + c0) * 512);
      ld16c<0>(qf[0], lo16, ws, (u32)(WS_QK + tbq * QK_TB + c0 * 512));
      ld16c<1024>(qf[1], lo16, ws, (u32)(WS_QK + tbq * QK_TB + c0 * 512));
#define LDK(n) { const u32 so_ = kbase + (u32)((kb[n] < 0 ? tbq : kb[n]) * QK_TB); ld16c<0>(kf[n][0], lo16, ws, so_); ld16c<1024>(kf[n][1], lo16, ws, so_); }
      LDK(0)
      gelu4(p[4], p[5], acc[8], acc[9], acc[10], acc[11], gc1);
      LDK(1) LDK(2)
      gelu4(p[6], p[7], acc[12], acc[13], acc[14], acc[15], gc1);
      LDK(3)
      if (grp == 0) LDK(4)
#undef LDK
      v4u o0, o1;
      swap_tile(p, o0, o1);
      const u32 so = (u32)(WS_HH + tb1 * HH_TB + blk * 2048);
      st16c<0>(o0, lo16, ws, so);
      st16c<1024>(o1, lo16, ws, so);
      pend = (u32)(WS_FH + (tb1 * 64 + blk) * 8);
    }
    STAMP(26);
    f32x16 md, mw;
    {
      const u32 ninf = grp == 0 ? 0xff800000u : 0u;
      const int dd = r32 - 4 * hh;
#pragma unroll
      for (int i = 0; i < 16; ++i) {
        const u32 t = (u32)((dd - ((i & 3) + 8 * (i >> 2))) >> 31) & ninf;
        md[i] = __builtin_bit_cast(float, t);
        mw[i] = __builtin_bit_cast(float, t ^ 0xff800000u);
      }
    }
    asm volatile("" : "+v"(md), "+v"(mw));
    if (grp == 0 && !heavy) wg_barrier();
    ld8c(fpoll, fo_v, ws);
    STAMP(32);
    CSTAMP(61);
    vm_wait<3>();
    pin(qf[0]); pin(qf[1]);
#pragma unroll
    for (int n = 0; n < 5; ++n) { pin(kf[n][0]); pin(kf[n][1]); }
    STAMP(33);
    {
      const u32 k0 = (odd && hh == 0) ? 0u : ~0u, k1 = (!odd && hh == 1) ? 0u : ~0u;
      qf[0][0] &= k0; qf[0][1] &= k0; qf[1][2] &= k1; qf[1][3] &= k1;
    }
    f32x16 sc[5];
    f32x16 zero;
#pragma unroll
    for (int i = 0; i < 16; ++i) zero[i] = 0.f;
    float mx = -1e30f;
#pragma unroll
    for (int n = 0; n < 4; ++n) {
      sc[n] = mfma(kf[n][0], qf[0], n == 0 ? md : zero); sc[n] = mfma(kf[n][1], qf[1], sc[n]);
      __builtin_amdgcn_sched_barrier(0);
    }
    if (grp == 0) { sc[4] = mfma(kf[4][0], qf[0], mw); sc[4] = mfma(kf[4][1], qf[1], sc[4]); }
    float gate[5];
#pragma unroll
    for (int n = 0; n < 5; ++n) gate[n] = kb[n] < 0 ? -__builtin_inff() : 0.f;
    mfma_settle(sc[0]);
    mx = __builtin_fmaxf(mx, max16(sc[0]) + gate[0]);
    mx = __builtin_fmaxf(mx, max16(sc[1]) + gate[1]);
    mx = __builtin_fmaxf(mx, max16(sc[2]) + gate[2]);
    mfma_settle(sc[3]);
    mx = __builtin_fmaxf(mx, max16(sc[3]) + gate[3]);
    if (grp == 0) { mfma_settle(sc[4]); mx = __builtin_fmaxf(mx, max16(sc[4]) + gate[4]); }
    mx = __builtin_fmaxf(mx, swap32(mx));
    float cs = 0.18898223650461362f * 1.4426950408889634f;
    asm volatile("" : "+v"(cs));
    const float nb = -mx * cs;
    float nbn[5];
#pragma unroll
    for (int n = 0; n < 5; ++n) nbn[n] = nb + gate[n];
    v4u pf[5][2];
    pf[0][0] = exp8(sc[0], 0, cs, nbn[0]); pf[0][1] = exp8(sc[0], 8, cs, nbn[0]);
    pf[1][0] = exp8(sc[1], 0, cs, nbn[1]); pf[1][1] = exp8(sc[1], 8, cs, nbn[1]);
    pin(pf[0][0]); pin(pf[0][1]); pin(pf[1][0]); pin(pf[1][1]);
    STAMP(34);
    vm_wait<0>();
    pin(fpoll);
    post_flag(pend, tag, ws);
    if (!flags_ok(fpoll, lane < 10, tag)) wait_flags(fo_v, lane < 10, tag, ws);
    STAMP(35);
    {
#define LDV(n) { const u32 so_ = (u32)((kb[n] < 0 ? tbq : kb[n]) * VT_TB); \
      ld16c<0>(vf[n][0], mad24(vkeep, so_, vbase), ws, 0u); ld16c<0>(vf[n][1], mad24(vkeep, so_ + 14336u, vbase), ws, 0u); \
      __builtin_amdgcn_sched_barrier(0); }
      LDV(0) LDV(1)
      pf[2][0] = exp8(sc[2], 0, cs, nbn[2]); pin(pf[2][0]);
      LDV(2)
      pf[2][1] = exp8(sc[2], 8, cs, nbn[2]); pin(pf[2][1]);
      LDV(3)
      pf[3][0] = exp8(sc[3], 0, cs, nbn[3]); pin(pf[3][0]);
      if (grp == 0) LDV(4)
      pf[3][1] = exp8(sc[3], 8, cs, nbn[3]); pin(pf[3][1]);
#undef LDV
    }
    if (grp == 0) { pf[4][0] = exp8(sc[4], 0, cs, nbn[4]); pf[4][1] = exp8(sc[4], 8, cs, nbn[4]); pin(pf[4][0]); pin(pf[4][1]); }
    STAMP(36);
    vm_wait<0>();
#pragma unroll
    for (int n = 0; n < 5; ++n) { pin(vf[n][0]); pin(vf[n][1]); }
    f32x16 o = zero;
#pragma unroll
    for (int n = 0; n < 4; ++n) { o = mfma(vf[n][0], pf[n][0], o); o = mfma(vf[n][1], pf[n][1], o); }
    LAS char* sp = L + 17 * 8192 + m * 4096 + lane * 16;
    LAS float* mp = (LAS float*)(L + 6 * 8192 + m * 256 + lane * 4);
    if (grp == 0) {
      o = mfma(vf[4][0], pf[4][0], o); o = mfma(vf[4][1], pf[4][1], o);
#pragma unroll
      for (int q = 0; q < 4; ++q) *(LAS f32x4*)(sp + q * 1024) = f32x4{o[4 * q], o[4 * q + 1], o[4 * q + 2], o[4 * q + 3]};
      *mp = nb;
    }
    STAMP(37);
    wg_barrier();
    STAMP(38);
    if (grp == 1) {
      const float na = *mp;
      const float nm = __builtin_fminf(na, nb);
      const float fa = __builtin_amdgcn_exp2f(nm - na), fb = __builtin_amdgcn_exp2f(nm - nb);
#pragma unroll
      for (int q = 0; q < 4; ++q) {
        const f32x4 t = *(LAS f32x4*)(sp + q * 1024);
#pragma unroll
        for (int j = 0; j < 4; ++j) o[4 * q + j] = o[4 * q + j] * fb + t[j] * fa;
      }
      const float lown = odd ? o[0] : o[12], loth = swap32(lown);
      const float rl = __builtin_amdgcn_rcpf((odd ? hh == 0 : hh == 1) ? lown : loth);
#pragma unroll
      for (int i = 0; i < 16; ++i) o[i] *= rl;
      v4u o0, o1;
      pack_tile(o, o0, o1);
      const u32 so = (u32)(WS_HH + tbq * HH_TB + (224 + c0) * 512);
      if (odd) {
        st16c<1024>(o1, lo16, ws, so);
        if (hh == 1) st16c<0>(o0, lo16, ws, so); else st8c<8>(v2u{o0[2], o0[3]}, lo16, ws, so);
      } else {
        st16c<0>(o0, lo16, ws, so);
        if (hh == 0) st16c<1024>(o1, lo16, ws, so); else st8c<1024>(v2u{o1[0], o1[1]}, lo16, ws, so);
      }
    }
    pend = (u32)(WS_FO + (tbq * 16 + hd) * 8);
    STAMP(39);
  }

  if (heavy) {
    if (grp == 1) { vm_wait<0>(); post_flag(pend, tag, ws); }
    if (b == 255 && w == 0) {
#pragma unroll 1
      for (u32 spins = 0; spins < SPIN_CAP; ++spins) {
        v4u f[8];
        ld16c<0>(f[0], lo16, ws, (u32)WS_FO); ld16c<1024>(f[1], lo16, ws, (u32)WS_FO);
        ld16c<2048>(f[2], lo16, ws, (u32)WS_FO); ld16c<3072>(f[3], lo16, ws, (u32)WS_FO);
        ld16c<0>(f[4], lo16, ws, (u32)(WS_FO + 4096)); ld16c<1024>(f[5], lo16, ws, (u32)(WS_FO + 4096));
        ld16c<2048>(f[6], lo16, ws, (u32)(WS_FO + 4096)); ld16c<3072>(f[7], lo16, ws, (u32)(WS_FO + 4096));
        vm_wait<0>();
        bool ok = true;
#pragma unroll
        for (int i = 0; i < 8; ++i) { pin(f[i]); ok = ok && f[i][0] == tag[0] && f[i][1] == tag[1] && f[i][2] == tag[0] && f[i][3] == tag[1]; }
        if (__all(ok)) break;
        __builtin_amdgcn_s_sleep(8);
      }
      post_flag((u32)WS_E, tag, ws);
    }
    STAMP_OUT();
    return;
  }

  {
    const int kq = m, mh = grp, tb = 2 * tt + mh;
    const __attribute__((address_space(4))) u64* ka = (const __attribute__((address_space(4))) u64*)__builtin_amdgcn_kernarg_segment_ptr();
    asm volatile("" : "+s"(ka));
    const v4i sx2 = make_srd((const void*)ka[0], S_ * D_ * 2), swo = make_srd((const void*)ka[2], D_ * D_ * 2);
    const v4i sb2 = make_srd((const void*)ka[6], D_ * 2), sout = make_srd((const void*)ka[7], S_ * D_ * 2);
    auto cell_of = [&](int pos) { const int i = 4 * pos + kq; return i < 6 ? i : (i < 19 ? i + 1 : 6); };
    const u32 fo_h = (u32)(WS_FH + (tb * 64 + ((lane >> 1) * 4 + kq) * 2 + (lane & 1)) * 8);
    ld8c(fpoll, fo_h, ws);
    load_pieces(lds0 + (u32)(11 * 8192 + w * 1024), goff_2, sw2, (u32)((640 + w * 8) * 896 + ct * 128), 8192u, 64u * 896u, 6);
    v4u xres, bres;
    const u32 oo = (u32)((tb * 32 + r32) * 896 + (ct * 64 + kq * 16 + hh * 8) * 2);
    ld16<0>(xres, oo, sx2, 0u);
    ld16<0>(bres, (u32)(hh * 16), sb2, (u32)((ct * 64 + kq * 16) * 2));
    vm_wait<8>();
    pin(fpoll);
    if (grp == 1) post_flag(pend, tag, ws);
    if (!flags_ok(fpoll, lane < 14, tag)) wait_flags(fo_h, lane < 14, tag, ws);
    STAMP(40);
    v4u hf[35];
    const u32 hb = (u32)(WS_HH + tb * HH_TB + kq * 4096);
#define LDH1(j, s) ld16c<(s) * 1024>(hf[4 * (j) + (s)], lo16, ws, (j) < 8 ? hb + (j) * 16384 : hb + 8 * 16384 - kq * 1024)
#pragma unroll
    for (int j = 0; j < 2; ++j) { LDH1(j, 0); LDH1(j, 1); LDH1(j, 2); LDH1(j, 3); }
    f32x16 acc0, acc1;
#pragma unroll
    for (int i = 0; i < 16; ++i) { acc0[i] = 0.f; acc1[i] = 0.f; }
    const u32 fo_o = (u32)(WS_FO + (tb * 16 + (lane & 15)) * 8);
#define STAGE(j, WAIT) { \
      constexpr int ns = (j) == 8 ? 3 : 4; \
      vm_wait<WAIT>(); \
      pin(hf[4 * (j)]); pin(hf[4 * (j) + 1]); pin(hf[4 * (j) + 2]); if (ns == 4) pin(hf[4 * (j) + (ns - 1)]); \
      if ((j) == 5) { pin(fpoll); if (!flags_ok(fpoll, true, tag)) wait_flags(fo_o, true, tag, ws); } \
      wg_barrier(); \
      STAMP(41 + (j)); \
      LAS char* p0 = L + cell_of((j) % 5) * 8192 + trl; \
      LAS char* p1 = L + cell_of((j) % 5) * 8192 + (trl ^ 64); \
      v4u wa[4], wb[4]; \
      wa[0] = tr_frag(p0); wb[0] = tr_frag(p1); wa[1] = tr_frag(p0 + 2048); wb[1] = tr_frag(p1 + 2048); \
      wa[2] = tr_frag(p0 + 4096); wb[2] = tr_frag(p1 + 4096); \
      if (ns == 4) { wa[3] = tr_frag(p0 + 6144); wb[3] = tr_frag(p1 + 6144); } \
      u32 gl = lds0 + (u32)(cell_of(((j) + 4) % 5) * 8192 + mh * ((j) == 4 ? 3072 : 4096)); \
      u32 gs = (u32)(((j) < 3 ? 256 * ((j) + 4) + 64 * kq + 32 * mh : ((j) == 3 ? 64 * kq + 32 * mh : 256 + 48 * kq + 24 * mh)) * 896 + ct * 128); \
      __builtin_amdgcn_sched_barrier(0); \
      if ((j) <= 6) LDH1(((j) <= 6 ? (j) + 2 : 0), 0); \
      acc0 = mfma(wa[0], hf[4 * (j)], acc0); __builtin_amdgcn_sched_barrier(0); \
      if ((j) <= 6) LDH1(((j) <= 6 ? (j) + 2 : 0), 1); \
      acc1 = mfma(wb[0], hf[4 * (j)], acc1); __builtin_amdgcn_sched_barrier(0); \
      if ((j) <= 6) LDH1(((j) <= 6 ? (j) + 2 : 0), 2); \
      acc0 = mfma(wa[1], hf[4 * (j) + 1], acc0); __builtin_amdgcn_sched_barrier(0); \
      if ((j) <= 5) LDH1(((j) <= 5 ? (j) + 2 : 0), 3); \
      acc1 = mfma(wb[1], hf[4 * (j) + 1], acc1); __builtin_amdgcn_sched_barrier(0); \
      if ((j) <= 4) glds16(gl, goff_2, (j) < 3 ? sw2 : swo, gs); \
      acc0 = mfma(wa[2], hf[4 * (j) + 2], acc0); __builtin_amdgcn_sched_barrier(0); \
      if ((j) <= 4) glds16(gl + 1024, goff_2, (j) < 3 ? sw2 : swo, gs + 7168); \
      acc1 = mfma(wb[2], hf[4 * (j) + 2], acc1); __builtin_amdgcn_sched_barrier(0); \
      if ((j) <= 4) glds16(gl + 2048, goff_2, (j) < 3 ? sw2 : swo, gs + 14336); \
      if (ns == 4) { acc0 = mfma(wa[3], hf[4 * (j) + (ns - 1)], acc0); __builtin_amdgcn_sched_barrier(0); } \
      if ((j) <= 3) glds16(gl + 3072, goff_2, (j) < 3 ? sw2 : swo, gs + 21504); \
      if ((j) == 3) ld8c(fpoll, fo_o, ws); \
      if (ns == 4) { acc1 = mfma(wb[3], hf[4 * (j) + (ns - 1)], acc1); __builtin_amdgcn_sched_barrier(0); } \
    }
    STAGE(0, 4) STAGE(1, 8) STAGE(2, 12) STAGE(3, 12) STAGE(4, 13) STAGE(5, 7) STAGE(6, 7) STAGE(7, 3) STAGE(8, 0)
#undef STAGE
#undef LDH1
    STAMP(50);
    {
      LAS char* rp = L + (w < 6 ? w : w + 1) * 8192 + lane * 16;
#pragma unroll
      for (int q = 0; q < 4; ++q) {
        *(LAS f32x4*)(rp + q * 1024) = f32x4{acc0[4 * q], acc0[4 * q + 1], acc0[4 * q + 2], acc0[4 * q + 3]};
        *(LAS f32x4*)(rp + 4096 + q * 1024) = f32x4{acc1[4 * q], acc1[4 * q + 1], acc1[4 * q + 2], acc1[4 * q + 3]};
      }
    }
    wg_barrier();
    STAMP(51);
    pin(xres); pin(bres);
    f32x4 a = {0.f, 0.f, 0.f, 0.f}, c = {0.f, 0.f, 0.f, 0.f};
#pragma unroll
    for (int k = 0; k < 4; ++k) {
      const int ww = 4 * mh + k;
      LAS char* rp = L + (ww < 6 ? ww : ww + 1) * 8192 + kq * 2048 + hh * 1024 + r32 * 16;
      a += *(LAS f32x4*)rp;
      c += *(LAS f32x4*)(rp + 512);
    }
    auto lo_f = [](u32 d) { return __builtin_bit_cast(float, d << 16); };
    auto hi_f = [](u32 d) { return __builtin_bit_cast(float, d & 0xffff0000u); };
    v4u o;
    o[0] = pk(a[0] + lo_f(xres[0]) + lo_f(bres[0]), a[1] + hi_f(xres[0]) + hi_f(bres[0]));
    o[1] = pk(a[2] + lo_f(xres[1]) + lo_f(bres[1]), a[3] + hi_f(xres[1]) + hi_f(bres[1]));
    o[2] = pk(c[0] + lo_f(xres[2]) + lo_f(bres[2]), c[1] + hi_f(xres[2]) + hi_f(bres[2]));
    o[3] = pk(c[2] + lo_f(xres[3]) + lo_f(bres[3]), c[3] + hi_f(xres[3]) + hi_f(bres[3]));
    st16(o, oo, sout);
    STAMP(52);
    CSTAMP(62);
    STAMP_OUT();
  }
}

extern "C" void kernel_launch(void* const* d_in, const int* in_sizes, int n_in, void* d_out, int out_size, void* d_ws, size_t ws_size,
                              hipStream_t stream) {
  (void)hipFuncSetAttribute((const void*)fused_block, hipFuncAttributeMaxDynamicSharedMemorySize, LDS_BYTES);
  fused_block<<<256, 512, LDS_BYTES, stream>>>((const u16*)d_in[0], (const u16*)d_in[1], (const u16*)d_in[2], (const u16*)d_in[4],
                                               (const u16*)d_in[5], (const u16*)d_in[6], (const u16*)d_in[7], (u16*)d_out, (char*)d_ws);
}
```

```cpp
#include <hip/hip_runtime.h>
#include <type_traits>

typedef unsigned short u16;
typedef unsigned int u32;
typedef unsigned long long u64;
typedef u32 v4u __attribute__((ext_vector_type(4)));
typedef u32 v2u __attribute__((ext_vector_type(2)));
typedef int v4i __attribute__((ext_vector_type(4)));
typedef short s16x4 __attribute__((ext_vector_type(4)));
typedef __bf16 bf16x8 __attribute__((ext_vector_type(8)));
typedef __bf16 bf16x2 __attribute__((ext_vector_type(2)));
typedef float f32x2 __attribute__((ext_vector_type(2)));
typedef float f32x4 __attribute__((ext_vector_type(4)));
typedef float f32x16 __attribute__((ext_vector_type(16)));
#define GAS __attribute__((address_space(1)))
#define LAS __attribute__((address_space(3)))
#define DI __device__ __forceinline__

#define APRIO 2
#define TOUCH 7
#define STAMPS 0

constexpr int S_ = 2048, D_ = 448, F_ = 1792;
constexpr int WS_E = 0;
constexpr int WS_F = 4096;
constexpr int WS_FO = WS_F + 64 * 1024;
constexpr int WS_QK = 1 << 20;
constexpr int QK_TB = 112 * 512;
constexpr int WS_VT = WS_QK + 64 * QK_TB;
constexpr int VT_TB = 4 * 448 * 16;
constexpr int WS_HH = WS_VT + 64 * VT_TB;
constexpr int HH_TB = 280 * 512;
constexpr int WS_END = WS_HH + 64 * HH_TB;
constexpr int WS_DBG = 32 << 20;
constexpr int LDS_BYTES = 163840;
constexpr int CODE_LINES = 260;
constexpr u32 SPIN_CAP = 1u << 16;

DI v4i make_srd(const void* p, u32 bytes) {
  const u64 a = (u64)p;
  v4i r;
  r[0] = (int)(u32)a; r[1] = (int)((u32)(a >> 32) & 0xffffu); r[2] = (int)bytes; r[3] = 0x00020000;
  return r;
}
template <int OFF> DI void ld16(v4u& d, u32 voff, v4i srd, u32 soff) {
  asm volatile("buffer_load_dwordx4 %0, %1, %2, %3 offen offset:%4" : "=v"(d) : "v"(voff), "s"(srd), "s"(soff), "i"(OFF) : "memory");
}
template <int OFF> DI void ld16c(v4u& d, u32 voff, v4i srd, u32 soff) {
  asm volatile("buffer_load_dwordx4 %0, %1, %2, %3 offen offset:%4 sc1" : "=v"(d) : "v"(voff), "s"(srd), "s"(soff), "i"(OFF) : "memory");
}
#define ABL 0
DI void ld8c(v2u& d, u32 voff, v4i srd) {
  if (ABL & 1) voff = 64;
  asm volatile("buffer_load_dwordx2 %0, %1, %2, 0 offen sc1" : "=v"(d) : "v"(voff), "s"(srd) : "memory");
}
DI void ld2(u32& d, u32 voff, v4i srd, u32 soff) {
  asm volatile("buffer_load_ushort %0, %1, %2, %3 offen" : "=v"(d) : "v"(voff), "s"(srd), "s"(soff) : "memory");
}
DI void glds16(u32 lds, u32 voff, v4i srd, u32 soff) {
  asm volatile("s_mov_b32 m0, %0\n\ts_nop 0\n\tbuffer_load_dwordx4 %1, %2, %3 offen lds" ::"s"(lds), "v"(voff), "s"(srd), "s"(soff) : "memory");
}
DI void load_cells(u32 lds, u32 voff, v4i srd, u32 soff, u32 stride, int n) {
#pragma unroll 1
  for (int c = 0; c < n; ++c) {
    u32 t;
    asm volatile("s_mov_b32 m0, %1\n\ts_add_u32 %0, %4, %5\n\tbuffer_load_dwordx4 %2, %3, %4 offen lds\n\t"
                 "s_add_u32 m0, %1, 0x400\n\ts_nop 0\n\tbuffer_load_dwordx4 %2, %3, %0 offen lds"
                 : "=&s"(t) : "s"(lds), "v"(voff), "s"(srd), "s"(soff), "s"(stride * 8) : "memory", "scc");
    lds += 8192; soff += stride * 64;
  }
}
DI void load_pieces(u32 lds, u32 voff, v4i srd, u32 soff, u32 lstep, u32 sstep, int n) {
#pragma unroll 1
  for (int c = 0; c < n; ++c) { glds16(lds, voff, srd, soff); lds += lstep; soff += sstep; }
}
template <int OFF> DI void st16c(v4u d, u32 voff, v4i srd, u32 soff) {
  asm volatile("buffer_store_dwordx4 %0, %1, %2, %3 offen offset:%4 sc1\n\ts_nop 1" ::"v"(d), "v"(voff), "s"(srd), "s"(soff), "i"(OFF) : "memory");
}
template <int OFF> DI void st8c(v2u d, u32 voff, v4i srd, u32 soff) {
  asm volatile("buffer_store_dwordx2 %0, %1, %2, %3 offen offset:%4 sc1" ::"v"(d), "v"(voff), "s"(srd), "s"(soff), "i"(OFF) : "memory");
}
DI void st16(v4u d, u32 voff, v4i srd) {
  asm volatile("buffer_store_dwordx4 %0, %1, %2, 0 offen\n\ts_nop 1" ::"v"(d), "v"(voff), "s"(srd) : "memory");
}
template <int N> DI void vm_wait() { asm volatile("s_waitcnt vmcnt(%0)" ::"i"(N) : "memory"); }
DI void pin(v4u& a) { asm volatile("" : "+v"(a)); }
DI void pin(v2u& a) { asm volatile("" : "+v"(a)); }
DI void pin(u32& a) { asm volatile("" : "+v"(a)); }
DI void keep_s(u32& a) { a = __builtin_amdgcn_readfirstlane(a); asm volatile("" : "+s"(a)); }
DI void wg_barrier() { asm volatile("s_waitcnt lgkmcnt(0)\n\ts_barrier" ::: "memory"); }
DI f32x16 mfma(v4u a, v4u b, f32x16 c) {
  return __builtin_amdgcn_mfma_f32_32x32x16_bf16(__builtin_bit_cast(bf16x8, a), __builtin_bit_cast(bf16x8, b), c, 0, 0, 0);
}
DI u32 f2u(float a) { return __builtin_bit_cast(u32, a); }
DI float u2f(u32 a) { return __builtin_bit_cast(float, a); }
DI u32 pk(float a, float b) { f32x2 f = {a, b}; return __builtin_bit_cast(u32, __builtin_convertvector(f, bf16x2)); }
DI v4u tr_frag(LAS char* p) {
  const s16x4 a = __builtin_amdgcn_ds_read_tr16_b64_v4i16((LAS s16x4*)p);
  const s16x4 b = __builtin_amdgcn_ds_read_tr16_b64_v4i16((LAS s16x4*)(p + 512));
  const v2u x = __builtin_bit_cast(v2u, a), y = __builtin_bit_cast(v2u, b);
  return v4u{x[0], x[1], y[0], y[1]};
}
template <int C> DI float dpp_add(float x) {
  return x + __builtin_bit_cast(float, __builtin_amdgcn_update_dpp(0, __builtin_bit_cast(int, x), C, 0xf, 0xf, true));
}
DI float red8(float x) { x = dpp_add<0xB1>(x); x = dpp_add<0x4E>(x); return dpp_add<0x141>(x); }
DI float swap32(float x) {
  const u32 u = __builtin_bit_cast(u32, x);
  const auto r = __builtin_amdgcn_permlane32_swap(u, u, false, false);
  return __builtin_bit_cast(float, (threadIdx.x & 32) ? r[0] : r[1]);
}
DI void pack_tile(const f32x16& a, v4u& o0, v4u& o1) {
  u32 p[8];
#pragma unroll
  for (int i = 0; i < 8; ++i) p[i] = pk(a[2 * i], a[2 * i + 1]);
  const auto r0 = __builtin_amdgcn_permlane32_swap(p[0], p[2], false, false);
  const auto r1 = __builtin_amdgcn_permlane32_swap(p[1], p[3], false, false);
  const auto r2 = __builtin_amdgcn_permlane32_swap(p[4], p[6], false, false);
  const auto r3 = __builtin_amdgcn_permlane32_swap(p[5], p[7], false, false);
  o0 = v4u{r0[0], r1[0], r0[1], r1[1]};
  o1 = v4u{r2[0], r3[0], r2[1], r3[1]};
}
DI void wait_flags(u32 voff, bool active, v2u tag, v4i ws) {
#pragma unroll 1
  for (u32 spins = 0; spins < SPIN_CAP; ++spins) {
    v2u f;
    ld8c(f, voff, ws);
    vm_wait<0>();
    pin(f);
    const bool ok = !active || (f[0] == tag[0] && f[1] == tag[1]);
    if (__all(ok)) break;
    __builtin_amdgcn_s_sleep(2);
  }
}
DI void post_flag(u32 off, v2u tag, v4i ws) {
  asm volatile("s_mov_b64 exec, 1\n\tbuffer_store_dwordx2 %0, off, %1, %2 sc1\n\ts_mov_b64 exec, -1" ::"v"(tag), "s"(ws), "s"(off) : "memory");
}
DI void post_flag_if(bool on, u32 off, v2u tag, v4i ws) {
  post_flag(on ? off : (u32)WS_END, tag, ws);
}
#define STAMP(i) do {} while (0)
#define STAMP_DECL
#define STAMP_ARG
#define STAMP_PASS
#define CSTAMP(i) do {} while (0)
#define STAMP_OUT() do {} while (0)

struct Stream { v4i srd; u32 voff, so, d8; };
DI void set_m0(u32 lds) { asm volatile("s_mov_b32 m0, %0\n\ts_nop 0" ::"s"(lds) : "memory"); }
template <int Q> DI void piece(Stream& g) {
  asm volatile("buffer_load_dwordx4 %1, %2, %0 offen offset:%4 lds\n\ts_add_u32 %0, %0, %3" : "+s"(g.so)
               : "v"(g.voff), "s"(g.srd), "s"(g.d8), "i"(Q * 1024) : "memory", "scc");
}
template <int Q> DI void piece_m0(Stream& g, u32 lds, u32 so) {
  g.so = so;
  asm volatile("s_mov_b32 m0, %4\n\ts_nop 0\n\tbuffer_load_dwordx4 %1, %2, %0 offen lds\n\ts_add_u32 %0, %0, %3" : "+s"(g.so)
               : "v"(g.voff), "s"(g.srd), "s"(g.d8), "s"(lds) : "memory", "scc");
}
template <int P> DI void piece128(Stream& g, u32 lds, u32 so, u32 s128) {
  if constexpr ((P & 3) == 0) piece_m0<0>(g, lds + (P >> 2) * 16384, so + (P >> 2) * s128);
  else piece<(P & 3)>(g);
}
DI u32 mad24(u32 a, u32 s, u32 c) { u32 d; asm("v_mad_u32_u24 %0, %1, %2, %3" : "=v"(d) : "v"(a), "s"(s), "v"(c)); return d; }
DI bool flags_ok(v2u f, bool active, v2u tag) { if (ABL & 1) return true; return __all(!active || (f[0] == tag[0] && f[1] == tag[1])); }
DI void raw_barrier() { asm volatile("s_barrier" ::: "memory"); }
DI f32x16 bias_init(u32 bias) {
  f32x16 z;
#pragma unroll
  for (int i = 0; i < 16; ++i) z[i] = 0.f;
  const v4u bf = {bias, 0u, 0u, 0u};
  const v4u one = {(threadIdx.x & 32) ? 0u : 0x3f80u, 0u, 0u, 0u};
  return mfma(bf, one, z);
}

template <bool SWAP, bool ZERO, int N1 = 4, int N2 = 24, class H>
DI f32x16 gemm1_block(const v4u (&xa)[28], LAS char* p1, LAS char* p2, LAS char* p3, f32x16 acc, H&& hook) {
  constexpr int PD = 5;
  auto rd = [&](int s) { return s < N1 ? tr_frag(p1 + s * 2048) : (s < N2 ? tr_frag(p2 + (s - N1) * 2048) : tr_frag(p3 + (s - N2) * 2048)); };
  __builtin_amdgcn_s_setprio(1);
  v4u wf[PD];
#pragma unroll
  for (int s = 0; s < PD; ++s) wf[s] = rd(s);
  if constexpr (ZERO) {
#pragma unroll
    for (int i = 0; i < 16; ++i) acc[i] = 0.f;
  }
  __builtin_amdgcn_sched_barrier(0);
#define G1STEP(s) { \
    const v4u cur = wf[(s) % PD]; \
    if ((s) == 24) asm volatile("" ::"v"(acc)); \
    hook(std::integral_constant<int, s>{}); \
    acc = SWAP ? mfma(xa[s], cur, acc) : mfma(cur, xa[s], acc); \
    if ((s) + PD < 28) wf[(s) % PD] = rd((s) + PD); \
    __builtin_amdgcn_sched_barrier(0); }
  G1STEP(0) G1STEP(1) G1STEP(2) G1STEP(3) G1STEP(4) G1STEP(5) G1STEP(6) G1STEP(7) G1STEP(8) G1STEP(9)
  G1STEP(10) G1STEP(11) G1STEP(12) G1STEP(13) G1STEP(14) G1STEP(15) G1STEP(16) G1STEP(17) G1STEP(18) G1STEP(19)
  G1STEP(20) G1STEP(21) G1STEP(22) G1STEP(23) G1STEP(24) G1STEP(25) G1STEP(26) G1STEP(27)
#undef G1STEP
  asm volatile("" ::"v"(acc));
  __builtin_amdgcn_s_setprio(0);
  return acc;
}

DI void gelu4(u32& o0, u32& o1, float a, float b, float c, float d, float c1) {
  float t0, t1, t2, t3;
  asm volatile(
      "v_mul_f32 %2, %6, %6\n\tv_mul_f32 %3, %7, %7\n\tv_mul_f32 %4, %8, %8\n\tv_mul_f32 %5, %9, %9\n\t"
      "v_fmaak_f32 %2, %2, %10, 0xc0135761\n\tv_fmaak_f32 %3, %3, %10, 0xc0135761\n\t"
      "v_fmaak_f32 %4, %4, %10, 0xc0135761\n\tv_fmaak_f32 %5, %5, %10, 0xc0135761\n\t"
      "v_mul_f32 %2, %2, %6\n\tv_mul_f32 %3, %3, %7\n\tv_mul_f32 %4, %4, %8\n\tv_mul_f32 %5, %5, %9\n\t"
      "v_exp_f32 %2, %2\n\tv_exp_f32 %3, %3\n\tv_exp_f32 %4, %4\n\tv_exp_f32 %5, %5\n\t"
      "v_add_f32 %2, 1.0, %2\n\tv_add_f32 %3, 1.0, %3\n\tv_add_f32 %4, 1.0, %4\n\tv_add_f32 %5, 1.0, %5\n\t"
      "v_rcp_f32 %2, %2\n\tv_rcp_f32 %3, %3\n\tv_rcp_f32 %4, %4\n\tv_rcp_f32 %5, %5\n\t"
      "v_mul_f32 %2, %2, %6\n\tv_mul_f32 %3, %3, %7\n\tv_mul_f32 %4, %4, %8\n\tv_mul_f32 %5, %5, %9\n\t"
      "v_cvt_pk_bf16_f32 %0, %2, %3\n\tv_cvt_pk_bf16_f32 %1, %4, %5\n\ts_nop 1"
      : "=&v"(o0), "=&v"(o1), "=&v"(t0), "=&v"(t1), "=&v"(t2), "=&v"(t3) : "v"(a), "v"(b), "v"(c), "v"(d), "v"(c1));
}
DI void swap_tile(const u32 (&p)[8], v4u& o0, v4u& o1) {
  const auto r0 = __builtin_amdgcn_permlane32_swap(p[0], p[2], false, false);
  const auto r1 = __builtin_amdgcn_permlane32_swap(p[1], p[3], false, false);
  const auto r2 = __builtin_amdgcn_permlane32_swap(p[4], p[6], false, false);
  const auto r3 = __builtin_amdgcn_permlane32_swap(p[5], p[7], false, false);
  o0 = v4u{r0[0], r1[0], r0[1], r1[1]};
  o1 = v4u{r2[0], r3[0], r2[1], r3[1]};
}
DI void mfma_settle(const f32x16& a) { asm volatile("s_nop 7\n\ts_nop 4" ::"v"(a)); }
DI void store_v(const f32x16& a, u32 vo, v4i ws, u32 so) {
  st16c<0>(v4u{pk(a[0], a[1]), pk(a[2], a[3]), pk(a[4], a[5]), pk(a[6], a[7])}, vo, ws, so);
  st16c<0>(v4u{pk(a[8], a[9]), pk(a[10], a[11]), pk(a[12], a[13]), pk(a[14], a[15])}, vo, ws, so + 14336u);
}
DI void store_gelu(const f32x16& a, float gc1, u32 lo16, v4i ws, u32 so) {
  u32 p[8];
  mfma_settle(a);
#pragma unroll
  for (int i = 0; i < 4; ++i) gelu4(p[2 * i], p[2 * i + 1], a[4 * i], a[4 * i + 1], a[4 * i + 2], a[4 * i + 3], gc1);
  v4u o0, o1;
  swap_tile(p, o0, o1);
  st16c<0>(o0, lo16, ws, so);
  st16c<1024>(o1, lo16, ws, so);
}
DI float max16(const f32x16& s) {
  float a, b;
  asm volatile("v_max3_f32 %0, %2, %3, %4\n\tv_max3_f32 %1, %5, %6, %7\n\tv_max3_f32 %0, %0, %8, %9\n\tv_max3_f32 %1, %1, %10, %11\n\t"
               "v_max3_f32 %0, %0, %12, %13\n\tv_max3_f32 %1, %1, %14, %15\n\tv_max3_f32 %0, %0, %16, %17\n\tv_max_f32 %0, %0, %1"
               : "=&v"(a), "=&v"(b) : "v"(s[0]), "v"(s[1]), "v"(s[2]), "v"(s[3]), "v"(s[4]), "v"(s[5]), "v"(s[6]), "v"(s[7]), "v"(s[8]),
                 "v"(s[9]), "v"(s[10]), "v"(s[11]), "v"(s[12]), "v"(s[13]), "v"(s[14]), "v"(s[15]));
  return a;
}
DI v4u exp8(const f32x16& s, int o, float cs, float nb) {
  v4u p;
#pragma unroll
  for (int j = 0; j < 4; ++j) {
    float t0, t1;
    asm("v_fma_f32 %0, %1, %2, %3" : "=v"(t0) : "v"(s[o + 2 * j]), "v"(cs), "v"(nb));
    asm("v_fma_f32 %0, %1, %2, %3" : "=v"(t1) : "v"(s[o + 2 * j + 1]), "v"(cs), "v"(nb));
    p[j] = pk(__builtin_amdgcn_exp2f(t0), __builtin_amdgcn_exp2f(t1));
  }
  return p;
}

DI int fq_off(int blk) { return blk < 28 ? blk * 8 : 256 + (blk - 28) * 8; }
DI int fh_off(int blk) {
  const int st = blk >> 1, g = blk & 1;
  return st < 11 ? 384 + g * 128 + st * 8 : (st < 27 ? 640 + g * 128 + (st - 11) * 8 : 896 + g * 8);
}
DI int hblock(int j, int idx) {
  if (j < 2) return 2 * idx + j;
  const int p = 8 * (j - 2) + idx;
  return p < 6 ? 16 + p : (p < 22 ? 2 * p + 10 : (p < 38 ? 2 * p - 21 : 16 + p));
}
DI void poll(v2u& d, u32 voff, v4i srd, u64 lanes) {
  asm volatile("s_mov_b64 exec, %3\n\tbuffer_load_dwordx2 %0, %1, %2, 0 offen sc1\n\ts_mov_b64 exec, -1" : "=v"(d) : "v"(voff), "s"(srd), "s"(lanes) : "memory");
}
DI void check(v2u& f, u32 voff, v4i ws, v2u tag, u64 lanes) {
  pin(f);
#pragma unroll 1
  for (u32 spins = 0; spins < SPIN_CAP; ++spins) {
    if ((~__ballot(f[0] == tag[0] && f[1] == tag[1]) & lanes) == 0) break;
    __builtin_amdgcn_s_sleep(1);
    poll(f, voff, ws, lanes);
    vm_wait<0>();
    pin(f);
  }
}
DI void dma2c(u32 lds, u32 voff, v4i srd, u32 so, u32 so2) {
  asm volatile("s_mov_b32 m0, %0\n\ts_nop 0\n\tbuffer_load_dwordx4 %1, %2, %3 offen sc1 lds\n\t"
               "buffer_load_dwordx4 %1, %2, %4 offen offset:1024 sc1 lds" ::"s"(lds), "v"(voff), "s"(srd), "s"(so), "s"(so2) : "memory");
}
constexpr int SY = 4 * 8192;
DI void lds_arrive(LAS char* p) {
  asm volatile("s_waitcnt lgkmcnt(0)\n\ts_mov_b64 exec, 1\n\tds_add_u32 %0, %1\n\ts_mov_b64 exec, -1" ::"v"((u32)(size_t)p), "v"(1u) : "memory");
}
DI void lds_arrive_nowait(LAS char* p) {
  asm volatile("s_mov_b64 exec, 1\n\tds_add_u32 %0, %1\n\ts_mov_b64 exec, -1" ::"v"((u32)(size_t)p), "v"(1u) : "memory");
}
DI void lds_wait(LAS char* p, u32 n) {
#pragma unroll 1
  for (u32 spins = 0; spins < (1u << 22); ++spins) {
    u32 v;
    asm volatile("ds_read_b32 %0, %1\n\ts_waitcnt lgkmcnt(0)" : "=v"(v) : "v"((u32)(size_t)p) : "memory");
    if (__builtin_amdgcn_readfirstlane(v) >= n) break;
  }
}

struct Ctx {
  v4i ws, sqkv, sw1, sw2;
  int m, b, xcd, cg, ct, tt, tb1, ia, hd, c0;
  bool heavy;
  u32 goff_q, goff_1, goff_2, trl, lo16, fo1, fo2, bias1, bias2, bias3, wtch;
  u32 xb[4];
  float gc1;
  v2u tag;
};

template <int GRP>
DI f32x16 phase1(Ctx& c, LAS char* L, f32x16& md, f32x16& mw, Stream& g2, v2u& fpoll, int& blk STAMP_ARG) {
  const u32 lds0 = (u32)(size_t)L;
  const int lane = threadIdx.x & 63, r32 = lane & 31, hh = lane >> 5;
  const int m = c.m, cg = c.cg, tb1 = c.tb1;
  const v4i ws = c.ws;
  const bool s1q = cg < 5, heavy = c.heavy;
  const u32 lo16 = c.lo16;
  const v2u tag = c.tag;
  v4u xa[28];
#define RDX(s) xa[s] = *(LAS v4u*)(L + c.xb[(s) & 3] + (((s) >> 2) == 6 ? (GRP == 0 ? -16384 : 6 * 16384) : 16384 * ((s) >> 2)))
  const u32 tx = c.trl ^ (GRP * 64);
  u32 q1 = tx + (GRP ? 49152 : 0), q2 = tx + (GRP ? 0 : 8192), q3 = tx + (GRP ? 40960 : 49152);
  u32 vst = (u32)(hh * 7168 + r32 * 16);
  Stream g;
  f32x16 acc;
  if constexpr (GRP == 0) {
#pragma unroll
    for (int s = 0; s < 28; ++s) RDX(s);
    g.srd = c.sqkv; g.voff = c.goff_q; g.d8 = 8 * 2688 - 1024;
    u32 tl = lds0 + (u32)(49152 + m * 2048), ts = (u32)((384 + m * 16) * 2688 + cg * 128);
    keep_s(tl); keep_s(ts);
    asm volatile("" : "+v"(q1), "+v"(q2), "+v"(q3), "+v"(vst));
    STAMP(5);
    vm_wait<0>();
    STAMP(6);
    wg_barrier();
    piece_m0<0>(g, tl, ts); piece<1>(g);
  } else {
#pragma unroll
    for (int s = 0; s < 4; ++s) RDX(s);
    const u32 st1 = s1q ? 2688u : 3584u;
    const u32 co1 = (u32)((s1q ? 16 + cg : cg - 5) * 128);
    g.srd = s1q ? c.sqkv : c.sw1; g.voff = s1q ? c.goff_q : c.goff_1; g.d8 = 8 * st1 - 1024;
    u32 hl = lds0 + (u32)(57344 + m * 2048), hs = (u32)(m * 16) * st1 + co1;
    u32 bl = lds0 + (u32)(65536 + 4096 * m), bs = (u32)(64 + 32 * m) * st1 + co1, s128 = 128 * st1;
    keep_s(hl); keep_s(hs); keep_s(bl); keep_s(bs); keep_s(s128); keep_s(g.d8);
    asm volatile("" : "+v"(q1), "+v"(q2), "+v"(q3), "+v"(vst), "+v"(g.voff));
    vm_wait<0>();
    wg_barrier();
#pragma unroll
    for (int s = 4; s < 16; ++s) RDX(s);
    piece_m0<0>(g, hl, hs); piece<1>(g);
    asm volatile("s_waitcnt lgkmcnt(0)" ::: "memory");
#pragma unroll
    for (int s = 16; s < 28; ++s) RDX(s);
    STAMP(5);
    piece128<0>(g, bl, bs, s128); piece128<1>(g, bl, bs, s128); piece128<2>(g, bl, bs, s128); piece128<3>(g, bl, bs, s128);
    piece128<4>(g, bl, bs, s128); piece128<5>(g, bl, bs, s128);
    STAMP(6);
    wg_barrier();
    piece128<6>(g, bl, bs, s128); piece128<7>(g, bl, bs, s128); piece128<8>(g, bl, bs, s128); piece128<9>(g, bl, bs, s128);
    piece128<10>(g, bl, bs, s128); piece128<11>(g, bl, bs, s128);
  }
#undef RDX
  pin(c.bias1); pin(c.bias2); pin(c.bias3); pin(c.wtch);
  STAMP(8);
  {
    auto hook = [&](auto S) {
      constexpr int s = decltype(S)::value;
      if constexpr (GRP == 0 && s == 19) { vm_wait<0>(); raw_barrier(); }
      if constexpr (GRP == 1 && s == 24) { vm_wait<0>(); raw_barrier(); __builtin_amdgcn_s_setprio(2); }
    };
    if (cg >= 14) {
      acc = gemm1_block<true, true>(xa, L + q1, L + q2, L + q3, acc, hook);
      STAMP(9);
      store_v(acc, vst, ws, (u32)(WS_VT + tb1 * VT_TB + (2 * cg + GRP - 28) * 512));
    } else {
      acc = gemm1_block<false, true>(xa, L + q1, L + q2, L + q3, acc, hook);
      STAMP(9);
      v4u o0, o1;
      pack_tile(acc, o0, o1);
      const u32 so = (u32)(WS_QK + tb1 * QK_TB + (2 * cg + GRP) * 2048);
      st16c<0>(o0, lo16, ws, so);
      st16c<1024>(o1, lo16, ws, so);
    }
  }
  u32 pend = (u32)(WS_F + tb1 * 1024 + fq_off(2 * cg + GRP));
  g.srd = c.sw1; g.voff = c.goff_1; g.d8 = 8 * 3584 - 1024;
  const u32 st2 = heavy ? 3584u : 896u;
  g2.srd = heavy ? c.sw1 : c.sw2; g2.voff = heavy ? c.goff_1 : c.goff_2; g2.d8 = 8 * st2 - 1024;
  {
    const u32 l2 = lds0 + (u32)(114688 + 4096 * m), s2 = (u32)(32 * m * 3584 + (11 + cg) * 128);
    if constexpr (GRP == 0) {
      const int dd = r32 - 4 * hh;
#define MSK(i) { const u32 t = (u32)((dd - (((i) & 3) + 8 * ((i) >> 2))) >> 31) & 0xff800000u; md[i] = u2f(t); mw[i] = u2f(t ^ 0xff800000u); }
      piece128<0>(g, l2, s2, 128u * 3584u); piece128<1>(g, l2, s2, 128u * 3584u); piece128<2>(g, l2, s2, 128u * 3584u);
      piece128<3>(g, l2, s2, 128u * 3584u);
      MSK(0) MSK(1) MSK(2) MSK(3) MSK(4) MSK(5) MSK(6) MSK(7) MSK(8) MSK(9) MSK(10) MSK(11) MSK(12) MSK(13) MSK(14) MSK(15)
#undef MSK
      asm volatile("" : "+v"(md), "+v"(mw));
      piece128<4>(g, l2, s2, 128u * 3584u); piece128<5>(g, l2, s2, 128u * 3584u); piece128<6>(g, l2, s2, 128u * 3584u);
      piece128<7>(g, l2, s2, 128u * 3584u);
    } else {
      piece128<8>(g, l2, s2, 128u * 3584u); piece128<9>(g, l2, s2, 128u * 3584u); piece128<10>(g, l2, s2, 128u * 3584u);
      piece128<11>(g, l2, s2, 128u * 3584u);
      piece_m0<0>(g, lds0 + (u32)(49152 + m * 2048), (u32)((384 + m * 16) * 3584 + (11 + cg) * 128)); piece<1>(g);
      if (m == 0) *(LAS v4u*)(L + SY) = v4u{0u, 0u, 0u, 0u};
    }
  }
  const u32 co2 = (u32)((heavy ? 27 : c.ct) * 128);
  auto ring_hook = [&](auto S, u32 wl, u32 w0, u32 w1) {
    constexpr int s = decltype(S)::value;
    if constexpr (s == 0) piece_m0<0>(g2, wl, w0);
    if constexpr (s == 8) piece_m0<0>(g2, wl + 4096, w1);
    if constexpr ((s & 1) == 0 && (s & 7) != 0 && s < 16) piece<((s & 7) / 2)>(g2);
  };
  {
    u32 p1 = q1 + 57344u, p2 = q2 + 57344u, p3 = q3 + 57344u;
    u32 wl = lds0 + (u32)(m * 8192), w0 = (u32)(heavy ? 64 * m : 128 * m) * st2 + co2, w1 = w0 + (heavy ? 32u : 64u) * st2;
    acc = bias_init(s1q ? 0u : c.bias1);
    asm volatile("" : "+v"(p1), "+v"(p2), "+v"(p3), "+v"(acc));
    keep_s(pend);
    if constexpr (GRP == 1) { keep_s(wl); keep_s(w0); keep_s(w1); keep_s(g2.d8); asm volatile("" : "+v"(g2.voff)); }
    vm_wait<(GRP == 0 ? 8 : 6)>();
    post_flag(pend, tag, ws);
    STAMP(10);
    if constexpr (GRP == 0) wg_barrier();
    STAMP(12);
    auto hook = [&](auto S) {
      constexpr int s = decltype(S)::value;
      if constexpr (GRP == 1) ring_hook(S, wl, w0, w1);
      if constexpr (GRP == 1 && s == 24) { vm_wait<9>(); raw_barrier(); __builtin_amdgcn_s_setprio(2); }
    };
    if (s1q) {
      acc = gemm1_block<true, false>(xa, L + p1, L + p2, L + p3, acc, hook);
      STAMP(13);
      store_v(acc, vst, ws, (u32)(WS_VT + tb1 * VT_TB + (2 * cg + GRP + 4) * 512));
    } else {
      acc = gemm1_block<false, false>(xa, L + p1, L + p2, L + p3, acc, hook);
      STAMP(13);
      store_gelu(acc, c.gc1, lo16, ws, (u32)(WS_HH + tb1 * HH_TB + (2 * cg - 10 + GRP) * 2048));
    }
  }
  pend = (u32)(WS_F + tb1 * 1024 + (s1q ? 256 + (4 + 2 * cg + GRP) * 8 : 384 + GRP * 128 + (cg - 5) * 8));
  blk = 22 + 2 * cg + GRP;
  {
    u32 wl = lds0 + (u32)((m == 0 ? 5 : (m == 3 ? 13 : 9 + m)) * 8192);
    u32 w0 = (u32)(heavy ? (m == 0 ? 384 : 192 + 64 * m + (m == 3 ? 64 : 0)) : 128 * m + 32) * st2 + co2, w1 = w0 + (heavy ? 32u : 64u) * st2;
    u32 p1 = q1 + (GRP ? 0u : 114688u), p2 = q2 + 114688u, p3 = q3 + (GRP ? 114688u : 0u);
    acc = bias_init(c.bias2);
    asm volatile("" : "+v"(p1), "+v"(p2), "+v"(p3), "+v"(acc));
    keep_s(pend);
    if constexpr (GRP == 1) { keep_s(wl); keep_s(w0); keep_s(w1); }
    if (s1q) { vm_wait<0>(); post_flag(pend, tag, ws); }
    STAMP(14);
    if constexpr (GRP == 0) { vm_wait<2>(); wg_barrier(); }
    STAMP(16);
    auto hook = [&](auto S) {
      constexpr int s = decltype(S)::value;
      if constexpr (GRP == 1) {
        ring_hook(S, wl, w0, w1);
        if constexpr (s == 16) poll(fpoll, c.fo1, ws, 0xfull);
        if constexpr (s == 23) { vm_wait<9>(); post_flag_if(!s1q, pend, tag, ws); }
        if constexpr (s == 24) { if (heavy) vm_wait<2>(); lds_arrive_nowait(L + SY); }
      } else {
        if constexpr (s == 8) poll(fpoll, c.fo1, ws, 0xffull);
        if constexpr (s == 21) { vm_wait<1>(); post_flag_if(!s1q, pend, tag, ws); }
      }
    };
    acc = gemm1_block<false, false>(xa, L + p1, L + p2, L + p3, acc, hook);
    if constexpr (GRP == 0) lds_arrive_nowait(L + SY);
    STAMP(17);
  }
  if constexpr (GRP == 1) {
    if (heavy) {
      store_gelu(acc, c.gc1, lo16, ws, (u32)(WS_HH + tb1 * HH_TB + blk * 2048));
      pend = (u32)(WS_F + tb1 * 1024 + fh_off(blk));
      blk = 54 + (c.xcd & 1);
      const u32 t = c.trl ^ (u32)((c.xcd & 1) * 64);
      acc = bias_init(c.bias3);
      lds_wait(L + SY, 8);
      STAMP(20);
      acc = gemm1_block<false, false, 4, 20>(xa, L + t + 5 * 8192, L + t, L + t + 10 * 8192, acc, [&](auto S) {
        if constexpr (decltype(S)::value == 21) { vm_wait<0>(); post_flag(pend, tag, ws); } });
      STAMP(21);
    }
  }
  return acc;
}

DI void group_a(Ctx& c, LAS char* L STAMP_ARG) {
  const u32 lds0 = (u32)(size_t)L;
  const int lane = threadIdx.x & 63, r32 = lane & 31, hh = lane >> 5;
  const int m = c.m, c0 = c.c0;
  const v4i ws = c.ws;
  const u32 lo16 = c.lo16;
  const v2u tag = c.tag;
  f32x16 md, mw;
  Stream g2;
  v2u fpoll;
  int blk;
  const f32x16 acc = phase1<0>(c, L, md, mw, g2, fpoll, blk STAMP_PASS);
  const int tbq = 4 * c.ia + m;
  const bool odd = c.hd & 1;
  int kl[3];
#pragma unroll
  for (int n = 0; n < 3; ++n) { const int k = 4 * c.ia - 8 + 3 * m + n; kl[n] = k < 0 ? tbq : k; }
  v4u qf[2];
  {
    u32 p[8];
    vm_wait<1>();
    check(fpoll, c.fo1, ws, tag, 0xffull);
    STAMP(24);
#pragma unroll
    for (int n = 0; n < 3; ++n) {
      const u32 so = (u32)(WS_QK + (56 + c0) * 512 + kl[n] * QK_TB);
      dma2c(lds0 + (u32)(57344 + (3 * m + n) * 2048), lo16, ws, so, so);
    }
    ld16c<0>(qf[0], lo16, ws, (u32)(WS_QK + tbq * QK_TB + c0 * 512));
    ld16c<1024>(qf[1], lo16, ws, (u32)(WS_QK + tbq * QK_TB + c0 * 512));
    mfma_settle(acc);
    gelu4(p[0], p[1], acc[0], acc[1], acc[2], acc[3], c.gc1);
    gelu4(p[2], p[3], acc[4], acc[5], acc[6], acc[7], c.gc1);
    gelu4(p[4], p[5], acc[8], acc[9], acc[10], acc[11], c.gc1);
    gelu4(p[6], p[7], acc[12], acc[13], acc[14], acc[15], c.gc1);
    v4u o0, o1;
    swap_tile(p, o0, o1);
    poll(fpoll, c.fo2, ws, 0x3full);
    const u32 so = (u32)(WS_HH + c.tb1 * HH_TB + blk * 2048);
    st16c<0>(o0, lo16, ws, so);
    st16c<1024>(o1, lo16, ws, so);
  }
  const u32 pend = (u32)(WS_F + c.tb1 * 1024 + fh_off(blk));
  STAMP(32);
  vm_wait<3>();
  pin(qf[0]); pin(qf[1]);
  lds_arrive_nowait(L + SY + 4);
  __builtin_amdgcn_s_setprio(APRIO);
  {
    const u32 k0 = (odd && hh == 0) ? 0u : ~0u, k1 = (!odd && hh == 1) ? 0u : ~0u;
    qf[0][0] &= k0; qf[0][1] &= k0; qf[1][2] &= k1; qf[1][3] &= k1;
  }
  lds_wait(L + SY + 4, 4);
  STAMP(33);
  f32x16 sc[9];
  f32x16 zero;
#pragma unroll
  for (int i = 0; i < 16; ++i) zero[i] = 0.f;
  float mx = -1e30f;
#define GATE(n) (tbq - 8 + (n) < 0 ? -__builtin_inff() : 0.f)
  {
    LAS char* kp = L + 57344 + m * 2048 + lo16;
    v4u kf[9][2];
#define RDK(n) { kf[n][0] = *(LAS v4u*)(kp + (n) * 2048); kf[n][1] = *(LAS v4u*)(kp + (n) * 2048 + 1024); }
#define QK(n, C) { sc[n] = mfma(kf[n][0], qf[0], C); sc[n] = mfma(kf[n][1], qf[1], sc[n]); if ((n) + 3 < 9) RDK(((n) + 3 < 9 ? (n) + 3 : 0)) \
      __builtin_amdgcn_sched_barrier(0); }
#define MX(n) { mx = __builtin_fmaxf(mx, max16(sc[n]) + GATE(n)); __builtin_amdgcn_sched_barrier(0); }
    RDK(0) RDK(1) RDK(2)
    __builtin_amdgcn_sched_barrier(0);
    QK(0, mw) QK(1, zero) QK(2, zero) mfma_settle(sc[0]); MX(0) QK(3, zero) MX(1) QK(4, zero) MX(2) QK(5, zero) MX(3)
    asm volatile("" ::"v"(sc[4]), "v"(sc[5]), "v"(mx));
    vm_wait<2>();
    check(fpoll, c.fo2, ws, tag, 0x3full);
    lds_wait(L + SY, 8);
    {
      const u32 vo = (u32)(hh * 7168 + (8 * c0 + r32) * 16);
#pragma unroll
      for (int n = 0; n < 3; ++n) {
        const u32 so = (u32)(WS_VT + kl[n] * VT_TB);
        dma2c(lds0 + (u32)(114688 + (3 * m + n) * 2048), vo, ws, so, so + 14336u - 1024u);
      }
    }
    STAMP(34);
    QK(6, zero) MX(4) QK(7, zero) MX(5) QK(8, md) MX(6) MX(7) mfma_settle(sc[8]); MX(8)
#undef MX
#undef QK
#undef RDK
  }
  vm_wait<6>();
  post_flag(pend, tag, ws);
  STAMP(35);
  mx = __builtin_fmaxf(mx, swap32(mx));
  float cs = 0.18898223650461362f * 1.4426950408889634f;
  asm volatile("" : "+v"(cs));
  const float nb = -mx * cs;
  v4u pf[9][2];
#define EX(n) { const float nb_ = nb + GATE(n); pf[n][0] = exp8(sc[n], 0, cs, nb_); pf[n][1] = exp8(sc[n], 8, cs, nb_); pin(pf[n][0]); pin(pf[n][1]); \
    __builtin_amdgcn_sched_barrier(0); }
  EX(0) EX(1)
  vm_wait<1>();
  if (lane < 12) *(LAS v4u*)(L + 114688 + m * 6144 + lane * 512 + (odd ? 0 : 28 * 16)) = v4u{0x3f803f80u, 0x3f803f80u, 0x3f803f80u, 0x3f803f80u};
  lds_arrive(L + SY + 8);
  EX(2)
  lds_wait(L + SY + 8, 4);
  STAMP(36);
  f32x16 o = zero;
  {
    LAS char* vp = L + 114688 + m * 2048 + lo16;
    v4u vf[9][2];
#define RDV(n) { vf[n][0] = *(LAS v4u*)(vp + (n) * 2048); vf[n][1] = *(LAS v4u*)(vp + (n) * 2048 + 1024); __builtin_amdgcn_sched_barrier(0); }
#define PV(n) { o = mfma(vf[n][0], pf[n][0], o); o = mfma(vf[n][1], pf[n][1], o); __builtin_amdgcn_sched_barrier(0); }
    RDV(0) RDV(1) EX(3) PV(0) PV(1) RDV(2) RDV(3) EX(4) PV(2) PV(3) RDV(4) RDV(5) EX(5) PV(4) PV(5) RDV(6) RDV(7) EX(6) PV(6) RDV(8) EX(7) PV(7) EX(8) PV(8)
#undef PV
#undef RDV
  }
#undef EX
#undef GATE
  STAMP(37);
  const u32 lsel = f2u(odd ? o[0] : o[12]);
  const auto lr = __builtin_amdgcn_permlane32_swap(lsel, lsel, false, false);
  float rl = __builtin_amdgcn_rcpf(u2f(odd ? lr[0] : lr[1]));
  asm volatile("s_nop 0" : "+v"(rl));
#pragma unroll
  for (int i = 0; i < 16; ++i) { float t; asm("v_mul_f32 %0, %1, %2" : "=v"(t) : "v"(o[i]), "v"(rl)); o[i] = t; }
  v4u o0, o1;
  pack_tile(o, o0, o1);
  const u32 so = (u32)(WS_HH + tbq * HH_TB + (224 + c0) * 512);
  if (odd) {
    asm volatile("buffer_store_dwordx4 %0, %3, %4, %5 offen offset:1024 sc1\n\t"
                 "s_mov_b32 exec_lo, 0\n\tbuffer_store_dwordx4 %1, %3, %4, %5 offen sc1\n\t"
                 "s_mov_b32 exec_lo, -1\n\ts_mov_b32 exec_hi, 0\n\tbuffer_store_dwordx2 %2, %3, %4, %5 offen offset:8 sc1\n\ts_mov_b64 exec, -1"
                 ::"v"(o1), "v"(o0), "v"(v2u{o0[2], o0[3]}), "v"(lo16), "s"(ws), "s"(so) : "memory");
  } else {
    asm volatile("buffer_store_dwordx4 %0, %3, %4, %5 offen sc1\n\t"
                 "s_mov_b32 exec_hi, 0\n\tbuffer_store_dwordx4 %1, %3, %4, %5 offen offset:1024 sc1\n\t"
                 "s_mov_b32 exec_hi, -1\n\ts_mov_b32 exec_lo, 0\n\tbuffer_store_dwordx2 %2, %3, %4, %5 offen offset:1024 sc1\n\ts_mov_b64 exec, -1"
                 ::"v"(o0), "v"(o1), "v"(v2u{o1[0], o1[1]}), "v"(lo16), "s"(ws), "s"(so) : "memory");
  }
  STAMP(38);
  vm_wait<0>();
  post_flag((u32)(WS_FO + (tbq * 16 + c.hd) * 8), tag, ws);
  STAMP(39);
}

DI void group_b(Ctx& c, LAS char* L STAMP_ARG) {
  const u32 lds0 = (u32)(size_t)L;
  const int lane = threadIdx.x & 63, r32 = lane & 31, hh = lane >> 5;
  const int kq = c.m, ct = c.ct, tt = c.tt;
  const v4i ws = c.ws;
  const u32 lo16 = c.lo16;
  const v2u tag = c.tag;
  f32x16 md, mw;
  Stream g;
  v2u fpoll;
  int blk;
  const f32x16 acc = phase1<1>(c, L, md, mw, g, fpoll, blk STAMP_PASS);
  const u32 hso = (u32)(WS_HH + c.tb1 * HH_TB + blk * 2048);
  const u32 pend2 = (u32)(WS_F + c.tb1 * 1024 + fh_off(blk));
  if (c.heavy) {
    store_gelu(acc, c.gc1, lo16, ws, hso);
    vm_wait<0>();
    post_flag(pend2, tag, ws);
    if (c.b == 255 && kq == 0 && !ABL) {
#pragma unroll 1
      for (u32 spins = 0; spins < SPIN_CAP; ++spins) {
        v4u f[8];
        ld16c<0>(f[0], lo16, ws, (u32)WS_FO); ld16c<1024>(f[1], lo16, ws, (u32)WS_FO);
        ld16c<2048>(f[2], lo16, ws, (u32)WS_FO); ld16c<3072>(f[3], lo16, ws, (u32)WS_FO);
        ld16c<0>(f[4], lo16, ws, (u32)(WS_FO + 4096)); ld16c<1024>(f[5], lo16, ws, (u32)(WS_FO + 4096));
        ld16c<2048>(f[6], lo16, ws, (u32)(WS_FO + 4096)); ld16c<3072>(f[7], lo16, ws, (u32)(WS_FO + 4096));
        vm_wait<0>();
        bool ok = true;
#pragma unroll
        for (int i = 0; i < 8; ++i) { pin(f[i]); ok = ok && f[i][0] == tag[0] && f[i][1] == tag[1] && f[i][2] == tag[0] && f[i][3] == tag[1]; }
        if (__all(ok)) break;
        __builtin_amdgcn_s_sleep(32);
      }
      post_flag((u32)WS_E, tag, ws);
    }
    return;
  }
  const __attribute__((address_space(4))) u64* ka = (const __attribute__((address_space(4))) u64*)__builtin_amdgcn_kernarg_segment_ptr();
  asm volatile("" : "+s"(ka));
  const v4i sx2 = make_srd((const void*)ka[0], S_ * D_ * 2), swo = make_srd((const void*)ka[2], D_ * D_ * 2);
  const v4i sb2 = make_srd((const void*)ka[6], D_ * 2), sout = make_srd((const void*)ka[7], S_ * D_ * 2);
  const u32 cell[3] = {(u32)(kq * 8192), (u32)((kq == 0 ? 5 : (kq == 3 ? 13 : 9 + kq)) * 8192), (u32)((kq == 0 ? 12 : (kq == 1 ? 6 : 15 + kq)) * 8192)};
  const u32 t0 = c.trl ^ (u32)(kq * 32);
  u32 ta[3], tb[3];
#pragma unroll
  for (int i = 0; i < 3; ++i) { ta[i] = cell[i] + t0; tb[i] = cell[i] + (t0 ^ 64u); }
  const u32 hb = (u32)(WS_HH + 2 * tt * HH_TB);
  v4u h0[9][4], h1[9][4], wa[4], wb[4];
  auto krun = [&](int j, int u) { return j < 7 ? 32 * hblock(j, 2 * kq + u) : (j == 7 ? 1792 + 64 * kq + 32 * u : 2048 + 48 * kq + 32 * u); };
#define LDH0(j, s) ld16c<((s) & 1) * 1024>(h0[j][s], lo16, ws, hb + (u32)krun(j, (s) >> 1) * 64u)
#define LDH1(j, s) ld16c<((s) & 1) * 1024>(h1[j][s], lo16, ws, hb + (u32)krun(j, (s) >> 1) * 64u + HH_TB)
#define RDW(j, s) { wa[s] = tr_frag(L + ta[(j) % 3] + (s) * 2048); wb[s] = tr_frag(L + tb[(j) % 3] + (s) * 2048); }
#define GP(J, P) { if (((P) & 3) == 0) { g.srd = (J) < 7 ? c.sw2 : swo; \
      piece_m0<0>(g, lds0 + cell[(J) % 3] + (P) * 1024, (u32)((krun(J, (P) >> 2) - ((J) < 7 ? 0 : 1792)) * 896 + ct * 128)); } \
    else piece<((P) & 3)>(g); }
#define SB __builtin_amdgcn_sched_barrier(0)
  {
    vm_wait<1>();
    check(fpoll, c.fo1, ws, tag, 0xfull);
    STAMP(24);
    u32 p[8];
    LDH0(0, 0); LDH1(0, 0); LDH0(0, 1); LDH1(0, 1);
    mfma_settle(acc);
    gelu4(p[0], p[1], acc[0], acc[1], acc[2], acc[3], c.gc1);
    LDH0(0, 2); LDH1(0, 2);
    gelu4(p[2], p[3], acc[4], acc[5], acc[6], acc[7], c.gc1);
    LDH0(0, 3); LDH1(0, 3);
    gelu4(p[4], p[5], acc[8], acc[9], acc[10], acc[11], c.gc1);
    poll(fpoll, c.fo1, ws, 0xf0ull);
    gelu4(p[6], p[7], acc[12], acc[13], acc[14], acc[15], c.gc1);
    v4u o0, o1;
    swap_tile(p, o0, o1);
    st16c<0>(o0, lo16, ws, hso);
    st16c<1024>(o1, lo16, ws, hso);
  }
  STAMP(32);
  CSTAMP(61);
  lds_wait(L + SY, 8);
  RDW(0, 0) RDW(0, 1) RDW(0, 2) RDW(0, 3)
  GP(2, 0) GP(2, 1) GP(2, 2) GP(2, 3) GP(2, 4) GP(2, 5) GP(2, 6) GP(2, 7)
  f32x16 a00, a01, a10, a11;
#pragma unroll
  for (int i = 0; i < 16; ++i) { a00[i] = 0.f; a01[i] = 0.f; a10[i] = 0.f; a11[i] = 0.f; }
  const u32 fo_o = (u32)(WS_FO + (2 * tt * 16 + r32) * 8);
  v4u xres0, xres1, bres;
  const u32 oo = (u32)((tt * 64 + r32) * 896 + (ct * 64 + kq * 16 + hh * 8) * 2);
  LAS char* r0 = L + cell[0] + lo16;
  LAS char* r1 = L + cell[1] + lo16;
#define W4(p, a, o) *(LAS f32x4*)(p) = f32x4{a[o], a[(o) + 1], a[(o) + 2], a[(o) + 3]}
  vm_wait<10>();
  check(fpoll, c.fo1, ws, tag, 0xf0ull);
  STAMP(41);
  vm_wait<17>();
  pin(h0[0][0]); pin(h1[0][0]); SB;
  a00 = mfma(wa[0], h0[0][0], a00); LDH0(1, 0); SB;
  a10 = mfma(wb[0], h0[0][0], a10); LDH1(1, 0); SB;
  a01 = mfma(wa[0], h1[0][0], a01); GP(3, 0) SB;
  a11 = mfma(wb[0], h1[0][0], a11); GP(3, 1) RDW(1, 0) SB;
  vm_wait<12>();
  post_flag(pend2, tag, ws);
  vm_wait<20>();
  pin(h0[0][1]); pin(h1[0][1]); SB;
  a00 = mfma(wa[1], h0[0][1], a00); LDH0(1, 1); SB;
  a10 = mfma(wb[1], h0[0][1], a10); LDH1(1, 1); SB;
  a01 = mfma(wa[1], h1[0][1], a01); GP(3, 2) SB;
  a11 = mfma(wb[1], h1[0][1], a11); GP(3, 3) RDW(1, 1) SB;
  poll(fpoll, c.fo1, ws, 0xff00ull);
  vm_wait<23>();
  pin(h0[0][2]); pin(h1[0][2]); SB;
  a00 = mfma(wa[2], h0[0][2], a00); LDH0(1, 2); SB;
  a10 = mfma(wb[2], h0[0][2], a10); LDH1(1, 2); SB;
  a01 = mfma(wa[2], h1[0][2], a01); GP(3, 4) SB;
  a11 = mfma(wb[2], h1[0][2], a11); GP(3, 5) RDW(1, 2) SB;
  vm_wait<25>();
  pin(h0[0][3]); pin(h1[0][3]); SB;
  a00 = mfma(wa[3], h0[0][3], a00); LDH0(1, 3); SB;
  a10 = mfma(wb[3], h0[0][3], a10); LDH1(1, 3); SB;
  a01 = mfma(wa[3], h1[0][3], a01); GP(3, 6) SB;
  a11 = mfma(wb[3], h1[0][3], a11); GP(3, 7) RDW(1, 3) SB;
  asm volatile("" ::"v"(a00), "v"(a01), "v"(a10), "v"(a11));
  vm_wait<8>();
  check(fpoll, c.fo1, ws, tag, 0xff00ull);
  STAMP(42);
  vm_wait<16>();
  pin(h0[1][0]); pin(h1[1][0]); SB;
  a00 = mfma(wa[0], h0[1][0], a00); LDH0(2, 0); SB;
  a10 = mfma(wb[0], h0[1][0], a10); LDH1(2, 0); SB;
  a01 = mfma(wa[0], h1[1][0], a01); GP(4, 0) SB;
  a11 = mfma(wb[0], h1[1][0], a11); GP(4, 1) RDW(2, 0) SB;
  vm_wait<15>();
  pin(h0[1][1]); pin(h1[1][1]); SB;
  a00 = mfma(wa[1], h0[1][1], a00); LDH0(2, 1); SB;
  a10 = mfma(wb[1], h0[1][1], a10); LDH1(2, 1); SB;
  a01 = mfma(wa[1], h1[1][1], a01); GP(4, 2) SB;
  a11 = mfma(wb[1], h1[1][1], a11); GP(4, 3) RDW(2, 1) SB;
  vm_wait<14>();
  pin(h0[1][2]); pin(h1[1][2]); SB;
  a00 = mfma(wa[2], h0[1][2], a00); LDH0(2, 2); SB;
  a10 = mfma(wb[2], h0[1][2], a10); LDH1(2, 2); SB;
  a01 = mfma(wa[2], h1[1][2], a01); GP(4, 4) SB;
  a11 = mfma(wb[2], h1[1][2], a11); GP(4, 5) RDW(2, 2) SB;
  vm_wait<14>();
  pin(h0[1][3]); pin(h1[1][3]); SB;
  a00 = mfma(wa[3], h0[1][3], a00); LDH0(2, 3); SB;
  a10 = mfma(wb[3], h0[1][3], a10); LDH1(2, 3); SB;
  a01 = mfma(wa[3], h1[1][3], a01); GP(4, 6) SB;
  a11 = mfma(wb[3], h1[1][3], a11); GP(4, 7) RDW(2, 3) SB;
  STAMP(43);
  vm_wait<14>();
  pin(h0[2][0]); pin(h1[2][0]); SB;
  a00 = mfma(wa[0], h0[2][0], a00); LDH0(3, 0); SB;
  a10 = mfma(wb[0], h0[2][0], a10); LDH1(3, 0); SB;
  a01 = mfma(wa[0], h1[2][0], a01); GP(5, 0) SB;
  a11 = mfma(wb[0], h1[2][0], a11); GP(5, 1) RDW(3, 0) SB;
  vm_wait<14>();
  pin(h0[2][1]); pin(h1[2][1]); SB;
  a00 = mfma(wa[1], h0[2][1], a00); LDH0(3, 1); SB;
  a10 = mfma(wb[1], h0[2][1], a10); LDH1(3, 1); SB;
  a01 = mfma(wa[1], h1[2][1], a01); GP(5, 2) SB;
  a11 = mfma(wb[1], h1[2][1], a11); GP(5, 3) RDW(3, 1) SB;
  poll(fpoll, c.fo1, ws, 0xfff0000ull);
  vm_wait<15>();
  pin(h0[2][2]); pin(h1[2][2]); SB;
  a00 = mfma(wa[2], h0[2][2], a00); LDH0(3, 2); SB;
  a10 = mfma(wb[2], h0[2][2], a10); LDH1(3, 2); SB;
  a01 = mfma(wa[2], h1[2][2], a01); GP(5, 4) SB;
  a11 = mfma(wb[2], h1[2][2], a11); GP(5, 5) RDW(3, 2) SB;
  vm_wait<15>();
  pin(h0[2][3]); pin(h1[2][3]); SB;
  a00 = mfma(wa[3], h0[2][3], a00); LDH0(3, 3); SB;
  a10 = mfma(wb[3], h0[2][3], a10); LDH1(3, 3); SB;
  a01 = mfma(wa[3], h1[2][3], a01); GP(5, 6) SB;
  a11 = mfma(wb[3], h1[2][3], a11); GP(5, 7) RDW(3, 3) SB;
  asm volatile("" ::"v"(a00), "v"(a01), "v"(a10), "v"(a11));
  vm_wait<8>();
  check(fpoll, c.fo1, ws, tag, 0xfff0000ull);
  STAMP(44);
  vm_wait<15>();
  pin(h0[3][0]); pin(h1[3][0]); SB;
  a00 = mfma(wa[0], h0[3][0], a00); LDH0(4, 0); SB;
  a10 = mfma(wb[0], h0[3][0], a10); LDH1(4, 0); SB;
  a01 = mfma(wa[0], h1[3][0], a01); GP(6, 0) SB;
  a11 = mfma(wb[0], h1[3][0], a11); GP(6, 1) RDW(4, 0) SB;
  vm_wait<15>();
  pin(h0[3][1]); pin(h1[3][1]); SB;
  a00 = mfma(wa[1], h0[3][1], a00); LDH0(4, 1); SB;
  a10 = mfma(wb[1], h0[3][1], a10); LDH1(4, 1); SB;
  a01 = mfma(wa[1], h1[3][1], a01); GP(6, 2) SB;
  a11 = mfma(wb[1], h1[3][1], a11); GP(6, 3) RDW(4, 1) SB;
  vm_wait<14>();
  pin(h0[3][2]); pin(h1[3][2]); SB;
  a00 = mfma(wa[2], h0[3][2], a00); LDH0(4, 2); SB;
  a10 = mfma(wb[2], h0[3][2], a10); LDH1(4, 2); SB;
  a01 = mfma(wa[2], h1[3][2], a01); GP(6, 4) SB;
  a11 = mfma(wb[2], h1[3][2], a11); GP(6, 5) RDW(4, 2) SB;
  vm_wait<14>();
  pin(h0[3][3]); pin(h1[3][3]); SB;
  a00 = mfma(wa[3], h0[3][3], a00); LDH0(4, 3); SB;
  a10 = mfma(wb[3], h0[3][3], a10); LDH1(4, 3); SB;
  a01 = mfma(wa[3], h1[3][3], a01); GP(6, 6) SB;
  a11 = mfma(wb[3], h1[3][3], a11); GP(6, 7) RDW(4, 3) SB;
  STAMP(45);
  vm_wait<14>();
  pin(h0[4][0]); pin(h1[4][0]); SB;
  a00 = mfma(wa[0], h0[4][0], a00); LDH0(5, 0); SB;
  a10 = mfma(wb[0], h0[4][0], a10); LDH1(5, 0); SB;
  a01 = mfma(wa[0], h1[4][0], a01); GP(7, 0) SB;
  a11 = mfma(wb[0], h1[4][0], a11); GP(7, 1) RDW(5, 0) SB;
  vm_wait<14>();
  pin(h0[4][1]); pin(h1[4][1]); SB;
  a00 = mfma(wa[1], h0[4][1], a00); LDH0(5, 1); SB;
  a10 = mfma(wb[1], h0[4][1], a10); LDH1(5, 1); SB;
  a01 = mfma(wa[1], h1[4][1], a01); GP(7, 2) SB;
  a11 = mfma(wb[1], h1[4][1], a11); GP(7, 3) RDW(5, 1) SB;
  vm_wait<14>();
  pin(h0[4][2]); pin(h1[4][2]); SB;
  a00 = mfma(wa[2], h0[4][2], a00); LDH0(5, 2); SB;
  a10 = mfma(wb[2], h0[4][2], a10); LDH1(5, 2); SB;
  a01 = mfma(wa[2], h1[4][2], a01); GP(7, 4) SB;
  a11 = mfma(wb[2], h1[4][2], a11); GP(7, 5) RDW(5, 2) SB;
  vm_wait<14>();
  pin(h0[4][3]); pin(h1[4][3]); SB;
  a00 = mfma(wa[3], h0[4][3], a00); LDH0(5, 3); SB;
  a10 = mfma(wb[3], h0[4][3], a10); LDH1(5, 3); SB;
  a01 = mfma(wa[3], h1[4][3], a01); GP(7, 6) SB;
  a11 = mfma(wb[3], h1[4][3], a11); GP(7, 7) RDW(5, 3) SB;
  STAMP(46);
  vm_wait<14>();
  pin(h0[5][0]); pin(h1[5][0]); SB;
  a00 = mfma(wa[0], h0[5][0], a00); LDH0(6, 0); SB;
  a10 = mfma(wb[0], h0[5][0], a10); LDH1(6, 0); SB;
  a01 = mfma(wa[0], h1[5][0], a01); GP(8, 0) SB;
  a11 = mfma(wb[0], h1[5][0], a11); GP(8, 1) RDW(6, 0) SB;
  poll(fpoll, fo_o, ws, 0xffffffffull);
  vm_wait<15>();
  pin(h0[5][1]); pin(h1[5][1]); SB;
  a00 = mfma(wa[1], h0[5][1], a00); LDH0(6, 1); SB;
  a10 = mfma(wb[1], h0[5][1], a10); LDH1(6, 1); SB;
  a01 = mfma(wa[1], h1[5][1], a01); GP(8, 2) SB;
  a11 = mfma(wb[1], h1[5][1], a11); GP(8, 3) RDW(6, 1) SB;
  vm_wait<15>();
  pin(h0[5][2]); pin(h1[5][2]); SB;
  a00 = mfma(wa[2], h0[5][2], a00); LDH0(6, 2); SB;
  a10 = mfma(wb[2], h0[5][2], a10); LDH1(6, 2); SB;
  a01 = mfma(wa[2], h1[5][2], a01); GP(8, 4) SB;
  a11 = mfma(wb[2], h1[5][2], a11); GP(8, 5) RDW(6, 2) SB;
  vm_wait<15>();
  pin(h0[5][3]); pin(h1[5][3]); SB;
  a00 = mfma(wa[3], h0[5][3], a00); LDH0(6, 3); SB;
  a10 = mfma(wb[3], h0[5][3], a10); LDH1(6, 3); SB;
  a01 = mfma(wa[3], h1[5][3], a01); SB;
  a11 = mfma(wb[3], h1[5][3], a11); RDW(6, 3) SB;
  ld16<0>(xres0, oo, sx2, 0u); ld16<0>(xres1, oo, sx2, 32u * 896u); ld16<0>(bres, (u32)(hh * 16), sb2, (u32)((ct * 64 + kq * 16) * 2));
  asm volatile("" ::"v"(a00), "v"(a01), "v"(a10), "v"(a11));
  vm_wait<13>();
  check(fpoll, fo_o, ws, tag, 0xffffffffull);
  STAMP(47);
  vm_wait<16>();
  pin(h0[6][0]); pin(h1[6][0]); SB;
  a00 = mfma(wa[0], h0[6][0], a00); LDH0(7, 0); SB;
  a10 = mfma(wb[0], h0[6][0], a10); LDH1(7, 0); SB;
  a01 = mfma(wa[0], h1[6][0], a01); SB;
  a11 = mfma(wb[0], h1[6][0], a11); RDW(7, 0) SB;
  vm_wait<13>();
  pin(h0[6][1]); pin(h1[6][1]); SB;
  a00 = mfma(wa[1], h0[6][1], a00); LDH0(7, 1); SB;
  a10 = mfma(wb[1], h0[6][1], a10); LDH1(7, 1); SB;
  a01 = mfma(wa[1], h1[6][1], a01); SB;
  a11 = mfma(wb[1], h1[6][1], a11); RDW(7, 1) SB;
  vm_wait<11>();
  pin(h0[6][2]); pin(h1[6][2]); SB;
  a00 = mfma(wa[2], h0[6][2], a00); LDH0(7, 2); SB;
  a10 = mfma(wb[2], h0[6][2], a10); LDH1(7, 2); SB;
  a01 = mfma(wa[2], h1[6][2], a01); SB;
  a11 = mfma(wb[2], h1[6][2], a11); RDW(7, 2) SB;
  vm_wait<9>();
  pin(h0[6][3]); pin(h1[6][3]); SB;
  a00 = mfma(wa[3], h0[6][3], a00); LDH0(7, 3); SB;
  a10 = mfma(wb[3], h0[6][3], a10); LDH1(7, 3); SB;
  a01 = mfma(wa[3], h1[6][3], a01); SB;
  a11 = mfma(wb[3], h1[6][3], a11); RDW(7, 3) SB;
  STAMP(48);
  vm_wait<6>();
  pin(h0[7][0]); pin(h1[7][0]); SB;
  a00 = mfma(wa[0], h0[7][0], a00); LDH0(8, 0); SB;
  a10 = mfma(wb[0], h0[7][0], a10); LDH1(8, 0); SB;
  a01 = mfma(wa[0], h1[7][0], a01); SB;
  a11 = mfma(wb[0], h1[7][0], a11); RDW(8, 0) SB;
  vm_wait<6>();
  pin(h0[7][1]); pin(h1[7][1]); SB;
  a00 = mfma(wa[1], h0[7][1], a00); LDH0(8, 1); SB;
  a10 = mfma(wb[1], h0[7][1], a10); LDH1(8, 1); SB;
  a01 = mfma(wa[1], h1[7][1], a01); SB;
  a11 = mfma(wb[1], h1[7][1], a11); RDW(8, 1) SB;
  vm_wait<6>();
  pin(h0[7][2]); pin(h1[7][2]); SB;
  a00 = mfma(wa[2], h0[7][2], a00); LDH0(8, 2); SB;
  a10 = mfma(wb[2], h0[7][2], a10); LDH1(8, 2); SB;
  a01 = mfma(wa[2], h1[7][2], a01); SB;
  a11 = mfma(wb[2], h1[7][2], a11); RDW(8, 2) SB;
  vm_wait<6>();
  pin(h0[7][3]); pin(h1[7][3]); SB;
  a00 = mfma(wa[3], h0[7][3], a00); SB;
  a10 = mfma(wb[3], h0[7][3], a10); SB;
  a01 = mfma(wa[3], h1[7][3], a01); SB;
  a11 = mfma(wb[3], h1[7][3], a11); SB;
  STAMP(49);
  vm_wait<0>();
  pin(h0[8][0]); pin(h1[8][0]); pin(h0[8][1]); pin(h1[8][1]); pin(h0[8][2]); pin(h1[8][2]); SB;
  a10 = mfma(wb[0], h0[8][0], a10); SB;
  a10 = mfma(wb[1], h0[8][1], a10); SB;
  a10 = mfma(wb[2], h0[8][2], a10); SB;
  a00 = mfma(wa[0], h0[8][0], a00); SB;
  a00 = mfma(wa[1], h0[8][1], a00); SB;
  a00 = mfma(wa[2], h0[8][2], a00); SB;
  a11 = mfma(wb[0], h1[8][0], a11); W4(r0 + 4096, a10, 0); SB;
  a11 = mfma(wb[1], h1[8][1], a11); W4(r0 + 5120, a10, 4); SB;
  a11 = mfma(wb[2], h1[8][2], a11); W4(r0 + 6144, a10, 8); SB;
  a01 = mfma(wa[0], h1[8][0], a01); W4(r0 + 7168, a10, 12); SB;
  a01 = mfma(wa[1], h1[8][1], a01); W4(r0 + 2048, a00, 8); SB;
  a01 = mfma(wa[2], h1[8][2], a01); W4(r0 + 3072, a00, 12); SB;
#undef SB
#undef GP
#undef RDW
#undef LDH1
#undef LDH0
  STAMP(50);
  W4(r1 + 4096, a11, 0); W4(r1 + 5120, a11, 4); W4(r1 + 6144, a11, 8); W4(r1 + 7168, a11, 12);
  W4(r1 + 2048, a01, 8); W4(r1 + 3072, a01, 12);
#undef W4
  vm_wait<0>();
  pin(xres0); pin(xres1); pin(bres);
  auto lo_f = [](u32 v) { return u2f(v << 16); };
  auto hi_f = [](u32 v) { return u2f(v & 0xffff0000u); };
  f32x4 a[2], d[2];
  {
    const auto b0 = __builtin_amdgcn_permlane32_swap(bres[0], bres[2], false, false), b1 = __builtin_amdgcn_permlane32_swap(bres[1], bres[3], false, false);
    const f32x4 ba = {lo_f(b0[0]), hi_f(b0[0]), lo_f(b1[0]), hi_f(b1[0])}, bd = {lo_f(b0[1]), hi_f(b0[1]), lo_f(b1[1]), hi_f(b1[1])};
    const auto s0 = __builtin_amdgcn_permlane32_swap(xres0[0], xres0[2], false, false), s1 = __builtin_amdgcn_permlane32_swap(xres0[1], xres0[3], false, false);
    const auto u0 = __builtin_amdgcn_permlane32_swap(xres1[0], xres1[2], false, false), u1 = __builtin_amdgcn_permlane32_swap(xres1[1], xres1[3], false, false);
    a[0] = f32x4{lo_f(s0[0]), hi_f(s0[0]), lo_f(s1[0]), hi_f(s1[0])} + ba + f32x4{a00[0], a00[1], a00[2], a00[3]};
    d[0] = f32x4{lo_f(s0[1]), hi_f(s0[1]), lo_f(s1[1]), hi_f(s1[1])} + bd + f32x4{a00[4], a00[5], a00[6], a00[7]};
    a[1] = f32x4{lo_f(u0[0]), hi_f(u0[0]), lo_f(u1[0]), hi_f(u1[0])} + ba + f32x4{a01[0], a01[1], a01[2], a01[3]};
    d[1] = f32x4{lo_f(u0[1]), hi_f(u0[1]), lo_f(u1[1]), hi_f(u1[1])} + bd + f32x4{a01[4], a01[5], a01[6], a01[7]};
  }
  u32 ra[2][3];
#pragma unroll
  for (int t = 1; t < 4; ++t) {
    const int k = kq ^ t;
    ra[0][t - 1] = (u32)(k * 8192 + t * 2048) + lo16;
    ra[1][t - 1] = (u32)((k == 0 ? 5 : (k == 3 ? 13 : 9 + k)) * 8192 + t * 2048) + lo16;
  }
  asm volatile("" : "+v"(a[0]), "+v"(d[0]), "+v"(a[1]), "+v"(d[1]), "+v"(ra[0][0]), "+v"(ra[0][1]), "+v"(ra[0][2]), "+v"(ra[1][0]), "+v"(ra[1][1]), "+v"(ra[1][2]));
  wg_barrier();
  STAMP(51);
  f32x4 pa[2][3], pd[2][3];
#pragma unroll
  for (int u = 0; u < 2; ++u)
#pragma unroll
    for (int t = 0; t < 3; ++t) { pa[u][t] = *(LAS f32x4*)(L + ra[u][t]); pd[u][t] = *(LAS f32x4*)(L + ra[u][t] + 1024); }
  __builtin_amdgcn_sched_barrier(0);
#pragma unroll
  for (int u = 0; u < 2; ++u) {
    const f32x4 ya = (a[u] + pa[u][0]) + (pa[u][1] + pa[u][2]), yd = (d[u] + pd[u][0]) + (pd[u][1] + pd[u][2]);
    const auto r0 = __builtin_amdgcn_permlane32_swap(pk(ya[0], ya[1]), pk(yd[0], yd[1]), false, false);
    const auto r1 = __builtin_amdgcn_permlane32_swap(pk(ya[2], ya[3]), pk(yd[2], yd[3]), false, false);
    st16(v4u{r0[0], r1[0], r0[1], r1[1]}, oo + (u32)(u * 32 * 896), sout);
  }
  STAMP(52);
  CSTAMP(62);
}

__global__ void __launch_bounds__(512, 2)
fused_block(const u16* x, const u16* wqkv, const u16* wo, const u16* w1, const u16* b1, const u16* w2, const u16* b2, u16* out, char* wsp) {
  asm volatile("" : "+s"(x), "+s"(wqkv), "+s"(wo), "+s"(w1), "+s"(b1), "+s"(w2), "+s"(wsp));
  extern __shared__ __attribute__((aligned(16))) char smem_[];
  LAS char* const L = (LAS char*)smem_;
  const u32 lds0 = (u32)(size_t)L;
  const int lane = threadIdx.x & 63;
  const int w = __builtin_amdgcn_readfirstlane(threadIdx.x >> 6);
  const int m = w & 3, grp = w >> 2;
  const int b = blockIdx.x, xcd = b & 7, r = b >> 3;
  const int r32 = lane & 31, hh = lane >> 5;
  Ctx c;
  c.ws = make_srd(wsp, WS_END);
  const v4i sx = make_srd(x, S_ * D_ * 2);
  STAMP_DECL;
  STAMP(0);
  CSTAMP(60);

  const int rt = 4 * (xcd >> 1) + (r & 3);
  const int r8 = lane >> 3, c8 = lane & 7;
  const bool heavy = r >= 28;
  const int cg = heavy ? (xcd & 1) : 2 + 7 * (xcd & 1) + (r >> 2);
  const bool s1q = cg < 5;
  c.sqkv = make_srd(wqkv, D_ * 3 * D_ * 2);
  c.sw1 = make_srd(w1, D_ * F_ * 2);
  u32 vtch = 0;
  if (w < 2) {
    u64 pc;
    asm volatile("s_getpc_b64 %0" : "=s"(pc));
    const u64 a = lane < 56 ? (u64)x + (u64)(rt * 114688 + ((r >> 2) * 112 + w * 56 + lane) * 128)
                            : (pc & ~127ull) + (u64)(min(r * 16 + w * 8 + lane - 56, CODE_LINES - 1) * 128);
    asm volatile("global_load_dword %0, %1, off" : "=v"(vtch) : "v"(a) : "memory");
  }
  v4u xr[14];
  {
    const u32 xo = (u32)((rt * 128 + 16 * w + r8) * 896 + c8 * 16);
#define LDX(g, i) ld16<(i) * 128>(xr[(g) * 7 + (i)], xo, sx, (u32)((g) * 7168))
    LDX(0, 0); LDX(0, 1); LDX(0, 2); LDX(0, 3); LDX(0, 4); LDX(0, 5); LDX(0, 6);
    LDX(1, 0); LDX(1, 1); LDX(1, 2); LDX(1, 3); LDX(1, 4); LDX(1, 5); LDX(1, 6);
#undef LDX
  }
  u32 wtch = 0;
  {
    u64 a;
    if (m < 2) {
      const int half = lane >= 28 && lane < 56, row = rt * 28 + lane - (lane >= 56 ? 56 : 28 * half);
      const bool q = m == 0 && (half == 0 || s1q);
      const int strip = m == 0 ? (half == 0 ? cg : (s1q ? 16 + cg : cg - 5)) : (half == 0 ? 11 + cg : 27);
      a = (q ? (u64)wqkv : (u64)w1) + (u64)(row * (q ? 2688 : 3584) + strip * 128);
    } else {
      a = lane < 49 ? (u64)w2 + (u64)((b * 49 + lane) * 128) : (u64)wo + (u64)(min(b * 13 + lane - 49, 3135) * 128);
    }
    if (grp == 1 && ((m < 2 && (TOUCH & 2)) || (m == 2 && (TOUCH & 4)))) asm volatile("global_load_dword %0, %1, off" : "=v"(wtch) : "v"(a) : "memory");
    else asm volatile("buffer_load_dword %0, off, %1, 0" : "=v"(wtch) : "s"(sx) : "memory");
  }
  const int tt = 4 * xcd + (r & 3), ct = r >> 2;
  c.sw2 = make_srd(w2, F_ * D_ * 2);
  const u32 gsw = (u32)((c8 ^ (((r8 >> 1) & 1) << 2)) * 16);
  c.goff_q = (u32)(r8 * 2688) + gsw; c.goff_1 = (u32)(r8 * 3584) + gsw; c.goff_2 = (u32)(r8 * 896) + gsw;
  {
    const int tq = (lane & 15) >> 2, tp = lane & 3, tg = lane >> 4;
    c.trl = (u32)(((tg >> 1) * 8 + tq) * 128 + (((2 * (tg & 1) + (tp >> 1)) ^ (((tq >> 1) & 1) << 2)) << 4) + (tp & 1) * 8);
  }
  c.m = m; c.b = b; c.xcd = xcd; c.cg = cg; c.ct = ct; c.tt = tt; c.heavy = heavy;
  c.tb1 = 4 * rt + m;
  c.lo16 = (u32)(lane * 16);
  c.gc1 = -0.1029432397f;
  c.hd = 2 * xcd + (r & 1); c.ia = 15 - (r >> 1);
  c.c0 = (28 * c.hd) >> 3;
  if (grp == 0) {
    const int n = (lane & 7) >> 1, tbq = 4 * c.ia + m;
    int kbl = 4 * c.ia - 8 + 3 * m + n;
    kbl = (kbl < 0 || n == 3) ? tbq : kbl;
    const int fbl = (8 * c.c0 + (lane & 1) * 31) >> 5;
    c.fo1 = (u32)(WS_F + kbl * 1024 + (fbl + (n < 3 ? 14 : 0)) * 8);
    c.fo2 = (u32)(WS_F + kbl * 1024 + 256 + fbl * 8);
  } else {
    c.fo1 = (u32)(WS_F + (2 * tt + ((lane >> 1) & 1)) * 1024 + fh_off(hblock(lane < 28 ? lane >> 2 : 0, 2 * m + (lane & 1))));
    c.fo2 = 0;
  }
#pragma unroll
  for (int j = 0; j < 4; ++j) c.xb[j] = (u32)(65536 - grp * 16384 + m * 4096 + (2 * j + hh) * 512 + ((r32 ^ (2 * j + hh)) << 4));
  asm volatile("" : "+v"(c.goff_q), "+v"(c.goff_1), "+v"(c.goff_2), "+v"(c.trl), "+v"(c.lo16), "+v"(c.fo1), "+v"(c.fo2));
  asm volatile("" : "+v"(c.xb[0]), "+v"(c.xb[1]), "+v"(c.xb[2]), "+v"(c.xb[3]), "+v"(c.gc1));
  u64 epoch;
  {
    u32 sel = ((lane & 15) == 4 * (lane >> 4)) ? 0x3f803f80u : 0u;
    v4u asum = {sel, sel, sel, sel};
    const int l3 = lane & 3;
    u32 dm0 = l3 == 0 ? 0xffffu : (l3 == 1 ? 0xffff0000u : 0u), dm1 = l3 == 2 ? 0xffffu : (l3 == 3 ? 0xffff0000u : 0u);
    f32x4 dsel = {l3 == 0 ? 1.f / 448.f : 0.f, l3 == 1 ? 1.f / 448.f : 0.f, l3 == 2 ? 1.f / 448.f : 0.f, l3 == 3 ? 1.f / 448.f : 0.f};
    u32 dsto[2][2];
#pragma unroll
    for (int g = 0; g < 2; ++g) {
      dsto[g][0] = (u32)(49152 + (w >> 1) * 4096 + c8 * 512 + (((16 * (w & 1) + 8 * g + r8) ^ c8) << 4));
      dsto[g][1] = dsto[g][0] + 65536u;
    }
    Stream g0;
    g0.srd = c.sqkv; g0.voff = c.goff_q; g0.d8 = 8 * 2688 - 1024;
    u32 l0 = lds0 + (u32)(m * 4096), s0 = (u32)(m * 32 * 2688 + cg * 128);
    keep_s(l0); keep_s(s0);
    asm volatile("" : "+v"(asum), "+v"(dm0), "+v"(dm1), "+v"(dsel), "+v"(dsto[0][0]), "+v"(dsto[0][1]), "+v"(dsto[1][0]), "+v"(dsto[1][1]));
#define LNSTAT(g, i) { vm_wait<((g) == 0 ? 14 : 7) - (i)>(); pin(xr[(g) * 7 + (i)]); const v4u d = xr[(g) * 7 + (i)]; \
        const s16x4 lo = __builtin_bit_cast(s16x4, v2u{d[0], d[1]}), hi = __builtin_bit_cast(s16x4, v2u{d[2], d[3]}); \
        qa[g] = __builtin_amdgcn_mfma_f32_4x4x4bf16_1k(lo, lo, qa[g], 0, 0, 0); \
        s1[g] = __builtin_amdgcn_mfma_f32_16x16x32_bf16(__builtin_bit_cast(bf16x8, asum), __builtin_bit_cast(bf16x8, d), s1[g], 0, 0, 0); \
        qa[g] = __builtin_amdgcn_mfma_f32_4x4x4bf16_1k(hi, hi, qa[g], 0, 0, 0); }
#define LNMUL(g, i) { const v4u d = xr[(g) * 7 + (i)]; \
        y0[(i) & 1] = __builtin_amdgcn_mfma_f32_4x4x4bf16_1k(an, __builtin_bit_cast(s16x4, v2u{d[0], d[1]}), cc, 0, 0, 0); \
        y1[(i) & 1] = __builtin_amdgcn_mfma_f32_4x4x4bf16_1k(an, __builtin_bit_cast(s16x4, v2u{d[2], d[3]}), cc, 0, 0, 0); }
#define LNOUT(g, i) { if ((i) < 6) LNMUL(g, ((i) < 6 ? (i) + 1 : 0)) \
        if ((g) == 0) LNSTAT(1, i) \
        __builtin_amdgcn_sched_barrier(0); \
        *(LAS v4u*)(L + ((i) == 6 ? dsto[g][0] : ((i) < 3 ? dsto[g][0] + 16384 * ((i) + 1) : dsto[g][1] + 16384 * ((i) - 3)))) = \
            v4u{pk(y0[(i) & 1][0], y0[(i) & 1][1]), pk(y0[(i) & 1][2], y0[(i) & 1][3]), pk(y1[(i) & 1][0], y1[(i) & 1][1]), pk(y1[(i) & 1][2], y1[(i) & 1][3])}; \
        __builtin_amdgcn_sched_barrier(0); }
#define LNGROUP(g) { \
      LSTAMP(1 + (g)); \
      float su = s1[g][0] * (1.f / 448.f), sq = (qa[g][0] * dsel[0] + qa[g][1] * dsel[1]) + (qa[g][2] * dsel[2] + qa[g][3] * dsel[3]); \
      su = dpp_add<0xB1>(su); sq = dpp_add<0xB1>(sq); su = dpp_add<0x4E>(su); sq = dpp_add<0x4E>(sq); \
      su = dpp_add<0x141>(su); sq = dpp_add<0x141>(sq); \
      const float rstd = __builtin_amdgcn_rsqf(__builtin_fmaxf(__builtin_fmaf(-su, su, sq), 0.f) + 1e-5f); \
      const u32 rr = pk(rstd, rstd); \
      const float nc = -su * u2f(rr << 16); \
      const f32x4 cc = {nc, nc, nc, nc}; \
      const s16x4 an = __builtin_bit_cast(s16x4, v2u{rr & dm0, rr & dm1}); \
      f32x4 y0[2], y1[2]; \
      LNMUL(g, 0) \
      LNOUT(g, 0) LNOUT(g, 1) LNOUT(g, 2) LNOUT(g, 3) LNOUT(g, 4) LNOUT(g, 5) LNOUT(g, 6) }
#define LSTAMP(i) do {} while (0)
    f32x4 s1[2], qa[2];
    s1[0] = s1[1] = qa[0] = qa[1] = f32x4{0.f, 0.f, 0.f, 0.f};
    asm volatile("s_load_dwordx2 %0, %1, 0x0" : "=s"(epoch) : "s"(wsp));
#define SB __builtin_amdgcn_sched_barrier(0);
    LNSTAT(0, 0) SB LNSTAT(0, 1) SB LNSTAT(0, 2) SB LNSTAT(0, 3) SB LNSTAT(0, 4) SB LNSTAT(0, 5) SB LNSTAT(0, 6) SB
#undef SB
    pin(vtch);
    LNGROUP(0)
    LNGROUP(1)
#undef LNGROUP
#undef LNOUT
#undef LNMUL
#undef LNSTAT
    if (grp == 0) {
      piece128<0>(g0, l0, s0, 128u * 2688u); piece128<1>(g0, l0, s0, 128u * 2688u); piece128<2>(g0, l0, s0, 128u * 2688u);
      piece128<3>(g0, l0, s0, 128u * 2688u); piece128<4>(g0, l0, s0, 128u * 2688u); piece128<5>(g0, l0, s0, 128u * 2688u);
      piece128<6>(g0, l0, s0, 128u * 2688u); piece128<7>(g0, l0, s0, 128u * 2688u); piece128<8>(g0, l0, s0, 128u * 2688u);
      piece128<9>(g0, l0, s0, 128u * 2688u); piece128<10>(g0, l0, s0, 128u * 2688u); piece128<11>(g0, l0, s0, 128u * 2688u);
    }
  }
  {
    const v4i sb1 = make_srd(b1, F_ * 2);
    const u32 bo = (u32)(r32 * 2);
    ld2(c.bias1, bo, sb1, (u32)(((s1q ? 0 : cg - 5) * 64 + grp * 32) * 2));
    ld2(c.bias2, bo, sb1, (u32)(((11 + cg) * 64 + grp * 32) * 2));
    ld2(c.bias3, bo, sb1, (u32)((27 * 64 + (xcd & 1) * 32) * 2));
  }
  LSTAMP(3);
  wg_barrier();
  asm volatile("" : "+s"(epoch));
  c.wtch = wtch;
  STAMP(4);
  epoch += 1;
  c.tag = v2u{(u32)epoch, (u32)(epoch >> 32)};
  asm volatile("" : "+v"(c.tag));

  if (grp == 0) group_a(c, L STAMP_PASS); else group_b(c, L STAMP_PASS);
  STAMP_OUT();
}

extern "C" void kernel_launch(void* const* d_in, const int* in_sizes, int n_in, void* d_out, int out_size, void* d_ws, size_t ws_size,
                              hipStream_t stream) {
  (void)hipFuncSetAttribute((const void*)fused_block, hipFuncAttributeMaxDynamicSharedMemorySize, LDS_BYTES);
  fused_block<<<256, 512, LDS_BYTES, stream>>>((const u16*)d_in[0], (const u16*)d_in[1], (const u16*)d_in[2], (const u16*)d_in[4],
                                               (const u16*)d_in[5], (const u16*)d_in[6], (const u16*)d_in[7], (u16*)d_out, (char*)d_ws);
}
```

```cpp
#include <hip/hip_runtime.h>
#include <type_traits>

typedef unsigned short u16;
typedef unsigned int u32;
typedef unsigned long long u64;
typedef u32 v4u __attribute__((ext_vector_type(4)));
typedef u32 v2u __attribute__((ext_vector_type(2)));
typedef int v4i __attribute__((ext_vector_type(4)));
typedef short s16x4 __attribute__((ext_vector_type(4)));
typedef __bf16 bf16x8 __attribute__((ext_vector_type(8)));
typedef __bf16 bf16x2 __attribute__((ext_vector_type(2)));
typedef float f32x2 __attribute__((ext_vector_type(2)));
typedef float f32x4 __attribute__((ext_vector_type(4)));
typedef float f32x16 __attribute__((ext_vector_type(16)));
#define GAS __attribute__((address_space(1)))
#define LAS __attribute__((address_space(3)))
#define DI __device__ __forceinline__

#define APRIO 0
#define PSLEEP 2
#define BSLEEP 0
#define LNPRIO 1
#define BPRIO 0
#define NTL ""
#define WSEND 0
#define QPOS 24
#define EARLYB1 1
#define BPOST 0
#define LSLEEP 0
#define PDEPTH 5
#define PRE 1
#define YPRIO 0
#define TOUCH 7
#define STAMPS 0

constexpr int S_ = 2048, D_ = 448, F_ = 1792;
constexpr int WS_E = 0;
constexpr int WS_F = 0;
constexpr int WS_FO = WS_F + 64 * 1024;
constexpr int FL0 = 4096;
constexpr int WS_QK = 2 << 20;
constexpr int QK_TB = 112 * 512;
constexpr int WS_VT = WS_QK + 64 * QK_TB;
constexpr int VT_TB = 4 * 448 * 16;
constexpr int WS_HH = WS_VT + 64 * VT_TB;
constexpr int HH_TB = 280 * 512;
constexpr int WS_END = WS_HH + 64 * HH_TB;
constexpr int WS_DBG = 32 << 20;
constexpr int LDS_BYTES = 163840;
constexpr int CODE_LINES = 280;
constexpr u32 SPIN_CAP = 1u << 16;

DI v4i make_srd(const void* p, u32 bytes) {
  const u64 a = (u64)p;
  v4i r;
  r[0] = (int)(u32)a; r[1] = (int)((u32)(a >> 32) & 0xffffu); r[2] = (int)bytes; r[3] = 0x00020000;
  return r;
}
template <int OFF> DI void ld16(v4u& d, u32 voff, v4i srd, u32 soff) {
  asm volatile("buffer_load_dwordx4 %0, %1, %2, %3 offen offset:%4" : "=v"(d) : "v"(voff), "s"(srd), "s"(soff), "i"(OFF) : "memory");
}
template <int OFF> DI void ld16c(v4u& d, u32 voff, v4i srd, u32 soff) {
  asm volatile("buffer_load_dwordx4 %0, %1, %2, %3 offen offset:%4 sc1" NTL : "=v"(d) : "v"(voff), "s"(srd), "s"(soff), "i"(OFF) : "memory");
}
DI void ld2(u32& d, u32 voff, v4i srd, u32 soff) {
  asm volatile("buffer_load_ushort %0, %1, %2, %3 offen" : "=v"(d) : "v"(voff), "s"(srd), "s"(soff) : "memory");
}
template <int OFF> DI void st16c(v4u d, u32 voff, v4i srd, u32 soff) {
  asm volatile("buffer_store_dwordx4 %0, %1, %2, %3 offen offset:%4 sc1\n\ts_nop 1" ::"v"(d), "v"(voff), "s"(srd), "s"(soff), "i"(OFF) : "memory");
}
DI void st16(v4u d, u32 voff, v4i srd) {
  asm volatile("buffer_store_dwordx4 %0, %1, %2, 0 offen\n\ts_nop 1" ::"v"(d), "v"(voff), "s"(srd) : "memory");
}
template <int N> DI void vm_wait() { asm volatile("s_waitcnt vmcnt(%0)" ::"i"(N) : "memory"); }
DI void pin(v4u& a) { asm volatile("" : "+v"(a)); }
DI void pin(v2u& a) { asm volatile("" : "+v"(a)); }
DI void pin(u32& a) { asm volatile("" : "+v"(a)); }
DI void keep_s(u32& a) { a = __builtin_amdgcn_readfirstlane(a); asm volatile("" : "+s"(a)); }
DI void wg_barrier() { asm volatile("s_waitcnt lgkmcnt(0)\n\ts_barrier" ::: "memory"); }
DI f32x16 mfma(v4u a, v4u b, f32x16 c) {
  return __builtin_amdgcn_mfma_f32_32x32x16_bf16(__builtin_bit_cast(bf16x8, a), __builtin_bit_cast(bf16x8, b), c, 0, 0, 0);
}
DI u32 f2u(float a) { return __builtin_bit_cast(u32, a); }
DI float u2f(u32 a) { return __builtin_bit_cast(float, a); }
DI u32 pk(float a, float b) { f32x2 f = {a, b}; return __builtin_bit_cast(u32, __builtin_convertvector(f, bf16x2)); }
DI v4u tr_frag(LAS char* p) {
  const s16x4 a = __builtin_amdgcn_ds_read_tr16_b64_v4i16((LAS s16x4*)p);
  const s16x4 b = __builtin_amdgcn_ds_read_tr16_b64_v4i16((LAS s16x4*)(p + 512));
  const v2u x = __builtin_bit_cast(v2u, a), y = __builtin_bit_cast(v2u, b);
  return v4u{x[0], x[1], y[0], y[1]};
}
template <int C> DI float dpp_add(float x) {
  return x + __builtin_bit_cast(float, __builtin_amdgcn_update_dpp(0, __builtin_bit_cast(int, x), C, 0xf, 0xf, true));
}
DI float swap32(float x) {
  const u32 u = __builtin_bit_cast(u32, x);
  const auto r = __builtin_amdgcn_permlane32_swap(u, u, false, false);
  return __builtin_bit_cast(float, (threadIdx.x & 32) ? r[0] : r[1]);
}
DI void pack_tile(const f32x16& a, v4u& o0, v4u& o1) {
  u32 p[8];
#pragma unroll
  for (int i = 0; i < 8; ++i) p[i] = pk(a[2 * i], a[2 * i + 1]);
  const auto r0 = __builtin_amdgcn_permlane32_swap(p[0], p[2], false, false);
  const auto r1 = __builtin_amdgcn_permlane32_swap(p[1], p[3], false, false);
  const auto r2 = __builtin_amdgcn_permlane32_swap(p[4], p[6], false, false);
  const auto r3 = __builtin_amdgcn_permlane32_swap(p[5], p[7], false, false);
  o0 = v4u{r0[0], r1[0], r0[1], r1[1]};
  o1 = v4u{r2[0], r3[0], r2[1], r3[1]};
}
DI void post_flag(u32 off, v2u tag, v4i ws) {
  asm volatile("s_mov_b64 exec, 0xffff\n\tbuffer_store_dwordx2 %0, %3, %1, %2 offen sc1\n\ts_mov_b64 exec, -1"
               ::"v"(tag), "s"(ws), "s"(off), "v"((threadIdx.x & 63u) * 8u) : "memory");
}
DI u32 fl(u32 slot8) { return FL0 + slot8 * 16u; }
#define STAMP(i) do {} while (0)
#define STAMP_DECL
#define STAMP_ARG
#define STAMP_PASS
#define CSTAMP(i) do {} while (0)
#define STAMP_OUT() do {} while (0)

struct Stream { v4i srd; u32 voff, so, d8; };
template <int Q> DI void piece(Stream& g) {
  asm volatile("buffer_load_dwordx4 %1, %2, %0 offen offset:%4 lds\n\ts_add_u32 %0, %0, %3" : "+s"(g.so)
               : "v"(g.voff), "s"(g.srd), "s"(g.d8), "i"(Q * 1024) : "memory", "scc");
}
template <int Q> DI void piece_m0(Stream& g, u32 lds, u32 so) {
  g.so = so;
  asm volatile("s_mov_b32 m0, %4\n\ts_nop 0\n\tbuffer_load_dwordx4 %1, %2, %0 offen lds\n\ts_add_u32 %0, %0, %3" : "+s"(g.so)
               : "v"(g.voff), "s"(g.srd), "s"(g.d8), "s"(lds) : "memory", "scc");
}
template <int P> DI void piece128(Stream& g, u32 lds, u32 so, u32 s128) {
  if constexpr ((P & 3) == 0) piece_m0<0>(g, lds + (P >> 2) * 16384, so + (P >> 2) * s128);
  else piece<(P & 3)>(g);
}
template <int LO, int HI> DI void pieces128(Stream& g, u32 lds, u32 so, u32 s128) {
  if constexpr (LO < HI) { piece128<LO>(g, lds, so, s128); pieces128<LO + 1, HI>(g, lds, so, s128); }
}
DI void raw_barrier() { asm volatile("s_barrier" ::: "memory"); }
DI f32x16 bias_init(u32 bias) {
  f32x16 z;
#pragma unroll
  for (int i = 0; i < 16; ++i) z[i] = 0.f;
  const v4u bf = {bias, 0u, 0u, 0u};
  const v4u one = {(threadIdx.x & 32) ? 0u : 0x3f80u, 0u, 0u, 0u};
  return mfma(bf, one, z);
}

template <bool SWAP, bool ZERO, int N1 = 4, int N2 = 24, class H>
DI f32x16 gemm1_block(v4u (&xa)[28], LAS char* p1, LAS char* p2, LAS char* p3, f32x16 acc, H&& hook) {
  constexpr int PD = PDEPTH;
  auto rd = [&](int s) { return s < N1 ? tr_frag(p1 + s * 2048) : (s < N2 ? tr_frag(p2 + (s - N1) * 2048) : tr_frag(p3 + (s - N2) * 2048)); };
  __builtin_amdgcn_s_setprio(1);
  v4u wf[PD];
#pragma unroll
  for (int s = 0; s < PD; ++s) wf[s] = rd(s);
  if constexpr (ZERO) {
#pragma unroll
    for (int i = 0; i < 16; ++i) acc[i] = 0.f;
  }
  __builtin_amdgcn_sched_barrier(0);
#define G1STEP(s) { \
    const v4u cur = wf[(s) % PD]; \
    if ((s) == 24 || (s) == 20) asm volatile("" ::"v"(acc)); \
    hook(std::integral_constant<int, s>{}); \
    acc = SWAP ? mfma(xa[s], cur, acc) : mfma(cur, xa[s], acc); \
    if ((s) + PD < 28) wf[(s) % PD] = rd((s) + PD); \
    __builtin_amdgcn_sched_barrier(0); }
  G1STEP(0) G1STEP(1) G1STEP(2) G1STEP(3) G1STEP(4) G1STEP(5) G1STEP(6) G1STEP(7) G1STEP(8) G1STEP(9)
  G1STEP(10) G1STEP(11) G1STEP(12) G1STEP(13) G1STEP(14) G1STEP(15) G1STEP(16) G1STEP(17) G1STEP(18) G1STEP(19)
  G1STEP(20) G1STEP(21) G1STEP(22) G1STEP(23) G1STEP(24) G1STEP(25) G1STEP(26) G1STEP(27)
#undef G1STEP
  asm volatile("" ::"v"(acc));
  __builtin_amdgcn_s_setprio(0);
  return acc;
}

template <bool LAST = false>
DI void gelu4(u32& o0, u32& o1, float a, float b, float c, float d, float c1) {
  float t0, t1, t2, t3;
  asm volatile(
      "v_mul_f32 %2, %6, %6\n\tv_mul_f32 %3, %7, %7\n\tv_mul_f32 %4, %8, %8\n\tv_mul_f32 %5, %9, %9\n\t"
      "v_fmaak_f32 %2, %2, %10, 0xc0135761\n\tv_fmaak_f32 %3, %3, %10, 0xc0135761\n\t"
      "v_fmaak_f32 %4, %4, %10, 0xc0135761\n\tv_fmaak_f32 %5, %5, %10, 0xc0135761\n\t"
      "v_mul_f32 %2, %2, %6\n\tv_mul_f32 %3, %3, %7\n\tv_mul_f32 %4, %4, %8\n\tv_mul_f32 %5, %5, %9\n\t"
      "v_exp_f32 %2, %2\n\tv_exp_f32 %3, %3\n\tv_exp_f32 %4, %4\n\tv_exp_f32 %5, %5\n\t"
      "v_add_f32 %2, 1.0, %2\n\tv_add_f32 %3, 1.0, %3\n\tv_add_f32 %4, 1.0, %4\n\tv_add_f32 %5, 1.0, %5\n\t"
      "v_rcp_f32 %2, %2\n\tv_rcp_f32 %3, %3\n\tv_rcp_f32 %4, %4\n\tv_rcp_f32 %5, %5\n\t"
      "v_mul_f32 %2, %2, %6\n\tv_mul_f32 %3, %3, %7\n\tv_mul_f32 %4, %4, %8\n\tv_mul_f32 %5, %5, %9\n\t"
      "v_cvt_pk_bf16_f32 %0, %2, %3\n\tv_cvt_pk_bf16_f32 %1, %4, %5"
      : "=&v"(o0), "=&v"(o1), "=&v"(t0), "=&v"(t1), "=&v"(t2), "=&v"(t3) : "v"(a), "v"(b), "v"(c), "v"(d), "v"(c1));
  if constexpr (LAST) asm volatile("s_nop 1" : "+v"(o0), "+v"(o1));
}
DI v4u swap_half(u32 p0, u32 p1, u32 p2, u32 p3) {
  const auto r0 = __builtin_amdgcn_permlane32_swap(p0, p2, false, false);
  const auto r1 = __builtin_amdgcn_permlane32_swap(p1, p3, false, false);
  return v4u{r0[0], r1[0], r0[1], r1[1]};
}
DI void mfma_settle(const f32x16& a) { asm volatile("s_nop 7\n\ts_nop 4" ::"v"(a)); }
DI void store_v(const f32x16& a, u32 vo, v4i ws, u32 so) {
  st16c<0>(v4u{pk(a[0], a[1]), pk(a[2], a[3]), pk(a[4], a[5]), pk(a[6], a[7])}, vo, ws, so);
  st16c<0>(v4u{pk(a[8], a[9]), pk(a[10], a[11]), pk(a[12], a[13]), pk(a[14], a[15])}, vo, ws, so + 14336u);
}
DI void store_gelu(const f32x16& a, float gc1, u32 lo16, v4i ws, u32 so) {
  u32 p[8];
  mfma_settle(a);
  gelu4(p[0], p[1], a[0], a[1], a[2], a[3], gc1);
  gelu4<true>(p[2], p[3], a[4], a[5], a[6], a[7], gc1);
  st16c<0>(swap_half(p[0], p[1], p[2], p[3]), lo16, ws, so);
  gelu4(p[4], p[5], a[8], a[9], a[10], a[11], gc1);
  gelu4<true>(p[6], p[7], a[12], a[13], a[14], a[15], gc1);
  st16c<1024>(swap_half(p[4], p[5], p[6], p[7]), lo16, ws, so);
}
DI float max16(const f32x16& s) {
  float a, b;
  asm volatile("v_max3_f32 %0, %2, %3, %4\n\tv_max3_f32 %1, %5, %6, %7\n\tv_max3_f32 %0, %0, %8, %9\n\tv_max3_f32 %1, %1, %10, %11\n\t"
               "v_max3_f32 %0, %0, %12, %13\n\tv_max3_f32 %1, %1, %14, %15\n\tv_max3_f32 %0, %0, %16, %17\n\tv_max_f32 %0, %0, %1"
               : "=&v"(a), "=&v"(b) : "v"(s[0]), "v"(s[1]), "v"(s[2]), "v"(s[3]), "v"(s[4]), "v"(s[5]), "v"(s[6]), "v"(s[7]), "v"(s[8]),
                 "v"(s[9]), "v"(s[10]), "v"(s[11]), "v"(s[12]), "v"(s[13]), "v"(s[14]), "v"(s[15]));
  return a;
}
DI void exp4(const f32x16& s, int o, float (&e)[8], int d) {
#pragma unroll
  for (int j = 0; j < 4; ++j) e[d + j] = __builtin_amdgcn_exp2f(s[o + j]);
}
DI v4u pack8(const float (&e)[8]) { return v4u{pk(e[0], e[1]), pk(e[2], e[3]), pk(e[4], e[5]), pk(e[6], e[7])}; }
constexpr u64 upmask(int i) {
  u64 m = 0;
  for (int l = 0; l < 64; ++l) if ((i & 3) + 8 * (i >> 2) + 4 * (l >> 5) > (l & 31)) m |= 1ull << l;
  return m;
}
DI u32 vsel(u32 a, u32 b, u64 m) { u32 d; asm("v_cndmask_b32 %0, %1, %2, %3" : "=v"(d) : "v"(a), "v"(b), "s"(m)); return d; }
DI float vmax(float a, float b) { float d; asm("v_max_f32 %0, %1, %2" : "=v"(d) : "v"(a), "v"(b)); return d; }
DI float vadd(float a, float b) { float d; asm("v_add_f32 %0, %1, %2" : "=v"(d) : "v"(a), "v"(b)); return d; }
DI float vmul(float a, float b) { float d; asm("v_mul_f32 %0, %1, %2" : "=v"(d) : "v"(a), "v"(b)); return d; }

DI int fq_off(int blk) { return blk < 28 ? blk * 8 : 256 + (blk - 28) * 8; }
DI int hblock(int j, int idx) {
  if (j < 2) return 2 * idx + j;
  const int p = 8 * (j - 2) + idx;
  return p < 6 ? 16 + p : (p < 22 ? 2 * p + 10 : (p < 38 ? 2 * p - 21 : 16 + p));
}
DI void poll(v2u& d, u32 voff, v4i srd, u64 lanes) {
  asm volatile("s_mov_b64 exec, %3\n\tbuffer_load_dwordx2 %0, %1, %2, 0 offen sc1\n\ts_mov_b64 exec, -1" : "+v"(d) : "v"(voff), "s"(srd), "s"(lanes) : "memory");
}
DI u64 missing(v2u f, v2u tag, u64 lanes) {
  return lanes & ~__builtin_amdgcn_uicmpl(__builtin_bit_cast(u64, f), __builtin_bit_cast(u64, tag), 32  );
}
DI void check(v2u& f, u32 voff, v4i ws, v2u tag, u64 lanes) {
  pin(f);
  if (__builtin_expect(missing(f, tag, lanes) != 0, 0)) {
#pragma unroll 1
    for (u32 spins = 0; spins < SPIN_CAP; ++spins) {
      __builtin_amdgcn_s_sleep(PSLEEP);
      poll(f, voff, ws, lanes);
      vm_wait<0>();
      pin(f);
      if (missing(f, tag, lanes) == 0) break;
    }
  }
}
DI void dma2c(u32 lds, u32 voff, v4i srd, u32 so, u32 so2) {
  asm volatile("s_mov_b32 m0, %0\n\ts_nop 0\n\tbuffer_load_dwordx4 %1, %2, %3 offen sc1" NTL " lds\n\t"
               "buffer_load_dwordx4 %1, %2, %4 offen offset:1024 sc1" NTL " lds" ::"s"(lds), "v"(voff), "s"(srd), "s"(so), "s"(so2) : "memory");
}
constexpr int SY = 4 * 8192;
DI void lds_arrive(LAS char* p) {
  asm volatile("s_waitcnt lgkmcnt(0)\n\ts_mov_b64 exec, 1\n\tds_add_u32 %0, %1\n\ts_mov_b64 exec, -1" ::"v"((u32)(size_t)p), "v"(1u) : "memory");
}
DI void lds_arrive_nowait(LAS char* p) {
  asm volatile("s_mov_b64 exec, 1\n\tds_add_u32 %0, %1\n\ts_mov_b64 exec, -1" ::"v"((u32)(size_t)p), "v"(1u) : "memory");
}
DI void lds_wait(LAS char* p, u32 n) {
#pragma unroll 1
  for (u32 spins = 0; spins < (1u << 22); ++spins) {
    u32 v;
    asm volatile("ds_read_b32 %0, %1\n\ts_waitcnt lgkmcnt(0)" : "=v"(v) : "v"((u32)(size_t)p) : "memory");
    if (__builtin_amdgcn_readfirstlane(v) >= n) break;
    if (LSLEEP) __builtin_amdgcn_s_sleep(LSLEEP);
  }
}

struct Args { const u16 *x, *wqkv, *wo, *w1, *b1, *w2; char* wsp; };
struct Ctx {
  v4i ws, sqkv, sw1, sw2;
  int m, b, xcd, r, rt, cg, tb1;
  bool heavy;
  u32 goff_q, goff_1, goff_2, trl, lo16, fo, vst, bias0, biasx, biasy, tch;
  u32 xb[4];
  float gc1;
  v2u tag;
  u64 epoch;
};
#define SB __builtin_amdgcn_sched_barrier(0);

DI void setup(Ctx& c, const int GRP) {
  const int lane = threadIdx.x & 63, r32 = lane & 31, hh = lane >> 5, r8 = lane >> 3, c8 = lane & 7;
  const u32 gsw = (u32)((c8 ^ (((r8 >> 1) & 1) << 2)) * 16);
  c.goff_q = (u32)(r8 * 2688) + gsw; c.goff_1 = (u32)(r8 * 3584) + gsw; c.goff_2 = (u32)(r8 * 896) + gsw;
  {
    const int tq = (lane & 15) >> 2, tp = lane & 3, tg = lane >> 4;
    c.trl = (u32)(((tg >> 1) * 8 + tq) * 128 + (((2 * (tg & 1) + (tp >> 1)) ^ (((tq >> 1) & 1) << 2)) << 4) + (tp & 1) * 8);
  }
  c.lo16 = (u32)(lane * 16);
  c.vst = (u32)(hh * 7168 + r32 * 16);
  c.gc1 = -0.1029432397f;
#pragma unroll
  for (int j = 0; j < 4; ++j) c.xb[j] = (u32)(65536 - GRP * 16384 + c.m * 4096 + (2 * j + hh) * 512 + ((r32 ^ (2 * j + hh)) << 4));
  asm volatile("" : "+v"(c.goff_q), "+v"(c.goff_1), "+v"(c.goff_2), "+v"(c.trl), "+v"(c.lo16), "+v"(c.vst));
  asm volatile("" : "+v"(c.xb[0]), "+v"(c.xb[1]), "+v"(c.xb[2]), "+v"(c.xb[3]), "+v"(c.gc1));
}
template <int GRP>
DI void setup_flags(Ctx& c) {
  int lane = threadIdx.x & 63;
  asm volatile("" : "+v"(lane));
  const int m = c.m, xcd = c.xcd, r = c.r;
  if constexpr (GRP == 0) {
    const int ia = 15 - (r >> 1), c0 = (28 * (2 * xcd + (r & 1))) >> 3;
    const int n = (lane & 7) >> 1;
    int kbl = 4 * ia - 8 + 3 * m + n;
    kbl = (kbl < 0 || n == 3) ? 4 * ia + m : kbl;
    const int fbl = (8 * c0 + (lane & 1) * 31) >> 5;
    c.fo = fl((u32)(WS_F + kbl * 1024 + fbl * 8 + (lane >= 8 ? 256 : (n < 3 ? 14 * 8 : 0))));
  } else {
    const int tt = 4 * xcd + (r & 3);
    c.fo = fl((u32)(WS_F + (2 * tt + ((lane >> 1) & 1)) * 1024 + 384 + (8 * (lane >> 2) + 2 * m + (lane & 1)) * 8));
  }
  asm volatile("" : "+v"(c.fo));
}

struct LnConst { v4u asum; u32 dm0, dm1, dsto[2][2], xo; f32x4 dsel; };
DI void ln_consts(LnConst& k, const int w, const int rt) {
  const int lane = threadIdx.x & 63, r8 = lane >> 3, c8 = lane & 7, l3 = lane & 3;
  const u32 sel = ((lane & 15) == 4 * (lane >> 4)) ? 0x3f803f80u : 0u;
  k.asum = v4u{sel, sel, sel, sel};
  k.dm0 = l3 == 0 ? 0xffffu : (l3 == 1 ? 0xffff0000u : 0u); k.dm1 = l3 == 2 ? 0xffffu : (l3 == 3 ? 0xffff0000u : 0u);
  k.dsel = f32x4{l3 == 0 ? 1.f / 448.f : 0.f, l3 == 1 ? 1.f / 448.f : 0.f, l3 == 2 ? 1.f / 448.f : 0.f, l3 == 3 ? 1.f / 448.f : 0.f};
#pragma unroll
  for (int g = 0; g < 2; ++g) {
    k.dsto[g][0] = (u32)(49152 + (w >> 1) * 4096 + c8 * 512 + (((16 * (w & 1) + 8 * g + r8) ^ c8) << 4));
    k.dsto[g][1] = k.dsto[g][0] + 65536u;
  }
  asm volatile("" : "+v"(k.asum), "+v"(k.dm0), "+v"(k.dm1), "+v"(k.dsel), "+v"(k.dsto[0][0]), "+v"(k.dsto[0][1]), "+v"(k.dsto[1][0]), "+v"(k.dsto[1][1]));
}

DI void layer_norm(const Args& a, Ctx& c, LnConst& k, LAS char* L, const int GRP, const v4i sx, u32& vt STAMP_ARG) {
  const int w = 4 * GRP + c.m;
  v4u xr[14];
  if (PRE == 0) { if (GRP == 1) { ln_consts(k, w, c.rt); setup(c, GRP); } }
  if (PRE == 1) { if (GRP == 1) setup(c, GRP); }
  {
    const u32 xo = k.xo;
#define LDX(g, i) ld16<(i) * 128>(xr[(g) * 7 + (i)], xo, sx, (u32)((g) * 7168));
    LDX(0, 0) LDX(0, 1) LDX(0, 2) LDX(0, 3) LDX(0, 4) LDX(0, 5) LDX(0, 6) LDX(1, 0) LDX(1, 1) LDX(1, 2) LDX(1, 3) LDX(1, 4) LDX(1, 5) LDX(1, 6)
#undef LDX
  }
  SB
  asm volatile("s_load_dwordx2 %0, %1, 0x0" : "=s"(c.epoch) : "s"(a.wsp));
  if (GRP == 0) { if (PRE == 0) ln_consts(k, w, c.rt); if (PRE < 2) setup(c, GRP); } else __builtin_amdgcn_s_setprio(LNPRIO);
  const v4u asum = k.asum;
  const u32 dm0 = k.dm0, dm1 = k.dm1;
  const f32x4 dsel = k.dsel;
  const u32 dsto[2][2] = {{k.dsto[0][0], k.dsto[0][1]}, {k.dsto[1][0], k.dsto[1][1]}};
#define LNSTAT(g, i) { vm_wait<((g) == 0 ? 13 : 6) - (i)>(); pin(xr[(g) * 7 + (i)]); const v4u d = xr[(g) * 7 + (i)]; \
      const s16x4 lo = __builtin_bit_cast(s16x4, v2u{d[0], d[1]}), hi = __builtin_bit_cast(s16x4, v2u{d[2], d[3]}); \
      qa[g] = __builtin_amdgcn_mfma_f32_4x4x4bf16_1k(lo, lo, qa[g], 0, 0, 0); \
      s1[g] = __builtin_amdgcn_mfma_f32_16x16x32_bf16(__builtin_bit_cast(bf16x8, asum), __builtin_bit_cast(bf16x8, d), s1[g], 0, 0, 0); \
      qa[g] = __builtin_amdgcn_mfma_f32_4x4x4bf16_1k(hi, hi, qa[g], 0, 0, 0); }
#define LNMUL(g, i) { const v4u d = xr[(g) * 7 + (i)]; \
      y0[(i) & 1] = __builtin_amdgcn_mfma_f32_4x4x4bf16_1k(an, __builtin_bit_cast(s16x4, v2u{d[0], d[1]}), cc, 0, 0, 0); \
      y1[(i) & 1] = __builtin_amdgcn_mfma_f32_4x4x4bf16_1k(an, __builtin_bit_cast(s16x4, v2u{d[2], d[3]}), cc, 0, 0, 0); }
#define LNOUT(g, i) { if ((i) < 6) LNMUL(g, ((i) < 6 ? (i) + 1 : 0)) \
      if ((g) == 0) LNSTAT(1, i) \
      if ((g) == 1) { if ((i) == 0) piece_m0<0>(g0, l0, s0); else if ((i) < 4) piece<((i) & 3)>(g0); \
        else if ((i) == 4) piece_m0<0>(g0, l0 + 4096, s0 + 32 * 2688); else if ((i) == 5) piece<1>(g0); } \
      SB \
      *(LAS v4u*)(L + ((i) == 6 ? dsto[g][0] : ((i) < 3 ? dsto[g][0] + 16384 * ((i) + 1) : dsto[g][1] + 16384 * ((i) - 3)))) = \
          v4u{pk(y0[(i) & 1][0], y0[(i) & 1][1]), pk(y0[(i) & 1][2], y0[(i) & 1][3]), pk(y1[(i) & 1][0], y1[(i) & 1][1]), pk(y1[(i) & 1][2], y1[(i) & 1][3])}; \
      SB }
#define LNGROUP(g) { \
    STAMP(1 + (g)); \
    float su = s1[g][0] * (1.f / 448.f), sq = (qa[g][0] * dsel[0] + qa[g][1] * dsel[1]) + (qa[g][2] * dsel[2] + qa[g][3] * dsel[3]); \
    su = dpp_add<0xB1>(su); sq = dpp_add<0xB1>(sq); su = dpp_add<0x4E>(su); sq = dpp_add<0x4E>(sq); \
    su = dpp_add<0x141>(su); sq = dpp_add<0x141>(sq); \
    const float rstd = __builtin_amdgcn_rsqf(__builtin_fmaxf(__builtin_fmaf(-su, su, sq), 0.f) + 1e-5f); \
    const u32 rr = pk(rstd, rstd); \
    const float nc = -su * u2f(rr << 16); \
    const f32x4 cc = {nc, nc, nc, nc}; \
    const s16x4 an = __builtin_bit_cast(s16x4, v2u{rr & dm0, rr & dm1}); \
    f32x4 y0[2], y1[2]; \
    LNMUL(g, 0) \
    LNOUT(g, 0) LNOUT(g, 1) LNOUT(g, 2) LNOUT(g, 3) LNOUT(g, 4) LNOUT(g, 5) LNOUT(g, 6) }
  Stream g0;
  g0.srd = c.sqkv; g0.voff = c.goff_q; g0.d8 = 8 * 2688 - 1024;
  u32 l0 = (u32)(size_t)L + (u32)(w * 6144), s0 = (u32)(w * 48 * 2688 + c.cg * 128);
  keep_s(l0); keep_s(s0);
  f32x4 s1[2], qa[2];
  s1[0] = s1[1] = qa[0] = qa[1] = f32x4{0.f, 0.f, 0.f, 0.f};
  LNSTAT(0, 0) SB LNSTAT(0, 1) SB LNSTAT(0, 2) SB LNSTAT(0, 3) SB LNSTAT(0, 4) SB LNSTAT(0, 5) SB LNSTAT(0, 6) SB
  pin(vt);
  asm volatile("s_waitcnt lgkmcnt(0)" : "+s"(c.epoch));
  LNGROUP(0)
  LNGROUP(1)
#undef LNGROUP
#undef LNOUT
#undef LNMUL
#undef LNSTAT
}
template <int GRP>
DI void load_bias(const Args& a, Ctx& c) {
  const v4i sb1 = make_srd(a.b1, F_ * 2);
  const u32 bo = (u32)((threadIdx.x & 31) * 2);
  ld2(c.bias0, bo, sb1, (u32)(((c.cg < 5 ? 0 : c.cg - 5) * 64 + GRP * 32) * 2));
  ld2(c.biasx, bo, sb1, (u32)(((11 + c.cg) * 64 + GRP * 32) * 2));
  if constexpr (GRP == 1) ld2(c.biasy, bo, sb1, (u32)((27 * 64 + (c.xcd & 1) * 32) * 2));
}
DI void first_barrier(Ctx& c STAMP_ARG) {
  STAMP(3);
  wg_barrier();
  STAMP(4);
  const u64 t = c.epoch + 1;
  c.tag = v2u{(u32)t, (u32)(t >> 32)};
  asm volatile("" : "+v"(c.tag));
}

template <int GRP, class H>
DI u32 slot0(const Ctx& c, v4u (&xa)[28], LAS char* p1, LAS char* p2, LAS char* p3, H&& hook STAMP_ARG) {
  f32x16 acc;
  if (c.cg >= 14) {
    acc = gemm1_block<true, true>(xa, p1, p2, p3, acc, hook);
    STAMP(9);
    store_v(acc, c.vst, c.ws, (u32)(WS_VT + c.tb1 * VT_TB + (2 * c.cg + GRP - 28) * 512));
  } else {
    acc = gemm1_block<false, true>(xa, p1, p2, p3, acc, hook);
    STAMP(9);
    float qs = c.cg < 7 ? 0.18898223650461362f * 1.4426950408889634f : 1.f;
    asm volatile("" : "+v"(qs));
    mfma_settle(acc);
    asm volatile("" : "+v"(qs));
#pragma unroll
    for (int i = 0; i < 16; ++i) acc[i] = vmul(acc[i], qs);
    v4u o0, o1;
    pack_tile(acc, o0, o1);
    const u32 so = (u32)(WS_QK + c.tb1 * QK_TB + (2 * c.cg + GRP) * 2048);
    st16c<0>(o0, c.lo16, c.ws, so);
    st16c<1024>(o1, c.lo16, c.ws, so);
  }
  return fl((u32)(WS_F + c.tb1 * 1024 + fq_off(2 * c.cg + GRP)));
}
template <int GRP, class H>
DI u32 slot1(const Ctx& c, v4u (&xa)[28], LAS char* p1, LAS char* p2, LAS char* p3, f32x16 acc, H&& hook STAMP_ARG) {
  if (c.cg < 5) {
    acc = gemm1_block<true, false>(xa, p1, p2, p3, acc, hook);
    STAMP(13);
    store_v(acc, c.vst, c.ws, (u32)(WS_VT + c.tb1 * VT_TB + (2 * c.cg + GRP + 4) * 512));
  } else {
    acc = gemm1_block<false, false>(xa, p1, p2, p3, acc, hook);
    STAMP(13);
    store_gelu(acc, c.gc1, c.lo16, c.ws, (u32)(WS_HH + c.tb1 * HH_TB + (2 * c.cg - 10 + GRP) * 2048));
  }
  return fl((u32)(WS_F + c.tb1 * 1024 + (c.cg < 5 ? 256 + (4 + 2 * c.cg + GRP) * 8 : 384 + (c.cg < 13 ? 8 * GRP + c.cg - 5 : 2 * c.cg - 10 + GRP) * 8)));
}

DI void group_a(const Args& a, Ctx c, LAS char* L STAMP_ARG) {
  const u32 lds0 = (u32)(size_t)L;
  const int lane = threadIdx.x & 63, r32 = lane & 31, hh = lane >> 5;
  const int m = c.m, cg = c.cg, tb1 = c.tb1;
  const v4i ws = c.ws;
  const bool s1v = cg < 5;
  const int hd = 2 * c.xcd + (c.r & 1), ia = 15 - (c.r >> 1), c0 = (28 * hd) >> 3;
  const int tbq = 4 * ia + m;
  const bool odd = hd & 1;
  f32x16 acc;
  v2u fpoll = {0u, 0u};
  u32 pend;
  int kl[3];
#pragma unroll
  for (int n = 0; n < 3; ++n) { const int k = 4 * ia - 8 + 3 * m + n; kl[n] = k < 0 ? tbq : k; }
  v4u qf[2];
  load_bias<0>(a, c);
  {
    u64 p;
    if (m < 2) {
      const int half = lane >= 28, row = c.rt * 28 + lane - 28 * half;
      const bool q = m == 0 && half == 0 && s1v;
      const int strip = m == 0 ? (half == 0 ? (s1v ? 16 + cg : cg - 5) : 11 + cg) : 27;
      p = (q ? (u64)a.wqkv : (u64)a.w1) + (u64)(row * (q ? 2688 : 3584) + strip * 128);
    } else {
      const int i = lane + (m - 2) * 31;
      p = i < 49 ? (u64)a.w2 + (u64)((c.b * 49 + i) * 128) : (u64)a.wo + (u64)(min(c.b * 13 + i - 49, 3135) * 128);
    }
    const u64 lanes = ~0ull >> (!(TOUCH & 2) ? 63 : (m >= 2 ? 33 : (m == 0 ? 8 : (c.heavy ? 36 : 63))));
    asm volatile("s_mov_b64 exec, %2\n\tglobal_load_dword %0, %1, off\n\ts_mov_b64 exec, -1" : "=v"(c.tch) : "v"(p), "s"(lanes) : "memory");
  }
  setup_flags<0>(c);
  first_barrier(c STAMP_PASS);
  const u32 lo16 = c.lo16;
  const v2u tag = c.tag;
  {
    v4u xa[28];
    u32 xh[4];
#pragma unroll
    for (int j = 0; j < 4; ++j) xh[j] = c.xb[j] + 49152u;
    auto rdx = [&](int s) { xa[s] = *(LAS v4u*)(L + (s >= 12 && s < 24 ? xh[s & 3] + 16384 * ((s >> 2) - 3) : c.xb[s & 3] + ((s >> 2) == 6 ? -16384 : 16384 * (s >> 2)))); };
#pragma unroll
    for (int s = 0; s < 28; ++s) if (!EARLYB1 || s < 12 || s >= 24) rdx(s);
    asm volatile("" : "+v"(xh[0]), "+v"(xh[1]), "+v"(xh[2]), "+v"(xh[3]));
    u32 q1 = c.trl, q3 = c.trl + 49152u;
    Stream g;
    {
      g.srd = c.sqkv; g.voff = c.goff_q; g.d8 = 8 * 2688 - 1024;
      u32 tl = lds0 + (u32)(49152 + m * 2048), ts = (u32)((384 + m * 16) * 2688 + cg * 128);
      keep_s(tl); keep_s(ts);
      asm volatile("" : "+v"(q3));
      vm_wait<1>();
      pin(c.bias0); pin(c.biasx);
      STAMP(6);
      wg_barrier();
      piece_m0<0>(g, tl, ts); piece<1>(g);
    }
    STAMP(8);
    pend = slot0<0>(c, xa, L + q1, L + q1 + 8192, L + q3, [&](auto S) {
      if constexpr (EARLYB1 && decltype(S)::value < 12) rdx(12 + decltype(S)::value);
      if constexpr (decltype(S)::value == 24 - PDEPTH) { vm_wait<0>(); raw_barrier(); }
    } STAMP_PASS);
    pin(c.tch);
    g.srd = c.sw1; g.voff = c.goff_1; g.d8 = 8 * 3584 - 1024;
    pieces128<0, 8>(g, lds0 + (u32)(114688 + 4096 * m), (u32)(32 * m * 3584 + (11 + cg) * 128), 128u * 3584u);
    {
      u32 p1 = q1 + 57344u, p3 = q3 + 57344u;
      acc = bias_init(s1v ? 0u : c.bias0);
      asm volatile("" : "+v"(p1), "+v"(p3), "+v"(acc));
      keep_s(pend);
      STAMP(10);
      wg_barrier();
      STAMP(12);
      pend = slot1<0>(c, xa, L + p1, L + p1 + 8192, L + p3, acc, [&](auto S) {
        if constexpr (decltype(S)::value == 2) { vm_wait<8>(); post_flag(pend, tag, ws); }
      } STAMP_PASS);
    }
    u32 p1 = q1 + 114688u;
    acc = bias_init(c.biasx);
    asm volatile("" : "+v"(p1), "+v"(acc));
    if (s1v) { vm_wait<0>(); post_flag(pend, tag, ws); pend = (u32)WS_END; }
    u32 ko[3], kd = lds0 + (u32)(57344 + m * 6144), qo = (u32)(WS_QK + tbq * QK_TB + c0 * 512);
#pragma unroll
    for (int n = 0; n < 3; ++n) { ko[n] = (u32)(WS_QK + (56 + c0) * 512 + kl[n] * QK_TB); keep_s(ko[n]); }
    keep_s(pend); keep_s(kd); keep_s(qo);
    vm_wait<2>();
    STAMP(14);
    wg_barrier();
    STAMP(16);
    acc = gemm1_block<false, false>(xa, L + p1, L + p1 + 8192, L + q3, acc, [&](auto S) {
      constexpr int s = decltype(S)::value;
      if constexpr (s == 3) poll(fpoll, c.fo, ws, 0xffull);
      if constexpr (s == 20) { vm_wait<0>(); post_flag(pend, tag, ws); check(fpoll, c.fo, ws, tag, 0xffull); }
      if constexpr (s == 21 || s == 23 || s == 25) dma2c(kd + (s - 21) / 2 * 2048, lo16, ws, ko[(s - 21) / 2], ko[(s - 21) / 2]);
      if constexpr (s == 27) { ld16c<0>(qf[0], lo16, ws, qo); ld16c<1024>(qf[1], lo16, ws, qo); }
    });
    lds_arrive_nowait(L + SY);
    STAMP(17);
  }
  {
    u32 p[8];
    mfma_settle(acc);
    gelu4(p[0], p[1], acc[0], acc[1], acc[2], acc[3], c.gc1);
    gelu4<true>(p[2], p[3], acc[4], acc[5], acc[6], acc[7], c.gc1);
    const u32 so = (u32)(WS_HH + tb1 * HH_TB + (22 + 2 * cg) * 2048);
    st16c<0>(swap_half(p[0], p[1], p[2], p[3]), lo16, ws, so);
    gelu4(p[4], p[5], acc[8], acc[9], acc[10], acc[11], c.gc1);
    vm_wait<1>();
    pin(qf[0]); pin(qf[1]);
    lds_arrive_nowait(L + SY + 4);
    gelu4<true>(p[6], p[7], acc[12], acc[13], acc[14], acc[15], c.gc1);
    st16c<1024>(swap_half(p[4], p[5], p[6], p[7]), lo16, ws, so);
    poll(fpoll, c.fo, ws, 0x3f00ull);
  }
  pend = fl((u32)(WS_F + tb1 * 1024 + 384 + (22 + cg) * 8));
  __builtin_amdgcn_s_setprio(APRIO);
  STAMP(32);
  {
    const u32 k0 = (odd && hh == 0) ? 0u : ~0u, k1 = (!odd && hh == 1) ? 0u : ~0u;
    qf[0][0] &= k0; qf[0][1] &= k0; qf[1][2] &= k1; qf[1][3] &= k1;
  }
  v4u pm0, pm1;
  u64 um[16];
  f32x16 s0, s8;
#pragma unroll
  for (int i = 0; i < 16; ++i) { um[i] = upmask(i); asm volatile("" : "+s"(um[i])); s0[i] = tbq < 8 ? -__builtin_inff() : 0.f; }
  asm volatile("" : "+v"(s0));
  {
    u32 z = 0u, hi = 0xffff0000u, all = ~0u;
    asm volatile("" : "+v"(z), "+v"(hi), "+v"(all));
#pragma unroll
    for (int j = 0; j < 4; ++j) {
      pm0[j] = vsel(vsel(z, hi, um[2 * j + 1]), all, um[2 * j]);
      pm1[j] = vsel(vsel(z, hi, um[9 + 2 * j]), all, um[8 + 2 * j]);
    }
    pin(pm0); pin(pm1);
  }
  lds_wait(L + SY + 4, 4);
  STAMP(33);
  CSTAMP(53);
  f32x16 zero;
#pragma unroll
  for (int i = 0; i < 16; ++i) zero[i] = 0.f;
  LAS char* kp = L + 57344 + m * 2048 + lo16;
  v4u kf[9][2];
  f32x16 sm, sc[8];
  float mx;
#define RDK(n) { kf[n][0] = *(LAS v4u*)(kp + (n) * 2048); kf[n][1] = *(LAS v4u*)(kp + (n) * 2048 + 1024); }
#define QK(S, n, C) { S = mfma(kf[n][0], qf[0], C); S = mfma(kf[n][1], qf[1], S); SB }
#define MX(n) { mx = vmax(mx, vadd(max16(sc[n]), tbq - 8 + (n) < 0 ? -__builtin_inff() : 0.f)); SB }
  RDK(0) RDK(8) RDK(1) RDK(2) RDK(3) RDK(4) RDK(5) SB
  QK(s0, 0, s0) RDK(6) QK(s8, 8, zero) RDK(7) QK(sc[1], 1, zero) QK(sc[2], 2, zero)
  vm_wait<1>();
  __builtin_amdgcn_s_setprio(3);
  post_flag(pend, tag, ws);
  __builtin_amdgcn_s_setprio(APRIO);
  mfma_settle(s8);
  asm volatile("" : "+v"(s0), "+v"(s8) : "v"(sc[1]), "v"(sc[2]));
#pragma unroll
  for (int i = 0; i < 8; ++i) sm[i] = u2f(vsel(f2u(s8[i]), f2u(s0[i]), um[i]));
  SB QK(sc[3], 3, zero)
#pragma unroll
  for (int i = 8; i < 16; ++i) sm[i] = u2f(vsel(f2u(s8[i]), f2u(s0[i]), um[i]));
  SB QK(sc[4], 4, zero)
  asm volatile("" ::"v"(sc[3]), "v"(sc[4]), "v"(sm));
  vm_wait<1>();
  check(fpoll, c.fo, ws, tag, 0x3f00ull);
  lds_wait(L + SY, 8);
  __builtin_amdgcn_s_setprio(3);
  {
    const u32 vo = c.vst + (u32)(c0 * 128);
#pragma unroll
    for (int n = 0; n < 3; ++n) {
      const u32 so = (u32)(WS_VT + kl[n] * VT_TB);
      dma2c(lds0 + (u32)(114688 + (3 * m + n) * 2048), vo, ws, so, so + 14336u - 1024u);
    }
  }
  __builtin_amdgcn_s_setprio(APRIO);
  STAMP(34);
  mx = max16(sm);
  MX(1) QK(sc[5], 5, zero) MX(2) QK(sc[6], 6, zero) MX(3) QK(sc[7], 7, zero) MX(4) MX(5)
  MX(6) MX(7)
#undef MX
  {
    const auto r = __builtin_amdgcn_permlane32_swap(f2u(mx), f2u(mx), false, false);
    mx = vmax(u2f(r[0]), u2f(r[1]));
  }
  STAMP(35);
  CSTAMP(54);
  f32x16 cm;
#pragma unroll
  for (int i = 0; i < 16; ++i) cm[i] = -mx;
  asm volatile("" : "+v"(cm));
  QK(sc[1], 1, cm) QK(sc[2], 2, cm)
  float e[8];
  v4u p0a, p0b, p8a, p8b;
  {
#pragma unroll
    for (int i = 0; i < 16; ++i) sm[i] = vadd(sm[i], cm[0]);
    exp4(sm, 0, e, 0); exp4(sm, 4, e, 4);
    const v4u pa = pack8(e);
    exp4(sm, 8, e, 0); exp4(sm, 12, e, 4);
    const v4u pb = pack8(e);
    p0a = pa & pm0; p8a = pa & ~pm0; p0b = pb & pm1; p8b = pb & ~pm1;
    pin(p0a); pin(p0b); pin(p8a); pin(p8b);
  }
  vm_wait<0>();
  if (lane < 12) *(LAS v4u*)(L + 114688 + m * 6144 + lane * 512 + (odd ? 0 : 28 * 16)) = v4u{0x3f803f80u, 0x3f803f80u, 0x3f803f80u, 0x3f803f80u};
  lds_arrive(L + SY + 8);
  v4u pa, pb;
#define LIVE(n) (tbq - 8 + (n) < 0 ? 0u : ~0u)
  asm volatile("" ::"v"(sc[1]), "v"(sc[2]));
  exp4(sc[1], 0, e, 0); exp4(sc[1], 4, e, 4); pa = pack8(e);
  exp4(sc[1], 8, e, 0); exp4(sc[1], 12, e, 4); pb = pack8(e);
  pin(pa); pin(pb);
  lds_wait(L + SY + 8, 4);
  STAMP(36);
  CSTAMP(55);
  LAS char* vp = kp + 57344;
  v4u vf[9][2];
#define RDV(n) { vf[n][0] = *(LAS v4u*)(vp + (n) * 2048); vf[n][1] = *(LAS v4u*)(vp + (n) * 2048 + 1024); }
  f32x16 o;
  RDV(0) RDV(8) RDV(1) RDV(2) SB
#define BLK(n, M0, M1, M2, M3) { v4u na, nb; \
    exp4(sc[n], 0, e, 0); SB M0 SB exp4(sc[n], 4, e, 4); SB M1 SB na = pack8(e); \
    exp4(sc[n], 8, e, 0); SB M2 SB exp4(sc[n], 12, e, 4); SB M3 SB nb = pack8(e); \
    pin(na); pin(nb); pa = na; pb = nb; SB }
#define QA(n) sc[n] = mfma(kf[n][0], qf[0], cm);
#define QB(n) sc[n] = mfma(kf[n][1], qf[1], sc[n]);
#define PVA(n) o = mfma(vf[n][0], pa & lv[n], o);
#define PVB(n) o = mfma(vf[n][1], pb & lv[n], o); if ((n) + 2 < 8) RDV(((n) + 2 < 8 ? (n) + 2 : 0))
#define PASS2 \
    BLK(2, QA(3) o = mfma(vf[0][0], p0a, zero);, QB(3) o = mfma(vf[0][1], p0b, o);, PVA(1) o = mfma(vf[8][0], p8a, o);, PVB(1) o = mfma(vf[8][1], p8b, o);) \
    BLK(3, QA(4), QB(4), PVA(2), PVB(2)) BLK(4, QA(5), QB(5), PVA(3), PVB(3)) BLK(5, QA(6), QB(6), PVA(4), PVB(4)) \
    BLK(6, QA(7), QB(7), PVA(5), PVB(5)) BLK(7, , , PVA(6), PVB(6)) PVA(7) PVB(7)
  if (tbq >= 8) {
    constexpr u32 lv[8] = {~0u, ~0u, ~0u, ~0u, ~0u, ~0u, ~0u, ~0u};
    PASS2
  } else {
    u32 lv[8];
#pragma unroll
    for (int n = 0; n < 8; ++n) { lv[n] = LIVE(n); keep_s(lv[n]); }
    PASS2
  }
#undef PASS2
#undef PVB
#undef PVA
#undef QB
#undef QA
#undef BLK
#undef RDV
#undef LIVE
#undef QK
#undef RDK
  __builtin_amdgcn_s_setprio(3);
  STAMP(37);
  CSTAMP(56);
  const u32 lsel = f2u(odd ? o[0] : o[12]);
  const auto lr = __builtin_amdgcn_permlane32_swap(lsel, lsel, false, false);
  float rl = __builtin_amdgcn_rcpf(u2f(odd ? lr[0] : lr[1]));
  asm volatile("s_nop 0" : "+v"(rl));
#pragma unroll
  for (int i = 0; i < 16; ++i) o[i] = vmul(o[i], rl);
  v4u o0, o1;
  pack_tile(o, o0, o1);
  const u32 so = (u32)(WS_HH + tbq * HH_TB + (224 + c0) * 512);
  if (odd) {
    asm volatile("buffer_store_dwordx4 %0, %3, %4, %5 offen offset:1024 sc1\n\t"
                 "s_mov_b32 exec_lo, 0\n\tbuffer_store_dwordx4 %1, %3, %4, %5 offen sc1\n\t"
                 "s_mov_b32 exec_lo, -1\n\ts_mov_b32 exec_hi, 0\n\tbuffer_store_dwordx2 %2, %3, %4, %5 offen offset:8 sc1\n\ts_mov_b64 exec, -1"
                 ::"v"(o1), "v"(o0), "v"(v2u{o0[2], o0[3]}), "v"(lo16), "s"(ws), "s"(so) : "memory");
  } else {
    asm volatile("buffer_store_dwordx4 %0, %3, %4, %5 offen sc1\n\t"
                 "s_mov_b32 exec_hi, 0\n\tbuffer_store_dwordx4 %1, %3, %4, %5 offen offset:1024 sc1\n\t"
                 "s_mov_b32 exec_hi, -1\n\ts_mov_b32 exec_lo, 0\n\tbuffer_store_dwordx2 %2, %3, %4, %5 offen offset:1024 sc1\n\ts_mov_b64 exec, -1"
                 ::"v"(o0), "v"(o1), "v"(v2u{o1[0], o1[1]}), "v"(lo16), "s"(ws), "s"(so) : "memory");
  }
  STAMP(38);
  CSTAMP(57);
  vm_wait<0>();
  post_flag(fl((u32)(WS_FO + (tbq * 16 + hd) * 8)), tag, ws);
  STAMP(39);
}

DI void group_b(const Args& a, Ctx c, LAS char* L STAMP_ARG) {
  const u32 lds0 = (u32)(size_t)L;
  const int lane = threadIdx.x & 63, r32 = lane & 31, hh = lane >> 5;
  const int m = c.m, kq = m, cg = c.cg, tb1 = c.tb1;
  const int tt = 4 * c.xcd + (c.r & 3), ct = c.r >> 2;
  const v4i ws = c.ws;
  const bool heavy = c.heavy;
  first_barrier(c STAMP_PASS);
  __builtin_amdgcn_s_setprio(0);
  load_bias<1>(a, c);
  const u32 lo16 = c.lo16;
  const v2u tag = c.tag;
  const u32 cell[3] = {(u32)(kq * 8192), (u32)((kq == 0 ? 5 : (kq == 3 ? 13 : 9 + kq)) * 8192), (u32)((kq == 0 ? 12 : (kq == 1 ? 6 : 15 + kq)) * 8192)};
  Stream g;
  v2u fpoll = {0u, 0u};
  f32x16 acc;
  {
    v4u xa[28];
#define RDX(s) xa[s] = *(LAS v4u*)(L + c.xb[(s) & 3] + 16384 * ((s) >> 2))
#pragma unroll
    for (int s = 0; s < 4; ++s) RDX(s);
    u32 q2 = c.trl ^ 64u, q1 = q2 + 49152u;
    const bool s1v = cg < 5;
    const u32 st1 = s1v ? 2688u : 3584u, co1 = (u32)((s1v ? 16 + cg : cg - 5) * 128);
    g.srd = s1v ? c.sqkv : c.sw1; g.voff = s1v ? c.goff_q : c.goff_1; g.d8 = 8 * st1 - 1024;
    u32 hl = lds0 + (u32)(57344 + m * 2048), hs = (u32)(m * 16) * st1 + co1;
    u32 bl = lds0 + (u32)(65536 + 4096 * m), bs = (u32)(64 + 32 * m) * st1 + co1, s128 = 128 * st1;
    keep_s(hl); keep_s(hs); keep_s(bl); keep_s(bs); keep_s(s128); keep_s(g.d8);
    asm volatile("" : "+v"(q1), "+v"(q2), "+v"(g.voff));
    setup_flags<1>(c);
    vm_wait<3>();
    wg_barrier();
#pragma unroll
    for (int s = 4; s < 16; ++s) RDX(s);
    piece_m0<0>(g, hl, hs); piece<1>(g);
    asm volatile("s_waitcnt lgkmcnt(0)" ::: "memory");
#pragma unroll
    for (int s = 16; s < 28; ++s) RDX(s);
#undef RDX
    pieces128<0, 6>(g, bl, bs, s128);
    STAMP(6);
    wg_barrier();
    pieces128<6, 12>(g, bl, bs, s128);
    STAMP(8);
    u32 pend = slot0<1>(c, xa, L + q1, L + q2, L + q2 + 40960, [&](auto S) {
      if constexpr (decltype(S)::value == 24) { vm_wait<0>(); raw_barrier(); __builtin_amdgcn_s_setprio(2); }
    } STAMP_PASS);
    pin(c.bias0); pin(c.biasx); pin(c.biasy);
    g.srd = c.sw1; g.voff = c.goff_1; g.d8 = 8 * 3584 - 1024;
    pieces128<8, 12>(g, lds0 + (u32)(114688 + 4096 * m), (u32)(32 * m * 3584 + (11 + cg) * 128), 128u * 3584u);
    piece_m0<0>(g, lds0 + (u32)(49152 + m * 2048), (u32)((384 + m * 16) * 3584 + (11 + cg) * 128)); piece<1>(g);
    if (m == 0) *(LAS v4u*)(L + SY) = v4u{0u, 0u, 0u, 0u};
    const u32 st2 = heavy ? 3584u : 896u, co2 = (u32)((heavy ? 27 : ct) * 128);
    g.srd = heavy ? c.sw1 : c.sw2; g.voff = heavy ? c.goff_1 : c.goff_2; g.d8 = 8 * st2 - 1024;
    u32 wd = (heavy ? 32u : 64u) * st2;
    auto ring = [&](auto S, u32 wl, u32 w0) {
      constexpr int s = decltype(S)::value;
      if constexpr (s == 2) piece_m0<0>(g, wl, w0);
      if constexpr (s == 10) piece_m0<0>(g, wl + 4096, w0 + wd);
      if constexpr ((s & 1) == 0 && s > 2 && s < 18 && s != 10) piece<(((s - 2) & 7) / 2)>(g);
    };
    {
      u32 p1 = q1 + 57344u, p2 = q2 + 57344u;
      u32 wl = lds0 + cell[0], w0 = (u32)(heavy ? 64 * m : 128 * m) * st2 + co2;
      acc = bias_init(s1v ? 0u : c.bias0);
      asm volatile("" : "+v"(p1), "+v"(p2), "+v"(g.voff), "+v"(acc));
      keep_s(pend); keep_s(wl); keep_s(w0); keep_s(wd); keep_s(g.d8);
      if (!BPOST) { vm_wait<6>(); post_flag(pend, tag, ws); }
      STAMP(12);
      pend = slot1<1>(c, xa, L + p1, L + p2, L + p2 + 40960, acc, [&](auto S) {
        if constexpr (BPOST && decltype(S)::value == 1) { vm_wait<6>(); post_flag(pend, tag, ws); }
        ring(S, wl, w0);
        if constexpr (decltype(S)::value == 24) { vm_wait<9>(); raw_barrier(); __builtin_amdgcn_s_setprio(2); }
      } STAMP_PASS);
    }
    {
      u32 p2 = q2 + 114688u;
      u32 wl = lds0 + cell[1], w0 = (u32)(heavy ? (m == 0 ? 384 : (m == 3 ? 448 : 192 + 64 * m)) : 128 * m + 32) * st2 + co2;
      acc = bias_init(c.biasx);
      asm volatile("" : "+v"(p2), "+v"(acc));
      if (s1v) { vm_wait<0>(); post_flag(pend, tag, ws); pend = (u32)WS_END; }
      keep_s(pend); keep_s(wl); keep_s(w0);
      STAMP(16);
      acc = gemm1_block<false, false>(xa, L + q1, L + p2, L + p2 + 40960, acc, [&](auto S) {
        constexpr int s = decltype(S)::value;
        ring(S, wl, w0);
        if constexpr (s == 18) poll(fpoll, c.fo, ws, 0xfull);
        if constexpr (s == 23) { vm_wait<9>(); post_flag(pend, tag, ws); }
        if constexpr (s == 24) { if (heavy) vm_wait<2>(); lds_arrive_nowait(L + SY); }
      });
      lds_arrive_nowait(L + SY + 12);
      __builtin_amdgcn_s_setprio(YPRIO);
      STAMP(17);
    }
    if (heavy) {
      store_gelu(acc, c.gc1, lo16, ws, (u32)(WS_HH + tb1 * HH_TB + (23 + 2 * cg) * 2048));
      pend = fl((u32)(WS_F + tb1 * 1024 + 384 + (38 + cg) * 8));
      const u32 t = c.trl ^ (u32)((c.xcd & 1) * 64);
      f32x16 acc3 = bias_init(c.biasy);
      lds_wait(L + SY, 8);
      STAMP(20);
      acc3 = gemm1_block<false, false, 4, 20>(xa, L + t + 5 * 8192, L + t, L + t + 10 * 8192, acc3, [&](auto S) {
        if constexpr (decltype(S)::value == 16) { vm_wait<0>(); pin(fpoll); post_flag(pend, tag, ws); } });
      __builtin_amdgcn_s_setprio(BPRIO);
      STAMP(21);
      store_gelu(acc3, c.gc1, lo16, ws, (u32)(WS_HH + tb1 * HH_TB + (54 + (c.xcd & 1)) * 2048));
      vm_wait<0>();
      post_flag(fl((u32)(WS_F + tb1 * 1024 + 384 + (54 + (c.xcd & 1)) * 8)), tag, ws);
      STAMP(22);
      if (c.b == 255 && kq == 0) {
#pragma unroll 1
        for (u32 spins = 0; spins < SPIN_CAP; ++spins) {
          __builtin_amdgcn_s_sleep(32);
          v2u f[16];
#pragma unroll
          for (int i = 0; i < 16; ++i) f[i] = v2u{0u, 0u};
#pragma unroll
          for (int i = 0; i < 16; ++i) poll(f[i], fl((u32)(WS_FO + (i * 64 + lane) * 8)), ws, ~0ull);
          vm_wait<0>();
          bool ok = true;
#pragma unroll
          for (int i = 0; i < 16; ++i) { pin(f[i]); ok = ok && f[i][0] == tag[0] && f[i][1] == tag[1]; }
          if (__all(ok)) break;
        }
        post_flag((u32)WS_E, tag, ws);
      }
    }
  }
  {
    u32 rr = (u32)c.r;
    keep_s(rr);
    if (rr >= 28) return;
  }
  const __attribute__((address_space(4))) u64* ka = (const __attribute__((address_space(4))) u64*)__builtin_amdgcn_kernarg_segment_ptr();
  asm volatile("" : "+s"(ka));
  const v4i sx2 = make_srd((const void*)ka[0], S_ * D_ * 2), swo = make_srd((const void*)ka[2], D_ * D_ * 2);
  const v4i sb2 = make_srd((const void*)ka[6], D_ * 2), sout = make_srd((const void*)ka[7], S_ * D_ * 2);
  const u32 t0 = c.trl ^ (u32)(kq * 32);
  u32 ta[3], tb[3];
#pragma unroll
  for (int i = 0; i < 3; ++i) { ta[i] = cell[i] + t0; tb[i] = cell[i] + (t0 ^ 64u); }
  const u32 hb = (u32)(WS_HH + 2 * tt * HH_TB);
  v4u h0[9][4], h1[9][4], wa[4], wb[4], wc[3], wd[3];
  const int k2 = kq < 3 ? 32 * (16 + 2 * kq) : 32 * 22, d2 = kq < 3 ? 32 : 64;
  const int k4 = kq < 3 ? 32 * (42 + 4 * kq) : 32 * 23;
  const int k6 = kq < 3 ? 32 * (43 + 4 * kq) : 32 * 54, d6 = kq < 3 ? 64 : 32;
  auto krun = [&](int j, int u) {
    return j == 2 ? k2 + d2 * u : (j == 4 ? k4 + 64 * u : (j == 6 ? k6 + d6 * u : j < 7 ? 32 * hblock(j, u) + 128 * kq : (j == 7 ? 1792 + 64 * kq + 32 * u : 2048 + 48 * kq + 32 * u)));
  };
#define LDH0(j, s) ld16c<((s) & 1) * 1024>(h0[j][s], lo16, ws, hb + (u32)krun(j, (s) >> 1) * 64u);
#define LDH1(j, s) ld16c<((s) & 1) * 1024>(h1[j][s], lo16, ws, hb + (u32)krun(j, (s) >> 1) * 64u + HH_TB);
#define RDW(j, s) { wa[s] = tr_frag(L + ta[(j) % 3] + (s) * 2048); wb[s] = tr_frag(L + tb[(j) % 3] + (s) * 2048); }
#define RDW8(s) { wc[s] = tr_frag(L + ta[2] + (s) * 2048); wd[s] = tr_frag(L + tb[2] + (s) * 2048); }
#define GP(J, P) { if (((P) & 3) == 0) { g.srd = (J) < 7 ? c.sw2 : swo; \
      piece_m0<0>(g, lds0 + cell[(J) % 3] + (P) * 1024, (u32)((krun(J, (P) >> 2) - ((J) < 7 ? 0 : 1792)) * 896 + ct * 128)); } \
    else piece<((P) & 3)>(g); }
#define KEEPACC asm volatile("" ::"v"(a00), "v"(a01), "v"(a10), "v"(a11));
#define W4(p, a, o) *(LAS f32x4*)(p) = f32x4{a[o], a[(o) + 1], a[(o) + 2], a[(o) + 3]};
  f32x16 a00, a01, a10, a11;
#pragma unroll
  for (int i = 0; i < 16; ++i) { a00[i] = 0.f; a01[i] = 0.f; a10[i] = 0.f; a11[i] = 0.f; }
  const u32 fo_o = fl((u32)(WS_FO + (2 * tt * 16 + r32) * 8));
  const u32 hm = kq == 0 ? 0x0607u : (kq == 1 ? 0x1c1cu : (kq == 2 ? 0x7070u : 0xc3c0u));
  const u64 omask = (u64)(hm | (hm << 16));
  v2u b2a, b2d;
  auto stage_x = [&]() {
    Stream gx;
    gx.srd = sx2; gx.voff = (u32)((lane >> 3) * 896 + (((lane & 7) ^ (lane >> 3)) << 4)); gx.d8 = 8 * 896 - 1024;
    piece_m0<0>(gx, lds0 + (u32)(19 * 8192 + kq * 2048), (u32)((tt * 64 + kq * 16) * 896 + ct * 128)); piece<1>(gx);
    asm volatile("buffer_load_dwordx2 %0, %2, %3, %4 offen\n\tbuffer_load_dwordx2 %1, %2, %3, %4 offen offset:16"
                 : "=&v"(b2a), "=&v"(b2d) : "v"((u32)(hh * 8)), "s"(sb2), "s"((u32)((ct * 64 + kq * 16) * 2)) : "memory");
  };
  f32x4 xa4[2], xd4[2];
  u32 ra[2][3];
  auto take_x = [&]() {
    auto up4 = [](v2u v) { return f32x4{u2f(v[0] << 16), u2f(v[0] & 0xffff0000u), u2f(v[1] << 16), u2f(v[1] & 0xffff0000u)}; };
    const u32 xo = (u32)(19 * 8192 + r32 * 128 + (((2 * kq) ^ (r32 & 7)) << 4) + hh * 8);
    lds_wait(L + SY + 12, 8);
    const f32x4 ba = up4(b2a), bd = up4(b2d);
#pragma unroll
    for (int u = 0; u < 2; ++u) {
      const f32x4 p = up4(*(LAS v2u*)(L + xo + u * 4096)), q = up4(*(LAS v2u*)(L + (xo ^ 16u) + u * 4096));
#pragma unroll
      for (int i = 0; i < 4; ++i) { xa4[u][i] = vadd(p[i], ba[i]); xd4[u][i] = vadd(q[i], bd[i]); }
    }
#pragma unroll
    for (int t = 1; t < 4; ++t) {
      const int k = kq ^ t;
      ra[0][t - 1] = (u32)(k * 8192 + t * 2048) + lo16;
      ra[1][t - 1] = (u32)((k == 0 ? 5 : (k == 3 ? 13 : 9 + k)) * 8192 + t * 2048) + lo16;
    }
    asm volatile("" : "+v"(xa4[0]), "+v"(xa4[1]), "+v"(xd4[0]), "+v"(xd4[1]), "+v"(ra[0][0]), "+v"(ra[0][1]), "+v"(ra[0][2]), "+v"(ra[1][0]), "+v"(ra[1][1]), "+v"(ra[1][2]));
  };
  const u32 oo = (u32)((tt * 64 + r32) * 896 + (ct * 64 + kq * 16 + hh * 8) * 2);
  LAS char* r0 = L + cell[0] + lo16;
  LAS char* r1 = L + cell[1] + lo16;
  {
    u32 p[8];
    mfma_settle(acc);
    gelu4(p[0], p[1], acc[0], acc[1], acc[2], acc[3], c.gc1);
  vm_wait<1>();
    check(fpoll, c.fo, ws, tag, 0xfull);
    STAMP(24);
    LDH0(0, 0) LDH1(0, 0) LDH0(0, 1) LDH1(0, 1)
    gelu4(p[2], p[3], acc[4], acc[5], acc[6], acc[7], c.gc1);
    LDH0(0, 2) LDH1(0, 2) LDH0(0, 3) LDH1(0, 3)
    gelu4(p[4], p[5], acc[8], acc[9], acc[10], acc[11], c.gc1);
    gelu4<true>(p[6], p[7], acc[12], acc[13], acc[14], acc[15], c.gc1);
    const u32 so = (u32)(WS_HH + tb1 * HH_TB + (23 + 2 * cg) * 2048);
    st16c<0>(swap_half(p[0], p[1], p[2], p[3]), lo16, ws, so);
    st16c<1024>(swap_half(p[4], p[5], p[6], p[7]), lo16, ws, so);
  }
  __builtin_amdgcn_s_setprio(BPRIO);
  STAMP(32);
  CSTAMP(61);
  lds_wait(L + SY, 8);
  lds_wait(L + SY + 12, 4);
  RDW(0, 0) RDW(0, 1) RDW(0, 2) RDW(0, 3)
  GP(2, 0) GP(2, 1) GP(2, 2) GP(2, 3) GP(2, 4) GP(2, 5) GP(2, 6) GP(2, 7)
  STAMP(41);
  poll(fpoll, c.fo, ws, 0xf0ull); asm volatile("s_waitcnt lgkmcnt(0)" ::: "memory"); GP(3, 0) GP(3, 1) GP(3, 2) GP(3, 3)
  vm_wait<13>();
  post_flag(fl((u32)(WS_F + tb1 * 1024 + 384 + (38 + cg) * 8)), tag, ws);
  vm_wait<22>();
  pin(h0[0][0]); pin(h1[0][0]); SB
  a00 = mfma(wa[0], h0[0][0], a00); SB asm volatile("s_waitcnt lgkmcnt(4)" ::: "memory"); GP(3, 4) SB
  a10 = mfma(wb[0], h0[0][0], a10); SB GP(3, 5) SB
  a01 = mfma(wa[0], h1[0][0], a01); SB asm volatile("s_waitcnt lgkmcnt(0)" ::: "memory"); GP(3, 6) SB
  a11 = mfma(wb[0], h1[0][0], a11); SB GP(3, 7) RDW(1, 0) SB
  KEEPACC
  vm_wait<9>();
  check(fpoll, c.fo, ws, tag, 0xf0ull);
  vm_wait<24>();
  pin(h0[0][1]); pin(h1[0][1]); SB
  a00 = mfma(wa[1], h0[0][1], a00); SB LDH0(1, 0) SB
  a10 = mfma(wb[1], h0[0][1], a10); SB LDH1(1, 0) SB
  a01 = mfma(wa[1], h1[0][1], a01); SB LDH0(1, 1) SB
  a11 = mfma(wb[1], h1[0][1], a11); SB LDH1(1, 1) RDW(1, 1) SB
  vm_wait<26>();
  pin(h0[0][2]); pin(h1[0][2]); SB
  a00 = mfma(wa[2], h0[0][2], a00); SB LDH0(1, 2) SB
  a10 = mfma(wb[2], h0[0][2], a10); SB LDH1(1, 2) poll(fpoll, c.fo, ws, 0xff00ull); SB
  a01 = mfma(wa[2], h1[0][2], a01); SB LDH0(1, 3) SB
  a11 = mfma(wb[2], h1[0][2], a11); SB LDH1(1, 3) RDW(1, 2) SB
  vm_wait<29>();
  pin(h0[0][3]); pin(h1[0][3]); SB
  a00 = mfma(wa[3], h0[0][3], a00); SB asm volatile("s_waitcnt lgkmcnt(4)" ::: "memory"); GP(4, 0) SB
  a10 = mfma(wb[3], h0[0][3], a10); SB GP(4, 1) SB
  a01 = mfma(wa[3], h1[0][3], a01); SB GP(4, 2) SB
  a11 = mfma(wb[3], h1[0][3], a11); SB GP(4, 3) RDW(1, 3) SB
  STAMP(42);
  CSTAMP(53);
  vm_wait<11>();
  pin(h0[1][0]); pin(h1[1][0]); SB
  a00 = mfma(wa[0], h0[1][0], a00); SB asm volatile("s_waitcnt lgkmcnt(4)" ::: "memory"); GP(4, 4) SB
  a10 = mfma(wb[0], h0[1][0], a10); SB GP(4, 5) SB
  a01 = mfma(wa[0], h1[1][0], a01); SB asm volatile("s_waitcnt lgkmcnt(0)" ::: "memory"); GP(4, 6) SB
  a11 = mfma(wb[0], h1[1][0], a11); SB GP(4, 7) RDW(2, 0) SB
  KEEPACC
  vm_wait<10>();
  check(fpoll, c.fo, ws, tag, 0xff00ull);
  vm_wait<13>();
  pin(h0[1][1]); pin(h1[1][1]); SB
  a00 = mfma(wa[1], h0[1][1], a00); SB LDH0(2, 0) SB
  a10 = mfma(wb[1], h0[1][1], a10); SB LDH1(2, 0) SB
  a01 = mfma(wa[1], h1[1][1], a01); SB LDH0(2, 1) SB
  a11 = mfma(wb[1], h1[1][1], a11); SB LDH1(2, 1) RDW(2, 1) SB
  vm_wait<15>();
  pin(h0[1][2]); pin(h1[1][2]); SB
  a00 = mfma(wa[2], h0[1][2], a00); SB LDH0(2, 2) SB
  a10 = mfma(wb[2], h0[1][2], a10); SB LDH1(2, 2) SB
  a01 = mfma(wa[2], h1[1][2], a01); SB LDH0(2, 3) SB
  a11 = mfma(wb[2], h1[1][2], a11); SB LDH1(2, 3) RDW(2, 2) SB
  vm_wait<16>();
  pin(h0[1][3]); pin(h1[1][3]); SB
  a00 = mfma(wa[3], h0[1][3], a00); SB asm volatile("s_waitcnt lgkmcnt(4)" ::: "memory"); GP(5, 0) SB
  a10 = mfma(wb[3], h0[1][3], a10); SB GP(5, 1) SB
  a01 = mfma(wa[3], h1[1][3], a01); SB GP(5, 2) SB
  a11 = mfma(wb[3], h1[1][3], a11); SB GP(5, 3) RDW(2, 3) SB
  STAMP(43);
  CSTAMP(54);
  vm_wait<10>();
  pin(h0[2][0]); pin(h1[2][0]); SB
  a00 = mfma(wa[0], h0[2][0], a00); SB asm volatile("s_waitcnt lgkmcnt(4)" ::: "memory"); GP(5, 4) SB
  a10 = mfma(wb[0], h0[2][0], a10); SB GP(5, 5) SB
  a01 = mfma(wa[0], h1[2][0], a01); SB asm volatile("s_waitcnt lgkmcnt(0)" ::: "memory"); GP(5, 6) SB
  a11 = mfma(wb[0], h1[2][0], a11); SB GP(5, 7) RDW(3, 0) SB
  vm_wait<12>();
  pin(h0[2][1]); pin(h1[2][1]); SB
  a00 = mfma(wa[1], h0[2][1], a00); SB LDH0(3, 0) SB
  a10 = mfma(wb[1], h0[2][1], a10); SB LDH1(3, 0) SB
  a01 = mfma(wa[1], h1[2][1], a01); SB LDH0(3, 1) SB
  a11 = mfma(wb[1], h1[2][1], a11); SB LDH1(3, 1) RDW(3, 1) SB
  vm_wait<14>();
  pin(h0[2][2]); pin(h1[2][2]); SB
  a00 = mfma(wa[2], h0[2][2], a00); SB LDH0(3, 2) SB
  a10 = mfma(wb[2], h0[2][2], a10); SB LDH1(3, 2) poll(fpoll, c.fo, ws, 0xfff0000ull); SB
  a01 = mfma(wa[2], h1[2][2], a01); SB LDH0(3, 3) SB
  a11 = mfma(wb[2], h1[2][2], a11); SB LDH1(3, 3) stage_x();    RDW(3, 2) SB
  vm_wait<21>();
  pin(h0[2][3]); pin(h1[2][3]); SB
  a00 = mfma(wa[3], h0[2][3], a00); SB asm volatile("s_waitcnt lgkmcnt(4)" ::: "memory"); GP(6, 0) SB
  a10 = mfma(wb[3], h0[2][3], a10); SB GP(6, 1) SB
  a01 = mfma(wa[3], h1[2][3], a01); SB GP(6, 2) SB
  a11 = mfma(wb[3], h1[2][3], a11); SB GP(6, 3) RDW(3, 3) SB
  STAMP(44);
  CSTAMP(55);
  vm_wait<15>();
  pin(h0[3][0]); pin(h1[3][0]); SB
  a00 = mfma(wa[0], h0[3][0], a00); SB asm volatile("s_waitcnt lgkmcnt(4)" ::: "memory"); GP(6, 4) SB
  a10 = mfma(wb[0], h0[3][0], a10); SB GP(6, 5) SB
  a01 = mfma(wa[0], h1[3][0], a01); SB asm volatile("s_waitcnt lgkmcnt(0)" ::: "memory"); GP(6, 6) SB
  a11 = mfma(wb[0], h1[3][0], a11); SB GP(6, 7) RDW(4, 0) SB
  KEEPACC
  vm_wait<14>();
  check(fpoll, c.fo, ws, tag, 0xfff0000ull);
  vm_wait<17>();
  pin(h0[3][1]); pin(h1[3][1]); SB
  a00 = mfma(wa[1], h0[3][1], a00); SB LDH0(4, 0) SB
  a10 = mfma(wb[1], h0[3][1], a10); SB LDH1(4, 0) SB
  a01 = mfma(wa[1], h1[3][1], a01); SB LDH0(4, 1) SB
  a11 = mfma(wb[1], h1[3][1], a11); SB LDH1(4, 1) RDW(4, 1) SB
  vm_wait<19>();
  pin(h0[3][2]); pin(h1[3][2]); SB
  a00 = mfma(wa[2], h0[3][2], a00); SB LDH0(4, 2) SB
  a10 = mfma(wb[2], h0[3][2], a10); SB LDH1(4, 2) SB
  a01 = mfma(wa[2], h1[3][2], a01); SB LDH0(4, 3) SB
  a11 = mfma(wb[2], h1[3][2], a11); SB LDH1(4, 3) RDW(4, 2) SB
  vm_wait<20>();
  pin(h0[3][3]); pin(h1[3][3]); SB
  a00 = mfma(wa[3], h0[3][3], a00); SB asm volatile("s_waitcnt lgkmcnt(4)" ::: "memory"); GP(7, 0) SB
  a10 = mfma(wb[3], h0[3][3], a10); SB GP(7, 1) SB
  a01 = mfma(wa[3], h1[3][3], a01); SB GP(7, 2) SB
  a11 = mfma(wb[3], h1[3][3], a11); SB GP(7, 3) RDW(4, 3) SB
  STAMP(45);
  CSTAMP(56);
  vm_wait<10>();
  pin(h0[4][0]); pin(h1[4][0]); SB
  a00 = mfma(wa[0], h0[4][0], a00); SB asm volatile("s_waitcnt lgkmcnt(4)" ::: "memory"); GP(7, 4) SB
  a10 = mfma(wb[0], h0[4][0], a10); SB GP(7, 5) SB
  a01 = mfma(wa[0], h1[4][0], a01); SB asm volatile("s_waitcnt lgkmcnt(0)" ::: "memory"); GP(7, 6) SB
  a11 = mfma(wb[0], h1[4][0], a11); SB GP(7, 7) RDW(5, 0) SB
  vm_wait<12>();
  pin(h0[4][1]); pin(h1[4][1]); SB
  a00 = mfma(wa[1], h0[4][1], a00); SB LDH0(5, 0) SB
  vm_wait<25>();
  pin(b2a); pin(b2d); lds_arrive_nowait(L + SY + 12);
  a10 = mfma(wb[1], h0[4][1], a10); SB LDH1(5, 0) SB
  a01 = mfma(wa[1], h1[4][1], a01); SB LDH0(5, 1) SB
  a11 = mfma(wb[1], h1[4][1], a11); SB LDH1(5, 1) RDW(5, 1) SB
  vm_wait<14>();
  pin(h0[4][2]); pin(h1[4][2]); SB
  a00 = mfma(wa[2], h0[4][2], a00); SB LDH0(5, 2) SB
  a10 = mfma(wb[2], h0[4][2], a10); SB LDH1(5, 2) SB
  a01 = mfma(wa[2], h1[4][2], a01); SB LDH0(5, 3) SB
  a11 = mfma(wb[2], h1[4][2], a11); SB LDH1(5, 3) RDW(5, 2) SB
  vm_wait<16>();
  pin(h0[4][3]); pin(h1[4][3]); SB
  a00 = mfma(wa[3], h0[4][3], a00); SB asm volatile("s_waitcnt lgkmcnt(4)" ::: "memory"); GP(8, 0) SB
  a10 = mfma(wb[3], h0[4][3], a10); SB GP(8, 1) SB
  a01 = mfma(wa[3], h1[4][3], a01); SB GP(8, 2) SB
  a11 = mfma(wb[3], h1[4][3], a11); SB GP(8, 3) RDW(5, 3) SB
  STAMP(46);
  CSTAMP(57);
  vm_wait<10>();
  pin(h0[5][0]); pin(h1[5][0]); SB
  a00 = mfma(wa[0], h0[5][0], a00); SB asm volatile("s_waitcnt lgkmcnt(4)" ::: "memory"); GP(8, 4) SB
  a10 = mfma(wb[0], h0[5][0], a10); SB GP(8, 5) SB
  a01 = mfma(wa[0], h1[5][0], a01); SB asm volatile("s_waitcnt lgkmcnt(0)" ::: "memory"); SB
  a11 = mfma(wb[0], h1[5][0], a11); SB  RDW(6, 0) SB
  vm_wait<10>();
  pin(h0[5][1]); pin(h1[5][1]); SB
  a00 = mfma(wa[1], h0[5][1], a00); SB LDH0(6, 0) SB
  a10 = mfma(wb[1], h0[5][1], a10); SB LDH1(6, 0) poll(fpoll, fo_o, ws, omask); SB
  a01 = mfma(wa[1], h1[5][1], a01); SB LDH0(6, 1) SB
  a11 = mfma(wb[1], h1[5][1], a11); SB LDH1(6, 1) RDW(6, 1) SB
  vm_wait<13>();
  pin(h0[5][2]); pin(h1[5][2]); SB
  a00 = mfma(wa[2], h0[5][2], a00); SB LDH0(6, 2) SB
  a10 = mfma(wb[2], h0[5][2], a10); SB LDH1(6, 2) SB
  a01 = mfma(wa[2], h1[5][2], a01); SB LDH0(6, 3) SB
  a11 = mfma(wb[2], h1[5][2], a11); SB LDH1(6, 3) RDW(6, 2) SB
  vm_wait<15>();
  pin(h0[5][3]); pin(h1[5][3]); SB
  a00 = mfma(wa[3], h0[5][3], a00); SB  SB
  KEEPACC take_x();
  a10 = mfma(wb[3], h0[5][3], a10); SB  SB
  a01 = mfma(wa[3], h1[5][3], a01); SB  SB
  a11 = mfma(wb[3], h1[5][3], a11); SB  RDW(6, 3) SB
  STAMP(47);
  KEEPACC
  vm_wait<6>();
  check(fpoll, fo_o, ws, tag, omask);
  LDH0(7, 0) LDH0(7, 1) LDH0(7, 2) LDH0(7, 3)
  vm_wait<11>();
  pin(h0[6][0]); pin(h1[6][0]); SB
  a00 = mfma(wa[0], h0[6][0], a00); SB LDH0(8, 0) SB
  a10 = mfma(wb[0], h0[6][0], a10); SB RDW8(0) SB
  a01 = mfma(wa[0], h1[6][0], a01); SB LDH0(8, 1) SB
  a11 = mfma(wb[0], h1[6][0], a11); SB  RDW(7, 0) SB
  vm_wait<10>();
  pin(h0[6][1]); pin(h1[6][1]); SB
  a00 = mfma(wa[1], h0[6][1], a00); SB LDH0(8, 2) SB
  a10 = mfma(wb[1], h0[6][1], a10); SB RDW8(1) SB
  a01 = mfma(wa[1], h1[6][1], a01); SB LDH1(7, 0) SB
  a11 = mfma(wb[1], h1[6][1], a11); SB  RDW(7, 1) SB
  vm_wait<10>();
  pin(h0[6][2]); pin(h1[6][2]); SB
  a00 = mfma(wa[2], h0[6][2], a00); SB LDH1(7, 1) SB
  a10 = mfma(wb[2], h0[6][2], a10); SB RDW8(2) SB
  a01 = mfma(wa[2], h1[6][2], a01); SB LDH1(7, 2) SB
  a11 = mfma(wb[2], h1[6][2], a11); SB  RDW(7, 2) SB
  vm_wait<10>();
  pin(h0[6][3]); pin(h1[6][3]); SB
  a00 = mfma(wa[3], h0[6][3], a00); SB LDH1(7, 3) SB
  a10 = mfma(wb[3], h0[6][3], a10); SB  SB
  a01 = mfma(wa[3], h1[6][3], a01); SB LDH1(8, 0) SB
  a11 = mfma(wb[3], h1[6][3], a11); SB  RDW(7, 3) SB
  STAMP(48);
  vm_wait<11>();
  pin(h0[7][0]); SB
  a10 = mfma(wb[0], h0[7][0], a10); SB a00 = mfma(wa[0], h0[7][0], a00); SB
  LDH1(8, 1) SB
  vm_wait<11>();
  pin(h0[7][1]); SB
  a10 = mfma(wb[1], h0[7][1], a10); SB a00 = mfma(wa[1], h0[7][1], a00); SB
  LDH1(8, 2) SB
  vm_wait<11>();
  pin(h0[7][2]); SB
  a10 = mfma(wb[2], h0[7][2], a10); SB a00 = mfma(wa[2], h0[7][2], a00); SB
  vm_wait<10>();
  pin(h0[7][3]); SB
  a10 = mfma(wb[3], h0[7][3], a10); SB a00 = mfma(wa[3], h0[7][3], a00); SB
  vm_wait<9>();
  pin(h0[8][0]); SB
  a10 = mfma(wd[0], h0[8][0], a10); SB a00 = mfma(wc[0], h0[8][0], a00); SB
  vm_wait<8>();
  pin(h0[8][1]); SB
  a10 = mfma(wd[1], h0[8][1], a10); SB a00 = mfma(wc[1], h0[8][1], a00); SB
  vm_wait<7>();
  pin(h0[8][2]); SB
  a10 = mfma(wd[2], h0[8][2], a10); SB a00 = mfma(wc[2], h0[8][2], a00); SB
  vm_wait<6>();
  pin(h1[7][0]); SB
  a11 = mfma(wb[0], h1[7][0], a11); SB  SB
  vm_wait<5>();
  pin(h1[7][1]); SB
  a11 = mfma(wb[1], h1[7][1], a11); SB W4(r0 + 4096, a10, 0) SB
  vm_wait<4>();
  pin(h1[7][2]); SB
  a11 = mfma(wb[2], h1[7][2], a11); SB W4(r0 + 5120, a10, 4) SB
  vm_wait<3>();
  pin(h1[7][3]); SB
  a11 = mfma(wb[3], h1[7][3], a11); SB W4(r0 + 6144, a10, 8) SB
  vm_wait<2>();
  pin(h1[8][0]); SB
  a11 = mfma(wd[0], h1[8][0], a11); SB W4(r0 + 7168, a10, 12) SB
  vm_wait<1>();
  pin(h1[8][1]); SB
  a11 = mfma(wd[1], h1[8][1], a11); SB W4(r0 + 2048, a00, 8) SB
  vm_wait<0>();
  pin(h1[8][2]); SB
  a11 = mfma(wd[2], h1[8][2], a11); SB W4(r0 + 3072, a00, 12) SB
  a01 = mfma(wa[0], h1[7][0], a01); SB  SB
  a01 = mfma(wa[1], h1[7][1], a01); SB  SB
  a01 = mfma(wa[2], h1[7][2], a01); SB W4(r1 + 4096, a11, 0) SB
  a01 = mfma(wa[3], h1[7][3], a01); SB W4(r1 + 5120, a11, 4) SB
  a01 = mfma(wc[0], h1[8][0], a01); SB  SB
  a01 = mfma(wc[1], h1[8][1], a01); SB W4(r1 + 6144, a11, 8) SB
  a01 = mfma(wc[2], h1[8][2], a01); SB W4(r1 + 7168, a11, 12) SB
  STAMP(50);
  W4(r1 + 2048, a01, 8) W4(r1 + 3072, a01, 12)
#undef W4
#undef KEEPACC
#undef GP
#undef RDW8
#undef RDW
#undef LDH1
#undef LDH0
  f32x4 ya[2], yd[2];
  ya[0] = xa4[0] + f32x4{a00[0], a00[1], a00[2], a00[3]}; yd[0] = xd4[0] + f32x4{a00[4], a00[5], a00[6], a00[7]};
  ya[1] = xa4[1] + f32x4{a01[0], a01[1], a01[2], a01[3]}; yd[1] = xd4[1] + f32x4{a01[4], a01[5], a01[6], a01[7]};
  asm volatile("" : "+v"(ya[0]), "+v"(yd[0]), "+v"(ya[1]), "+v"(yd[1]));
  wg_barrier();
  STAMP(51);
  f32x4 pa[2][3], pd[2][3];
#pragma unroll
  for (int u = 0; u < 2; ++u)
#pragma unroll
    for (int t = 0; t < 3; ++t) { pa[u][t] = *(LAS f32x4*)(L + ra[u][t]); pd[u][t] = *(LAS f32x4*)(L + ra[u][t] + 1024); }
  SB
#pragma unroll
  for (int u = 0; u < 2; ++u) {
    const f32x4 za = (ya[u] + pa[u][0]) + (pa[u][1] + pa[u][2]), zd = (yd[u] + pd[u][0]) + (pd[u][1] + pd[u][2]);
    const auto e0 = __builtin_amdgcn_permlane32_swap(pk(za[0], za[1]), pk(zd[0], zd[1]), false, false);
    const auto e1 = __builtin_amdgcn_permlane32_swap(pk(za[2], za[3]), pk(zd[2], zd[3]), false, false);
    st16(v4u{e0[0], e1[0], e0[1], e1[1]}, oo + (u32)(u * 32 * 896), sout);
  }
  STAMP(52);
  CSTAMP(62);
}

__global__ void __launch_bounds__(512, 2)
fused_block(const u16*, const u16*, const u16*, const u16*, const u16*, const u16*, const u16*, u16*, char*) {
  extern __shared__ __attribute__((aligned(16))) char smem_[];
  LAS char* const L = (LAS char*)smem_;
  STAMP_DECL;
  STAMP(0);
  CSTAMP(60);
  typedef u64 u64x4 __attribute__((ext_vector_type(4)));
  typedef u64 u64x2 __attribute__((ext_vector_type(2)));
  u64x4 ka0;
  u64x2 ka1;
  u64 ka2;
  asm volatile("s_load_dwordx8 %0, %3, 0x0\n\ts_load_dwordx4 %1, %3, 0x20\n\ts_load_dwordx2 %2, %3, 0x40"
               : "=&s"(ka0), "=&s"(ka1), "=&s"(ka2) : "s"(__builtin_amdgcn_kernarg_segment_ptr()));
  SB
  Ctx c;
  c.b = blockIdx.x; c.xcd = c.b & 7; c.r = c.b >> 3;
  c.rt = 4 * (c.xcd >> 1) + (c.r & 3);
  u32 vt = 0;
  {
    u64 pc;
    asm volatile("s_getpc_b64 %0" : "=s"(pc));
    const u32 cl = min((u32)(c.r * 16) + ((threadIdx.x >> 5) & 14u) + (threadIdx.x & 1u), (u32)(CODE_LINES - 1)) * 128u;
    asm volatile("s_mov_b64 exec, 3\n\tglobal_load_dword %0, %1, %2\n\ts_mov_b64 exec, -1" : "+v"(vt) : "v"(cl), "s"(pc) : "memory");
  }
  const int w = __builtin_amdgcn_readfirstlane(threadIdx.x >> 6);
  c.m = w & 3;
  c.heavy = c.r >= 28;
  c.cg = (int)((((c.xcd & 1) ? 0x1fedcba9u : 0x08765432u) >> (4 * (c.r >> 2))) & 15);
  c.tb1 = 4 * c.rt + c.m;
  LnConst k;
  k.xo = (u32)((c.rt * 128 + 16 * w + ((threadIdx.x & 63) >> 3)) * 896 + (threadIdx.x & 7) * 16);
  u32 xtv = (threadIdx.x & 63u) * 128u, xts = (u32)(c.rt * 114688 + ((c.r >> 2) * 112 + w * 14) * 128);
  asm volatile("" : "+v"(k.xo), "+v"(xtv));
  keep_s(xts);
  if (PRE >= 1) ln_consts(k, w, c.rt);
  if (PRE >= 2) setup(c, w >> 2);
  asm volatile("s_waitcnt lgkmcnt(0)" : "+s"(ka0), "+s"(ka1), "+s"(ka2));
  const u16 *x = (const u16*)ka0[0], *wqkv = (const u16*)ka0[1], *wo = (const u16*)ka0[2], *w1 = (const u16*)ka0[3];
  const u16 *b1 = (const u16*)ka1[0], *w2 = (const u16*)ka1[1];
  char* wsp = (char*)ka2;
  const v4i sx = make_srd(x, S_ * D_ * 2);
  asm volatile("s_mov_b64 exec, 0x3fff\n\tbuffer_load_dword %0, %1, %2, %3 offen\n\ts_mov_b64 exec, -1" : "+v"(vt) : "v"(xtv), "s"(sx), "s"(xts) : "memory");
  const Args a{x, wqkv, wo, w1, b1, w2, wsp};
  c.sqkv = make_srd(wqkv, D_ * 3 * D_ * 2);
  layer_norm(a, c, k, L, w >> 2, sx, vt STAMP_PASS);
  c.ws = make_srd(wsp, WS_END);
  c.sw1 = make_srd(w1, D_ * F_ * 2);
  c.sw2 = make_srd(w2, F_ * D_ * 2);
  if (w < 4) group_a(a, c, L STAMP_PASS);
  u32 w2nd = (u32)w;
  keep_s(w2nd);
  if (w2nd >= 4) group_b(a, c, L STAMP_PASS);
  STAMP_OUT();
}

extern "C" void kernel_launch(void* const* d_in, const int* in_sizes, int n_in, void* d_out, int out_size, void* d_ws, size_t ws_size,
                              hipStream_t stream) {
  (void)hipFuncSetAttribute((const void*)fused_block, hipFuncAttributeMaxDynamicSharedMemorySize, LDS_BYTES);
  fused_block<<<256, 512, LDS_BYTES, stream>>>((const u16*)d_in[0], (const u16*)d_in[1], (const u16*)d_in[2], (const u16*)d_in[4],
                                               (const u16*)d_in[5], (const u16*)d_in[6], (const u16*)d_in[7], (u16*)d_out, (char*)d_ws + (WSEND ? ((ws_size - WS_END) & ~(size_t)0x1fffff) / (WSEND == 2 ? 2 : 1) & ~(size_t)0x1fffff : 0));
}
```
